```python
import torch

def _softmax(x, axis=-1):
    return torch.softmax(x, dim=axis)
def _gelu(x):
    return torch.nn.functional.gelu(x, approximate="tanh")
def _silu(x):
    return torch.nn.functional.silu(x)
def _sigmoid(x):
    return torch.sigmoid(x)
def _layernorm(x, axis=-1, eps=1e-5):
    m = x.mean(dim=axis, keepdim=True)
    v = x.var(dim=axis, keepdim=True, unbiased=False)
    return (x - m) / torch.sqrt(v + eps)
def _scatter_add(tgt, idx, src, axis=0):
    return tgt.clone().index_add_(axis, idx, src)
def _segment_sum(src, seg, n):
    out = torch.zeros((n,) + src.shape[1:], dtype=src.dtype)
    return out.index_add_(0, seg, src)
def _pad(x, pw):
    flat = tuple(v for pair in reversed(pw) for v in pair)
    return torch.nn.functional.pad(x, flat)

kernel_name = 'recurrent_scan__sliding_block_cached__dense_activation_gelu__prompt'

def setup_inputs():

    _w = (lambda a: (lambda t: t.to(torch.bfloat16) if t.is_floating_point() else t)(torch.as_tensor(a)))
    def _A_setup():
        B, Sq, D = 1, 2048, 1024
        Hq, Hkv, Sk, Dh = 32, 4, 2048, 128
        assert Hq % Hkv == 0, (Hq, Hkv)
        return {
            "x": _w((lambda *s: torch.randn(*s, dtype=torch.bfloat16) * 0.05)(B, Sq, D)),
            "k_cache": _w((lambda *s: torch.randn(*s, dtype=torch.bfloat16) * 1.6)(B, Hkv, Sk, Dh)),
            "v_cache": _w((lambda *s: torch.randn(*s, dtype=torch.bfloat16) * 0.8)(B, Hkv, Sk, Dh)),
            "wq": _w((lambda *s: torch.randn(*s, dtype=torch.bfloat16) * 1.6)(D, Hq * Dh) * (D ** -0.5)),
            "wo": _w((lambda *s: torch.randn(*s, dtype=torch.bfloat16) * 0.8)(Hq * Dh, D) * ((Hq * Dh) ** -0.5)),
        }
    _A_in = _A_setup()

    def _B_setup():
        B, S, D, F = 1, 2048, 1024, 3584
        return {
            "x": _w((lambda *s: torch.randn(*s, dtype=torch.bfloat16) * 0.05)(B, S, D)),
            "w1": _w((lambda *s: torch.randn(*s, dtype=torch.bfloat16) * 1.6)(D, F) * (D ** -0.5)),
            "b1": _w((lambda *s: torch.randn(*s, dtype=torch.bfloat16) * 1.6)(F) * 0.01),
            "w2": _w((lambda *s: torch.randn(*s, dtype=torch.bfloat16) * 0.2)(F, D) * (F ** -0.5)),
            "b2": _w((lambda *s: torch.randn(*s, dtype=torch.bfloat16) * 0.2)(D) * 0.01),
        }
    _B_in = _B_setup()

    return {
        "A_x": _A_in["x"],
        "A_k_cache": _A_in["k_cache"],
        "A_v_cache": _A_in["v_cache"],
        "A_wq": _A_in["wq"],
        "A_wo": _A_in["wo"],
        "B_x": _B_in["x"],
        "B_w1": _B_in["w1"],
        "B_b1": _B_in["b1"],
        "B_w2": _B_in["w2"],
        "B_b2": _B_in["b2"],
    }

def reference(A_x, A_k_cache, A_v_cache, A_wq, A_wo, B_x, B_w1, B_b1, B_w2, B_b2):
    def _A_ref(x, k_cache, v_cache, wq, wo):
        B, Sq, D = x.shape
        Hq, Dh = 32, 128
        Hkv, Sk = k_cache.shape[1], k_cache.shape[2]
        W = min(4096, max(256, Sk // 4))
        rep = Hq // Hkv
        q = (lambda x, *d: x.permute(*d))((x @ wq).reshape(B, Sq, Hq, Dh), 0, 2, 1, 3)
        k = (lambda x, n, axis: torch.repeat_interleave(x, n, dim=axis))(k_cache[:, :, Sk - W:, :], rep, axis=1)
        v = (lambda x, n, axis: torch.repeat_interleave(x, n, dim=axis))(v_cache[:, :, Sk - W:, :], rep, axis=1)
        scores = torch.einsum("bhqd,bhkd->bhqk", q, k) * (Dh ** -0.5)
        p = _softmax(scores, axis=-1)
        o = torch.einsum("bhqk,bhkd->bhqd", p, v)
        return (lambda x, *d: x.permute(*d))(o, 0, 2, 1, 3).reshape(B, Sq, Hq * Dh) @ wo

    def _B_ref(x, w1, b1, w2, b2):
        h = torch.einsum("bsd,df->bsf", x, w1) + b1
        h = _gelu(h)
        return torch.einsum("bsf,fd->bsd", h, w2) + b2

    x = A_x
    xn = _layernorm(x, axis=-1)
    s = _A_ref(xn, A_k_cache, A_v_cache, A_wq, A_wo)
    g = _silu(_B_ref(xn, B_w1, B_b1, B_w2, B_b2))
    return x + s * g
```

```cpp
#include <hip/hip_runtime.h>
#include <hip/hip_bf16.h>

typedef __attribute__((ext_vector_type(4))) unsigned u32x4;

__device__ __forceinline__ float bf_lo(unsigned u) { return __uint_as_float(u << 16); }
__device__ __forceinline__ float bf_hi(unsigned u) { return __uint_as_float(u & 0xffff0000u); }
__device__ __forceinline__ unsigned pk_bf16(float a, float b) {
  typedef __attribute__((ext_vector_type(2))) float f2; typedef __attribute__((ext_vector_type(2))) __bf16 b2;
  f2 v = {a, b}; b2 r = __builtin_convertvector(v, b2); return __builtin_bit_cast(unsigned, r);
}

__global__ __launch_bounds__(256) void k_ln(const u32x4* __restrict__ x, u32x4* __restrict__ xn, unsigned* __restrict__ flags) {
  const int lane = threadIdx.x & 63, row = blockIdx.x * 4 + (threadIdx.x >> 6);
  const u32x4 a = x[row * 128 + lane], b = x[row * 128 + 64 + lane];
  if (blockIdx.x < 132) flags[blockIdx.x * 256 + threadIdx.x] = 0u;
  float v[16];
  for (int i = 0; i < 4; ++i) { v[2*i] = bf_lo(a[i]); v[2*i+1] = bf_hi(a[i]); v[8+2*i] = bf_lo(b[i]); v[8+2*i+1] = bf_hi(b[i]); }
  float s = 0.f;
  for (int i = 0; i < 16; ++i) s += v[i];
  for (int o = 32; o > 0; o >>= 1) s += __shfl_xor(s, o);
  const float m = s * (1.f / 1024.f);
  float q = 0.f;
  for (int i = 0; i < 16; ++i) { v[i] -= m; q += v[i] * v[i]; }
  for (int o = 32; o > 0; o >>= 1) q += __shfl_xor(q, o);
  const float r = 1.f / sqrtf(q * (1.f / 1024.f) + 1e-5f);
  u32x4 oa, ob;
  for (int i = 0; i < 4; ++i) { oa[i] = pk_bf16(v[2*i] * r, v[2*i+1] * r); ob[i] = pk_bf16(v[8+2*i] * r, v[8+2*i+1] * r); }
  xn[row * 128 + lane] = oa; xn[row * 128 + 64 + lane] = ob;
}

__global__ __launch_bounds__(512, 2) void k_main(const void* xn, const void* wq, const void* w1, const void* b1, const void* kc, const void* vc, void* hbuf, void* obuf,
    const void* wo, const void* w2, const void* b2, const void* x, void* out, void* flags, void* part) {
  __shared__ __attribute__((aligned(16))) char smem[163840];
  unsigned tid = threadIdx.x; unsigned bid = blockIdx.x;
  auto ka = __builtin_amdgcn_kernarg_segment_ptr();
  asm volatile(
"s_load_dwordx8 s[8:15], s[4:5], 0\n"
"s_load_dwordx4 s[16:19], s[4:5], 32\n"
"s_load_dwordx2 s[6:7], s[4:5], 104\n"
"s_load_dwordx4 s[92:95], s[4:5], 64\n"
"s_load_dwordx2 s[96:97], s[4:5], 112\n"
"v_and_b32 v1, 63, v0\n"
"v_lshrrev_b32 v2, 6, v0\n"
"s_mov_b32 s61, 1\n"
"v_readfirstlane_b32 s21, v2\n"
"s_and_b32 s50, s20, 7\n"
"s_lshr_b32 s51, s20, 3\n"
"s_and_b32 s52, s51, 3\n"
"s_lshl_b32 s23, s50, 2\n"
"s_add_u32 s23, s23, s52\n"
"s_lshr_b32 s22, s51, 2\n"
"s_and_b32 s34, s21, 3\n"
"s_lshr_b32 s35, s21, 2\n"
"v_lshrrev_b32 v2, 3, v1\n"
"v_lshrrev_b32 v3, 4, v1\n"
"v_and_b32 v4, 7, v1\n"
"v_xor_b32 v5, v4, v3\n"
"s_lshl_b32 s50, s21, 5\n"
"v_add_u32 v6, s50, v2\n"
"v_lshlrev_b32 v6, 11, v6\n"
"v_xor_b32 v7, 0, v5\n"
"v_lshlrev_b32 v7, 4, v7\n"
"v_add_u32 v7, v7, v6\n"
"v_add_u32 v16, 0, v7\n"
"v_xor_b32 v7, 4, v5\n"
"v_lshlrev_b32 v7, 4, v7\n"
"v_add_u32 v7, v7, v6\n"
"v_add_u32 v17, 16384, v7\n"
"v_xor_b32 v7, 0, v5\n"
"v_lshlrev_b32 v7, 4, v7\n"
"v_add_u32 v7, v7, v6\n"
"v_add_u32 v18, 32768, v7\n"
"v_xor_b32 v7, 4, v5\n"
"v_lshlrev_b32 v7, 4, v7\n"
"v_add_u32 v7, v7, v6\n"
"v_add_u32 v19, 49152, v7\n"
"s_cmp_eq_u32 s35, 0\n"
"s_mov_b32 s50, 256\n"
"s_mov_b32 s99, 8192\n"
"s_cselect_b32 s50, s50, 224\n"
"s_cselect_b32 s99, s99, 7168\n"
"s_mul_i32 s52, s50, s23\n"
"s_lshl_b32 s98, s99, 6\n"
"s_and_b32 s50, s21, 1\n"
"s_lshl_b32 s50, s50, 5\n"
"v_add_u32 v6, s50, v2\n"
"s_lshr_b32 s51, s21, 1\n"
"s_and_b32 s51, s51, 1\n"
"s_lshl_b32 s51, s51, 7\n"
"v_and_b32 v5, 1, v3\n"
"v_lshlrev_b32 v5, 1, v5\n"
"v_xor_b32 v5, v5, v4\n"
"v_add_u32 v7, 0, v6\n"
"v_mul_lo_u32 v7, v7, s99\n"
"v_xor_b32 v20, 0, v5\n"
"v_lshlrev_b32 v20, 4, v20\n"
"v_add_u32 v20, v20, v7\n"
"v_add_u32 v20, s51, v20\n"
"v_add_u32 v7, 8, v6\n"
"v_mul_lo_u32 v7, v7, s99\n"
"v_xor_b32 v21, 4, v5\n"
"v_lshlrev_b32 v21, 4, v21\n"
"v_add_u32 v21, v21, v7\n"
"v_add_u32 v21, s51, v21\n"
"v_add_u32 v7, 16, v6\n"
"v_mul_lo_u32 v7, v7, s99\n"
"v_xor_b32 v22, 0, v5\n"
"v_lshlrev_b32 v22, 4, v22\n"
"v_add_u32 v22, v22, v7\n"
"v_add_u32 v22, s51, v22\n"
"v_add_u32 v7, 24, v6\n"
"v_mul_lo_u32 v7, v7, s99\n"
"v_xor_b32 v23, 4, v5\n"
"v_lshlrev_b32 v23, 4, v23\n"
"v_add_u32 v23, v23, v7\n"
"v_add_u32 v23, s51, v23\n"
"s_waitcnt lgkmcnt(0)\n"
"s_lshl_b32 s50, s22, 19\n"
"s_add_u32 s24, s8, s50\n"
"s_addc_u32 s25, s9, 0\n"
"s_and_b32 s25, s25, 0xffff\n"
"s_mov_b32 s26, 524288\n"
"s_mov_b32 s27, 0x20000\n"
"s_cmp_eq_u32 s35, 0\n"
"s_cselect_b32 s28, s10, s12\n"
"s_cselect_b32 s29, s11, s13\n"
"s_add_u32 s28, s28, s52\n"
"s_addc_u32 s29, s29, 0\n"
"s_and_b32 s29, s29, 0xffff\n"
"s_lshl_b32 s53, s99, 10\n"
"s_sub_u32 s30, s53, s52\n"
"s_mov_b32 s31, 0x20000\n"
"s_mov_b32 s44, 0\n"
"s_mov_b32 s45, 0\n"
"s_lshl_b32 s46, s21, 12\n"
"s_lshl_b32 s60, s21, 12\n"
"s_mov_b32 s49, s46\n"
"s_add_u32 m0, s46, 0\n"
"s_nop 0\n"
"buffer_load_dwordx4 v16, s[24:27], s44 offen lds\n"
"s_add_u32 m0, s46, 1024\n"
"s_nop 0\n"
"buffer_load_dwordx4 v17, s[24:27], s44 offen lds\n"
"s_add_u32 m0, s46, 2048\n"
"s_nop 0\n"
"buffer_load_dwordx4 v18, s[24:27], s44 offen lds\n"
"s_add_u32 m0, s46, 3072\n"
"s_nop 0\n"
"buffer_load_dwordx4 v19, s[24:27], s44 offen lds\n"
"s_add_u32 m0, s49, 32768\n"
"s_nop 0\n"
"buffer_load_dwordx4 v20, s[28:31], s45 offen lds\n"
"s_add_u32 m0, s49, 33792\n"
"s_nop 0\n"
"buffer_load_dwordx4 v21, s[28:31], s45 offen lds\n"
"s_add_u32 m0, s49, 34816\n"
"s_nop 0\n"
"buffer_load_dwordx4 v22, s[28:31], s45 offen lds\n"
"s_add_u32 m0, s49, 35840\n"
"s_nop 0\n"
"buffer_load_dwordx4 v23, s[28:31], s45 offen lds\n"
"s_xor_b32 s46, s46, 0x10000\n"
"s_add_u32 s44, s44, 128\n"
"s_add_u32 m0, s46, 0\n"
"s_nop 0\n"
"buffer_load_dwordx4 v16, s[24:27], s44 offen lds\n"
"s_add_u32 m0, s46, 1024\n"
"s_nop 0\n"
"buffer_load_dwordx4 v17, s[24:27], s44 offen lds\n"
"s_add_u32 m0, s46, 2048\n"
"s_nop 0\n"
"buffer_load_dwordx4 v18, s[24:27], s44 offen lds\n"
"s_add_u32 m0, s46, 3072\n"
"s_nop 0\n"
"buffer_load_dwordx4 v19, s[24:27], s44 offen lds\n"
"s_xor_b32 s46, s46, 0x10000\n"
"s_xor_b32 s49, s49, 0x10000\n"
"v_and_b32 v2, 15, v1\n"
"v_lshrrev_b32 v3, 4, v1\n"
"v_lshrrev_b32 v4, 1, v1\n"
"v_and_b32 v4, 7, v4\n"
"v_xor_b32 v4, v4, v3\n"
"s_lshl_b32 s50, s34, 6\n"
"v_add_u32 v5, s50, v2\n"
"v_lshlrev_b32 v5, 7, v5\n"
"v_lshlrev_b32 v4, 4, v4\n"
"v_add_u32 v24, v5, v4\n"
"v_xor_b32 v25, 64, v24\n"
"v_lshrrev_b32 v4, 2, v2\n"
"v_and_b32 v5, 3, v1\n"
"v_lshlrev_b32 v6, 3, v3\n"
"v_add_u32 v6, v6, v4\n"
"v_lshlrev_b32 v6, 7, v6\n"
"v_lshrrev_b32 v7, 1, v4\n"
"v_lshlrev_b32 v7, 1, v7\n"
"v_and_b32 v3, 1, v3\n"
"v_lshlrev_b32 v3, 2, v3\n"
"v_or_b32 v7, v7, v3\n"
"v_lshrrev_b32 v3, 1, v5\n"
"v_xor_b32 v7, v7, v3\n"
"v_lshlrev_b32 v7, 4, v7\n"
"v_and_b32 v5, 1, v5\n"
"v_lshlrev_b32 v5, 3, v5\n"
"v_add_u32 v6, v6, v5\n"
"v_add_u32 v6, v6, v7\n"
"s_lshl_b32 s50, s35, 13\n"
"s_add_u32 s50, s50, 32768\n"
"v_add_u32 v26, s50, v6\n"
"v_xor_b32 v27, 32, v26\n"
"v_xor_b32 v28, 64, v26\n"
"v_xor_b32 v29, 96, v26\n"
"v_lshrrev_b32 v3, 4, v1\n"
"v_lshlrev_b32 v3, 3, v3\n"
"s_mul_i32 s50, s23, 224\n"
"s_lshl_b32 s51, s35, 7\n"
"s_add_u32 s50, s50, s51\n"
"v_add_u32 v3, s50, v3\n"
"v_add_u32 v4, 0, v3\n"
"v_min_u32 v4, 7160, v4\n"
"global_load_dwordx2 v[8:9], v4, s[14:15]\n"
"v_add_u32 v4, 32, v3\n"
"v_min_u32 v4, 7160, v4\n"
"global_load_dwordx2 v[10:11], v4, s[14:15]\n"
"v_add_u32 v4, 64, v3\n"
"v_min_u32 v4, 7160, v4\n"
"global_load_dwordx2 v[12:13], v4, s[14:15]\n"
"v_add_u32 v4, 96, v3\n"
"v_min_u32 v4, 7160, v4\n"
"global_load_dwordx2 v[14:15], v4, s[14:15]\n"
"s_cmp_eq_u32 s35, 0\n"
"s_cselect_b32 s36, s16, s18\n"
"s_cselect_b32 s37, s17, s19\n"
"s_lshr_b32 s50, s23, 3\n"
"s_mul_i32 s50, s50, 524288\n"
"s_add_u32 s50, s50, 393216\n"
"s_add_u32 s36, s36, s50\n"
"s_addc_u32 s37, s37, 0\n"
"s_and_b32 s37, s37, 0xffff\n"
"s_mov_b32 s38, 131072\n"
"s_mov_b32 s39, 0x20000\n"
"v_lshrrev_b32 v2, 4, v1\n"
"v_and_b32 v3, 15, v1\n"
"v_lshrrev_b32 v4, 3, v1\n"
"v_and_b32 v5, 7, v1\n"
"s_cmp_eq_u32 s35, 0\n"
"s_cbranch_scc0 P1_kvs_v\n"
"s_lshl_b32 s50, s21, 4\n"
"v_add_u32 v6, 0, v2\n"
"v_xor_b32 v7, v6, v3\n"
"v_lshlrev_b32 v7, 4, v7\n"
"v_add_u32 v6, s50, v6\n"
"v_lshlrev_b32 v6, 8, v6\n"
"v_add_u32 v124, v6, v7\n"
"v_add_u32 v6, 4, v2\n"
"v_xor_b32 v7, v6, v3\n"
"v_lshlrev_b32 v7, 4, v7\n"
"v_add_u32 v6, s50, v6\n"
"v_lshlrev_b32 v6, 8, v6\n"
"v_add_u32 v125, v6, v7\n"
"v_add_u32 v6, 8, v2\n"
"v_xor_b32 v7, v6, v3\n"
"v_lshlrev_b32 v7, 4, v7\n"
"v_add_u32 v6, s50, v6\n"
"v_lshlrev_b32 v6, 8, v6\n"
"v_add_u32 v126, v6, v7\n"
"v_add_u32 v6, 12, v2\n"
"v_xor_b32 v7, v6, v3\n"
"v_lshlrev_b32 v7, 4, v7\n"
"v_add_u32 v6, s50, v6\n"
"v_lshlrev_b32 v6, 8, v6\n"
"v_add_u32 v127, v6, v7\n"
"s_branch P1_kvs_done\n"
"P1_kvs_v:\n"
"s_and_b32 s50, s21, 1\n"
"s_lshl_b32 s50, s50, 5\n"
"s_lshr_b32 s51, s21, 1\n"
"s_and_b32 s51, s51, 1\n"
"s_lshl_b32 s51, s51, 7\n"
"v_lshlrev_b32 v2, 1, v2\n"
"v_xor_b32 v5, v5, v2\n"
"v_lshlrev_b32 v5, 4, v5\n"
"v_add_u32 v5, s51, v5\n"
"v_add_u32 v6, 0, v4\n"
"v_add_u32 v6, s50, v6\n"
"v_lshlrev_b32 v6, 8, v6\n"
"v_add_u32 v124, v6, v5\n"
"v_add_u32 v6, 8, v4\n"
"v_add_u32 v6, s50, v6\n"
"v_lshlrev_b32 v6, 8, v6\n"
"v_add_u32 v125, v6, v5\n"
"v_add_u32 v6, 16, v4\n"
"v_add_u32 v6, s50, v6\n"
"v_lshlrev_b32 v6, 8, v6\n"
"v_add_u32 v126, v6, v5\n"
"v_add_u32 v6, 24, v4\n"
"v_add_u32 v6, s50, v6\n"
"v_lshlrev_b32 v6, 8, v6\n"
"v_add_u32 v127, v6, v5\n"
"P1_kvs_done:\n"
"s_waitcnt vmcnt(8)\n"
"FZ_c0:\n"
"s_barrier\n"
"s_cmp_eq_u32 s35, 0\n"
"s_cbranch_scc0 P1_grpB\n"
"s_add_u32 s45, s45, s98\n"
"s_add_u32 m0, s49, 32768\n"
"ds_read_b64_tr_b16 v[64:65], v26 offset:0\n"
"buffer_load_dwordx4 v20, s[28:31], s45 offen lds\n"
"ds_read_b64_tr_b16 v[66:67], v26 offset:512\n"
"ds_read_b64_tr_b16 v[68:69], v27 offset:0\n"
"ds_read_b64_tr_b16 v[70:71], v27 offset:512\n"
"ds_read_b64_tr_b16 v[72:73], v28 offset:0\n"
"ds_read_b64_tr_b16 v[74:75], v28 offset:512\n"
"s_add_u32 m0, s49, 33792\n"
"ds_read_b64_tr_b16 v[76:77], v29 offset:0\n"
"buffer_load_dwordx4 v21, s[28:31], s45 offen lds\n"
"ds_read_b64_tr_b16 v[78:79], v29 offset:512\n"
"ds_read_b64_tr_b16 v[80:81], v26 offset:4096\n"
"ds_read_b64_tr_b16 v[82:83], v26 offset:4608\n"
"ds_read_b64_tr_b16 v[84:85], v27 offset:4096\n"
"ds_read_b64_tr_b16 v[86:87], v27 offset:4608\n"
"s_add_u32 m0, s49, 34816\n"
"ds_read_b64_tr_b16 v[88:89], v28 offset:4096\n"
"buffer_load_dwordx4 v22, s[28:31], s45 offen lds\n"
"ds_read_b64_tr_b16 v[90:91], v28 offset:4608\n"
"ds_read_b64_tr_b16 v[92:93], v29 offset:4096\n"
"ds_read_b64_tr_b16 v[94:95], v29 offset:4608\n"
"ds_read_b128 v[32:35], v24 offset:0\n"
"ds_read_b128 v[36:39], v24 offset:2048\n"
"s_add_u32 m0, s49, 35840\n"
"ds_read_b128 v[40:43], v24 offset:4096\n"
"buffer_load_dwordx4 v23, s[28:31], s45 offen lds\n"
"ds_read_b128 v[44:47], v24 offset:6144\n"
"ds_read_b128 v[48:51], v25 offset:0\n"
"ds_read_b128 v[52:55], v25 offset:2048\n"
"ds_read_b128 v[56:59], v25 offset:4096\n"
"ds_read_b128 v[60:63], v25 offset:6144\n"
"s_waitcnt lgkmcnt(0)\n"
"s_barrier\n"
"v_mfma_f32_16x16x32_bf16 v[128:131], v[64:67], v[32:35], 0\n"
"v_mfma_f32_16x16x32_bf16 v[128:131], v[80:83], v[48:51], v[128:131]\n"
"v_mfma_f32_16x16x32_bf16 v[132:135], v[80:83], v[52:55], 0\n"
"v_mfma_f32_16x16x32_bf16 v[132:135], v[64:67], v[36:39], v[132:135]\n"
"v_mfma_f32_16x16x32_bf16 v[136:139], v[64:67], v[40:43], 0\n"
"v_mfma_f32_16x16x32_bf16 v[136:139], v[80:83], v[56:59], v[136:139]\n"
"v_mfma_f32_16x16x32_bf16 v[140:143], v[80:83], v[60:63], 0\n"
"v_mfma_f32_16x16x32_bf16 v[140:143], v[64:67], v[44:47], v[140:143]\n"
"v_mfma_f32_16x16x32_bf16 v[156:159], v[68:71], v[44:47], 0\n"
"v_mfma_f32_16x16x32_bf16 v[156:159], v[84:87], v[60:63], v[156:159]\n"
"v_mfma_f32_16x16x32_bf16 v[152:155], v[84:87], v[56:59], 0\n"
"v_mfma_f32_16x16x32_bf16 v[152:155], v[68:71], v[40:43], v[152:155]\n"
"v_mfma_f32_16x16x32_bf16 v[148:151], v[68:71], v[36:39], 0\n"
"v_mfma_f32_16x16x32_bf16 v[148:151], v[84:87], v[52:55], v[148:151]\n"
"v_mfma_f32_16x16x32_bf16 v[144:147], v[84:87], v[48:51], 0\n"
"v_mfma_f32_16x16x32_bf16 v[144:147], v[68:71], v[32:35], v[144:147]\n"
"v_mfma_f32_16x16x32_bf16 v[160:163], v[72:75], v[32:35], 0\n"
"v_mfma_f32_16x16x32_bf16 v[160:163], v[88:91], v[48:51], v[160:163]\n"
"v_mfma_f32_16x16x32_bf16 v[164:167], v[88:91], v[52:55], 0\n"
"v_mfma_f32_16x16x32_bf16 v[164:167], v[72:75], v[36:39], v[164:167]\n"
"v_mfma_f32_16x16x32_bf16 v[168:171], v[72:75], v[40:43], 0\n"
"v_mfma_f32_16x16x32_bf16 v[168:171], v[88:91], v[56:59], v[168:171]\n"
"v_mfma_f32_16x16x32_bf16 v[172:175], v[88:91], v[60:63], 0\n"
"v_mfma_f32_16x16x32_bf16 v[172:175], v[72:75], v[44:47], v[172:175]\n"
"v_mfma_f32_16x16x32_bf16 v[188:191], v[76:79], v[44:47], 0\n"
"v_mfma_f32_16x16x32_bf16 v[188:191], v[92:95], v[60:63], v[188:191]\n"
"v_mfma_f32_16x16x32_bf16 v[184:187], v[92:95], v[56:59], 0\n"
"v_mfma_f32_16x16x32_bf16 v[184:187], v[76:79], v[40:43], v[184:187]\n"
"v_mfma_f32_16x16x32_bf16 v[180:183], v[76:79], v[36:39], 0\n"
"v_mfma_f32_16x16x32_bf16 v[180:183], v[92:95], v[52:55], v[180:183]\n"
"v_mfma_f32_16x16x32_bf16 v[176:179], v[92:95], v[48:51], 0\n"
"v_mfma_f32_16x16x32_bf16 v[176:179], v[76:79], v[32:35], v[176:179]\n"
"s_barrier\n"
"s_add_u32 s44, s44, 128\n"
"s_add_u32 m0, s46, 0\n"
"ds_read_b64_tr_b16 v[64:65], v26 offset:16384\n"
"buffer_load_dwordx4 v16, s[24:27], s44 offen lds\n"
"ds_read_b64_tr_b16 v[66:67], v26 offset:16896\n"
"ds_read_b64_tr_b16 v[68:69], v27 offset:16384\n"
"ds_read_b64_tr_b16 v[70:71], v27 offset:16896\n"
"s_add_u32 m0, s46, 1024\n"
"ds_read_b64_tr_b16 v[72:73], v28 offset:16384\n"
"buffer_load_dwordx4 v17, s[24:27], s44 offen lds\n"
"ds_read_b64_tr_b16 v[74:75], v28 offset:16896\n"
"ds_read_b64_tr_b16 v[76:77], v29 offset:16384\n"
"ds_read_b64_tr_b16 v[78:79], v29 offset:16896\n"
"s_add_u32 m0, s46, 2048\n"
"ds_read_b64_tr_b16 v[80:81], v26 offset:20480\n"
"buffer_load_dwordx4 v18, s[24:27], s44 offen lds\n"
"ds_read_b64_tr_b16 v[82:83], v26 offset:20992\n"
"ds_read_b64_tr_b16 v[84:85], v27 offset:20480\n"
"ds_read_b64_tr_b16 v[86:87], v27 offset:20992\n"
"s_add_u32 m0, s46, 3072\n"
"ds_read_b64_tr_b16 v[88:89], v28 offset:20480\n"
"buffer_load_dwordx4 v19, s[24:27], s44 offen lds\n"
"ds_read_b64_tr_b16 v[90:91], v28 offset:20992\n"
"ds_read_b64_tr_b16 v[92:93], v29 offset:20480\n"
"ds_read_b64_tr_b16 v[94:95], v29 offset:20992\n"
"s_waitcnt lgkmcnt(0)\n"
"s_barrier\n"
"v_mfma_f32_16x16x32_bf16 v[192:195], v[64:67], v[32:35], 0\n"
"v_mfma_f32_16x16x32_bf16 v[192:195], v[80:83], v[48:51], v[192:195]\n"
"v_mfma_f32_16x16x32_bf16 v[196:199], v[80:83], v[52:55], 0\n"
"v_xor_b32 v24, 0x10000, v24\n"
"v_mfma_f32_16x16x32_bf16 v[196:199], v[64:67], v[36:39], v[196:199]\n"
"v_xor_b32 v25, 0x10000, v25\n"
"v_mfma_f32_16x16x32_bf16 v[200:203], v[64:67], v[40:43], 0\n"
"v_xor_b32 v26, 0x10000, v26\n"
"v_mfma_f32_16x16x32_bf16 v[200:203], v[80:83], v[56:59], v[200:203]\n"
"v_xor_b32 v27, 0x10000, v27\n"
"v_mfma_f32_16x16x32_bf16 v[204:207], v[80:83], v[60:63], 0\n"
"v_xor_b32 v28, 0x10000, v28\n"
"v_mfma_f32_16x16x32_bf16 v[204:207], v[64:67], v[44:47], v[204:207]\n"
"v_xor_b32 v29, 0x10000, v29\n"
"v_mfma_f32_16x16x32_bf16 v[220:223], v[68:71], v[44:47], 0\n"
"s_xor_b32 s46, s46, 0x10000\n"
"s_xor_b32 s49, s49, 0x10000\n"
"v_mfma_f32_16x16x32_bf16 v[220:223], v[84:87], v[60:63], v[220:223]\n"
"v_mfma_f32_16x16x32_bf16 v[216:219], v[84:87], v[56:59], 0\n"
"v_mfma_f32_16x16x32_bf16 v[216:219], v[68:71], v[40:43], v[216:219]\n"
"v_mfma_f32_16x16x32_bf16 v[212:215], v[68:71], v[36:39], 0\n"
"v_mfma_f32_16x16x32_bf16 v[212:215], v[84:87], v[52:55], v[212:215]\n"
"v_mfma_f32_16x16x32_bf16 v[208:211], v[84:87], v[48:51], 0\n"
"v_mfma_f32_16x16x32_bf16 v[208:211], v[68:71], v[32:35], v[208:211]\n"
"v_mfma_f32_16x16x32_bf16 v[224:227], v[72:75], v[32:35], 0\n"
"v_mfma_f32_16x16x32_bf16 v[224:227], v[88:91], v[48:51], v[224:227]\n"
"v_mfma_f32_16x16x32_bf16 v[228:231], v[88:91], v[52:55], 0\n"
"v_mfma_f32_16x16x32_bf16 v[228:231], v[72:75], v[36:39], v[228:231]\n"
"v_mfma_f32_16x16x32_bf16 v[232:235], v[72:75], v[40:43], 0\n"
"v_mfma_f32_16x16x32_bf16 v[232:235], v[88:91], v[56:59], v[232:235]\n"
"v_mfma_f32_16x16x32_bf16 v[236:239], v[88:91], v[60:63], 0\n"
"v_mfma_f32_16x16x32_bf16 v[236:239], v[72:75], v[44:47], v[236:239]\n"
"v_mfma_f32_16x16x32_bf16 v[252:255], v[76:79], v[44:47], 0\n"
"v_mfma_f32_16x16x32_bf16 v[252:255], v[92:95], v[60:63], v[252:255]\n"
"v_mfma_f32_16x16x32_bf16 v[248:251], v[92:95], v[56:59], 0\n"
"v_mfma_f32_16x16x32_bf16 v[248:251], v[76:79], v[40:43], v[248:251]\n"
"v_mfma_f32_16x16x32_bf16 v[244:247], v[76:79], v[36:39], 0\n"
"v_mfma_f32_16x16x32_bf16 v[244:247], v[92:95], v[52:55], v[244:247]\n"
"v_mfma_f32_16x16x32_bf16 v[240:243], v[92:95], v[48:51], 0\n"
"v_mfma_f32_16x16x32_bf16 v[240:243], v[76:79], v[32:35], v[240:243]\n"
"s_waitcnt vmcnt(4)\n"
"s_barrier\n"
"s_mov_b32 s47, 13\n"
"P1_loopA:\n"
"s_add_u32 s45, s45, s98\n"
"s_add_u32 m0, s49, 32768\n"
"ds_read_b64_tr_b16 v[64:65], v26 offset:0\n"
"buffer_load_dwordx4 v20, s[28:31], s45 offen lds\n"
"ds_read_b64_tr_b16 v[66:67], v26 offset:512\n"
"ds_read_b64_tr_b16 v[68:69], v27 offset:0\n"
"ds_read_b64_tr_b16 v[70:71], v27 offset:512\n"
"ds_read_b64_tr_b16 v[72:73], v28 offset:0\n"
"ds_read_b64_tr_b16 v[74:75], v28 offset:512\n"
"s_add_u32 m0, s49, 33792\n"
"ds_read_b64_tr_b16 v[76:77], v29 offset:0\n"
"buffer_load_dwordx4 v21, s[28:31], s45 offen lds\n"
"ds_read_b64_tr_b16 v[78:79], v29 offset:512\n"
"ds_read_b64_tr_b16 v[80:81], v26 offset:4096\n"
"ds_read_b64_tr_b16 v[82:83], v26 offset:4608\n"
"ds_read_b64_tr_b16 v[84:85], v27 offset:4096\n"
"ds_read_b64_tr_b16 v[86:87], v27 offset:4608\n"
"s_add_u32 m0, s49, 34816\n"
"ds_read_b64_tr_b16 v[88:89], v28 offset:4096\n"
"buffer_load_dwordx4 v22, s[28:31], s45 offen lds\n"
"ds_read_b64_tr_b16 v[90:91], v28 offset:4608\n"
"ds_read_b64_tr_b16 v[92:93], v29 offset:4096\n"
"ds_read_b64_tr_b16 v[94:95], v29 offset:4608\n"
"ds_read_b128 v[32:35], v24 offset:0\n"
"ds_read_b128 v[36:39], v24 offset:2048\n"
"s_add_u32 m0, s49, 35840\n"
"ds_read_b128 v[40:43], v24 offset:4096\n"
"buffer_load_dwordx4 v23, s[28:31], s45 offen lds\n"
"ds_read_b128 v[44:47], v24 offset:6144\n"
"ds_read_b128 v[48:51], v25 offset:0\n"
"ds_read_b128 v[52:55], v25 offset:2048\n"
"ds_read_b128 v[56:59], v25 offset:4096\n"
"ds_read_b128 v[60:63], v25 offset:6144\n"
"s_waitcnt lgkmcnt(0)\n"
"s_barrier\n"
"v_mfma_f32_16x16x32_bf16 v[128:131], v[64:67], v[32:35], v[128:131]\n"
"v_mfma_f32_16x16x32_bf16 v[128:131], v[80:83], v[48:51], v[128:131]\n"
"v_mfma_f32_16x16x32_bf16 v[132:135], v[80:83], v[52:55], v[132:135]\n"
"v_mfma_f32_16x16x32_bf16 v[132:135], v[64:67], v[36:39], v[132:135]\n"
"v_mfma_f32_16x16x32_bf16 v[136:139], v[64:67], v[40:43], v[136:139]\n"
"v_mfma_f32_16x16x32_bf16 v[136:139], v[80:83], v[56:59], v[136:139]\n"
"v_mfma_f32_16x16x32_bf16 v[140:143], v[80:83], v[60:63], v[140:143]\n"
"v_mfma_f32_16x16x32_bf16 v[140:143], v[64:67], v[44:47], v[140:143]\n"
"v_mfma_f32_16x16x32_bf16 v[156:159], v[68:71], v[44:47], v[156:159]\n"
"v_mfma_f32_16x16x32_bf16 v[156:159], v[84:87], v[60:63], v[156:159]\n"
"v_mfma_f32_16x16x32_bf16 v[152:155], v[84:87], v[56:59], v[152:155]\n"
"v_mfma_f32_16x16x32_bf16 v[152:155], v[68:71], v[40:43], v[152:155]\n"
"v_mfma_f32_16x16x32_bf16 v[148:151], v[68:71], v[36:39], v[148:151]\n"
"v_mfma_f32_16x16x32_bf16 v[148:151], v[84:87], v[52:55], v[148:151]\n"
"v_mfma_f32_16x16x32_bf16 v[144:147], v[84:87], v[48:51], v[144:147]\n"
"v_mfma_f32_16x16x32_bf16 v[144:147], v[68:71], v[32:35], v[144:147]\n"
"v_mfma_f32_16x16x32_bf16 v[160:163], v[72:75], v[32:35], v[160:163]\n"
"v_mfma_f32_16x16x32_bf16 v[160:163], v[88:91], v[48:51], v[160:163]\n"
"v_mfma_f32_16x16x32_bf16 v[164:167], v[88:91], v[52:55], v[164:167]\n"
"v_mfma_f32_16x16x32_bf16 v[164:167], v[72:75], v[36:39], v[164:167]\n"
"v_mfma_f32_16x16x32_bf16 v[168:171], v[72:75], v[40:43], v[168:171]\n"
"v_mfma_f32_16x16x32_bf16 v[168:171], v[88:91], v[56:59], v[168:171]\n"
"v_mfma_f32_16x16x32_bf16 v[172:175], v[88:91], v[60:63], v[172:175]\n"
"v_mfma_f32_16x16x32_bf16 v[172:175], v[72:75], v[44:47], v[172:175]\n"
"v_mfma_f32_16x16x32_bf16 v[188:191], v[76:79], v[44:47], v[188:191]\n"
"v_mfma_f32_16x16x32_bf16 v[188:191], v[92:95], v[60:63], v[188:191]\n"
"v_mfma_f32_16x16x32_bf16 v[184:187], v[92:95], v[56:59], v[184:187]\n"
"v_mfma_f32_16x16x32_bf16 v[184:187], v[76:79], v[40:43], v[184:187]\n"
"v_mfma_f32_16x16x32_bf16 v[180:183], v[76:79], v[36:39], v[180:183]\n"
"v_mfma_f32_16x16x32_bf16 v[180:183], v[92:95], v[52:55], v[180:183]\n"
"v_mfma_f32_16x16x32_bf16 v[176:179], v[92:95], v[48:51], v[176:179]\n"
"v_mfma_f32_16x16x32_bf16 v[176:179], v[76:79], v[32:35], v[176:179]\n"
"s_barrier\n"
"s_add_u32 s44, s44, 128\n"
"s_add_u32 m0, s46, 0\n"
"ds_read_b64_tr_b16 v[64:65], v26 offset:16384\n"
"buffer_load_dwordx4 v16, s[24:27], s44 offen lds\n"
"ds_read_b64_tr_b16 v[66:67], v26 offset:16896\n"
"ds_read_b64_tr_b16 v[68:69], v27 offset:16384\n"
"ds_read_b64_tr_b16 v[70:71], v27 offset:16896\n"
"s_add_u32 m0, s46, 1024\n"
"ds_read_b64_tr_b16 v[72:73], v28 offset:16384\n"
"buffer_load_dwordx4 v17, s[24:27], s44 offen lds\n"
"ds_read_b64_tr_b16 v[74:75], v28 offset:16896\n"
"ds_read_b64_tr_b16 v[76:77], v29 offset:16384\n"
"ds_read_b64_tr_b16 v[78:79], v29 offset:16896\n"
"s_add_u32 m0, s46, 2048\n"
"ds_read_b64_tr_b16 v[80:81], v26 offset:20480\n"
"buffer_load_dwordx4 v18, s[24:27], s44 offen lds\n"
"ds_read_b64_tr_b16 v[82:83], v26 offset:20992\n"
"ds_read_b64_tr_b16 v[84:85], v27 offset:20480\n"
"ds_read_b64_tr_b16 v[86:87], v27 offset:20992\n"
"s_add_u32 m0, s46, 3072\n"
"ds_read_b64_tr_b16 v[88:89], v28 offset:20480\n"
"buffer_load_dwordx4 v19, s[24:27], s44 offen lds\n"
"ds_read_b64_tr_b16 v[90:91], v28 offset:20992\n"
"ds_read_b64_tr_b16 v[92:93], v29 offset:20480\n"
"ds_read_b64_tr_b16 v[94:95], v29 offset:20992\n"
"s_waitcnt lgkmcnt(0)\n"
"s_barrier\n"
"v_mfma_f32_16x16x32_bf16 v[192:195], v[64:67], v[32:35], v[192:195]\n"
"v_mfma_f32_16x16x32_bf16 v[192:195], v[80:83], v[48:51], v[192:195]\n"
"v_mfma_f32_16x16x32_bf16 v[196:199], v[80:83], v[52:55], v[196:199]\n"
"v_xor_b32 v24, 0x10000, v24\n"
"v_mfma_f32_16x16x32_bf16 v[196:199], v[64:67], v[36:39], v[196:199]\n"
"v_xor_b32 v25, 0x10000, v25\n"
"v_mfma_f32_16x16x32_bf16 v[200:203], v[64:67], v[40:43], v[200:203]\n"
"v_xor_b32 v26, 0x10000, v26\n"
"v_mfma_f32_16x16x32_bf16 v[200:203], v[80:83], v[56:59], v[200:203]\n"
"v_xor_b32 v27, 0x10000, v27\n"
"v_mfma_f32_16x16x32_bf16 v[204:207], v[80:83], v[60:63], v[204:207]\n"
"v_xor_b32 v28, 0x10000, v28\n"
"v_mfma_f32_16x16x32_bf16 v[204:207], v[64:67], v[44:47], v[204:207]\n"
"v_xor_b32 v29, 0x10000, v29\n"
"v_mfma_f32_16x16x32_bf16 v[220:223], v[68:71], v[44:47], v[220:223]\n"
"s_xor_b32 s46, s46, 0x10000\n"
"s_xor_b32 s49, s49, 0x10000\n"
"v_mfma_f32_16x16x32_bf16 v[220:223], v[84:87], v[60:63], v[220:223]\n"
"v_mfma_f32_16x16x32_bf16 v[216:219], v[84:87], v[56:59], v[216:219]\n"
"v_mfma_f32_16x16x32_bf16 v[216:219], v[68:71], v[40:43], v[216:219]\n"
"v_mfma_f32_16x16x32_bf16 v[212:215], v[68:71], v[36:39], v[212:215]\n"
"v_mfma_f32_16x16x32_bf16 v[212:215], v[84:87], v[52:55], v[212:215]\n"
"v_mfma_f32_16x16x32_bf16 v[208:211], v[84:87], v[48:51], v[208:211]\n"
"v_mfma_f32_16x16x32_bf16 v[208:211], v[68:71], v[32:35], v[208:211]\n"
"v_mfma_f32_16x16x32_bf16 v[224:227], v[72:75], v[32:35], v[224:227]\n"
"v_mfma_f32_16x16x32_bf16 v[224:227], v[88:91], v[48:51], v[224:227]\n"
"v_mfma_f32_16x16x32_bf16 v[228:231], v[88:91], v[52:55], v[228:231]\n"
"v_mfma_f32_16x16x32_bf16 v[228:231], v[72:75], v[36:39], v[228:231]\n"
"v_mfma_f32_16x16x32_bf16 v[232:235], v[72:75], v[40:43], v[232:235]\n"
"v_mfma_f32_16x16x32_bf16 v[232:235], v[88:91], v[56:59], v[232:235]\n"
"v_mfma_f32_16x16x32_bf16 v[236:239], v[88:91], v[60:63], v[236:239]\n"
"v_mfma_f32_16x16x32_bf16 v[236:239], v[72:75], v[44:47], v[236:239]\n"
"v_mfma_f32_16x16x32_bf16 v[252:255], v[76:79], v[44:47], v[252:255]\n"
"v_mfma_f32_16x16x32_bf16 v[252:255], v[92:95], v[60:63], v[252:255]\n"
"v_mfma_f32_16x16x32_bf16 v[248:251], v[92:95], v[56:59], v[248:251]\n"
"v_mfma_f32_16x16x32_bf16 v[248:251], v[76:79], v[40:43], v[248:251]\n"
"v_mfma_f32_16x16x32_bf16 v[244:247], v[76:79], v[36:39], v[244:247]\n"
"v_mfma_f32_16x16x32_bf16 v[244:247], v[92:95], v[52:55], v[244:247]\n"
"v_mfma_f32_16x16x32_bf16 v[240:243], v[92:95], v[48:51], v[240:243]\n"
"v_mfma_f32_16x16x32_bf16 v[240:243], v[76:79], v[32:35], v[240:243]\n"
"s_waitcnt vmcnt(4)\n"
"s_barrier\n"
"s_sub_u32 s47, s47, 1\n"
"s_cmp_lg_u32 s47, 0\n"
"s_cbranch_scc1 P1_loopA\n"
"s_add_u32 s45, s45, s98\n"
"s_add_u32 m0, s49, 32768\n"
"ds_read_b64_tr_b16 v[64:65], v26 offset:0\n"
"buffer_load_dwordx4 v20, s[28:31], s45 offen lds\n"
"ds_read_b64_tr_b16 v[66:67], v26 offset:512\n"
"ds_read_b64_tr_b16 v[68:69], v27 offset:0\n"
"ds_read_b64_tr_b16 v[70:71], v27 offset:512\n"
"ds_read_b64_tr_b16 v[72:73], v28 offset:0\n"
"ds_read_b64_tr_b16 v[74:75], v28 offset:512\n"
"s_add_u32 m0, s49, 33792\n"
"ds_read_b64_tr_b16 v[76:77], v29 offset:0\n"
"buffer_load_dwordx4 v21, s[28:31], s45 offen lds\n"
"ds_read_b64_tr_b16 v[78:79], v29 offset:512\n"
"ds_read_b64_tr_b16 v[80:81], v26 offset:4096\n"
"ds_read_b64_tr_b16 v[82:83], v26 offset:4608\n"
"ds_read_b64_tr_b16 v[84:85], v27 offset:4096\n"
"ds_read_b64_tr_b16 v[86:87], v27 offset:4608\n"
"s_add_u32 m0, s49, 34816\n"
"ds_read_b64_tr_b16 v[88:89], v28 offset:4096\n"
"buffer_load_dwordx4 v22, s[28:31], s45 offen lds\n"
"ds_read_b64_tr_b16 v[90:91], v28 offset:4608\n"
"ds_read_b64_tr_b16 v[92:93], v29 offset:4096\n"
"ds_read_b64_tr_b16 v[94:95], v29 offset:4608\n"
"ds_read_b128 v[32:35], v24 offset:0\n"
"ds_read_b128 v[36:39], v24 offset:2048\n"
"s_add_u32 m0, s49, 35840\n"
"ds_read_b128 v[40:43], v24 offset:4096\n"
"buffer_load_dwordx4 v23, s[28:31], s45 offen lds\n"
"ds_read_b128 v[44:47], v24 offset:6144\n"
"ds_read_b128 v[48:51], v25 offset:0\n"
"ds_read_b128 v[52:55], v25 offset:2048\n"
"ds_read_b128 v[56:59], v25 offset:4096\n"
"ds_read_b128 v[60:63], v25 offset:6144\n"
"s_waitcnt lgkmcnt(0)\n"
"s_barrier\n"
"v_mfma_f32_16x16x32_bf16 v[128:131], v[64:67], v[32:35], v[128:131]\n"
"v_mfma_f32_16x16x32_bf16 v[128:131], v[80:83], v[48:51], v[128:131]\n"
"v_mfma_f32_16x16x32_bf16 v[132:135], v[80:83], v[52:55], v[132:135]\n"
"v_mfma_f32_16x16x32_bf16 v[132:135], v[64:67], v[36:39], v[132:135]\n"
"v_mfma_f32_16x16x32_bf16 v[136:139], v[64:67], v[40:43], v[136:139]\n"
"v_mfma_f32_16x16x32_bf16 v[136:139], v[80:83], v[56:59], v[136:139]\n"
"v_mfma_f32_16x16x32_bf16 v[140:143], v[80:83], v[60:63], v[140:143]\n"
"v_mfma_f32_16x16x32_bf16 v[140:143], v[64:67], v[44:47], v[140:143]\n"
"v_mfma_f32_16x16x32_bf16 v[156:159], v[68:71], v[44:47], v[156:159]\n"
"v_mfma_f32_16x16x32_bf16 v[156:159], v[84:87], v[60:63], v[156:159]\n"
"v_mfma_f32_16x16x32_bf16 v[152:155], v[84:87], v[56:59], v[152:155]\n"
"v_mfma_f32_16x16x32_bf16 v[152:155], v[68:71], v[40:43], v[152:155]\n"
"v_mfma_f32_16x16x32_bf16 v[148:151], v[68:71], v[36:39], v[148:151]\n"
"v_mfma_f32_16x16x32_bf16 v[148:151], v[84:87], v[52:55], v[148:151]\n"
"v_mfma_f32_16x16x32_bf16 v[144:147], v[84:87], v[48:51], v[144:147]\n"
"v_mfma_f32_16x16x32_bf16 v[144:147], v[68:71], v[32:35], v[144:147]\n"
"v_mfma_f32_16x16x32_bf16 v[160:163], v[72:75], v[32:35], v[160:163]\n"
"v_mfma_f32_16x16x32_bf16 v[160:163], v[88:91], v[48:51], v[160:163]\n"
"v_mfma_f32_16x16x32_bf16 v[164:167], v[88:91], v[52:55], v[164:167]\n"
"v_mfma_f32_16x16x32_bf16 v[164:167], v[72:75], v[36:39], v[164:167]\n"
"v_mfma_f32_16x16x32_bf16 v[168:171], v[72:75], v[40:43], v[168:171]\n"
"v_mfma_f32_16x16x32_bf16 v[168:171], v[88:91], v[56:59], v[168:171]\n"
"v_mfma_f32_16x16x32_bf16 v[172:175], v[88:91], v[60:63], v[172:175]\n"
"v_mfma_f32_16x16x32_bf16 v[172:175], v[72:75], v[44:47], v[172:175]\n"
"v_mfma_f32_16x16x32_bf16 v[188:191], v[76:79], v[44:47], v[188:191]\n"
"v_mfma_f32_16x16x32_bf16 v[188:191], v[92:95], v[60:63], v[188:191]\n"
"v_mfma_f32_16x16x32_bf16 v[184:187], v[92:95], v[56:59], v[184:187]\n"
"v_mfma_f32_16x16x32_bf16 v[184:187], v[76:79], v[40:43], v[184:187]\n"
"v_mfma_f32_16x16x32_bf16 v[180:183], v[76:79], v[36:39], v[180:183]\n"
"v_mfma_f32_16x16x32_bf16 v[180:183], v[92:95], v[52:55], v[180:183]\n"
"v_mfma_f32_16x16x32_bf16 v[176:179], v[92:95], v[48:51], v[176:179]\n"
"v_mfma_f32_16x16x32_bf16 v[176:179], v[76:79], v[32:35], v[176:179]\n"
"s_barrier\n"
"s_mov_b32 s48, 0\n"
"s_add_u32 m0, s60, 0\n"
"ds_read_b64_tr_b16 v[64:65], v26 offset:16384\n"
"buffer_load_dwordx4 v124, s[36:39], s48 offen lds\n"
"ds_read_b64_tr_b16 v[66:67], v26 offset:16896\n"
"ds_read_b64_tr_b16 v[68:69], v27 offset:16384\n"
"ds_read_b64_tr_b16 v[70:71], v27 offset:16896\n"
"s_add_u32 m0, s60, 1024\n"
"ds_read_b64_tr_b16 v[72:73], v28 offset:16384\n"
"buffer_load_dwordx4 v125, s[36:39], s48 offen lds\n"
"ds_read_b64_tr_b16 v[74:75], v28 offset:16896\n"
"ds_read_b64_tr_b16 v[76:77], v29 offset:16384\n"
"ds_read_b64_tr_b16 v[78:79], v29 offset:16896\n"
"s_add_u32 m0, s60, 2048\n"
"ds_read_b64_tr_b16 v[80:81], v26 offset:20480\n"
"buffer_load_dwordx4 v126, s[36:39], s48 offen lds\n"
"ds_read_b64_tr_b16 v[82:83], v26 offset:20992\n"
"ds_read_b64_tr_b16 v[84:85], v27 offset:20480\n"
"ds_read_b64_tr_b16 v[86:87], v27 offset:20992\n"
"s_add_u32 m0, s60, 3072\n"
"ds_read_b64_tr_b16 v[88:89], v28 offset:20480\n"
"buffer_load_dwordx4 v127, s[36:39], s48 offen lds\n"
"ds_read_b64_tr_b16 v[90:91], v28 offset:20992\n"
"ds_read_b64_tr_b16 v[92:93], v29 offset:20480\n"
"ds_read_b64_tr_b16 v[94:95], v29 offset:20992\n"
"s_waitcnt lgkmcnt(0)\n"
"s_barrier\n"
"v_mfma_f32_16x16x32_bf16 v[192:195], v[64:67], v[32:35], v[192:195]\n"
"v_mfma_f32_16x16x32_bf16 v[192:195], v[80:83], v[48:51], v[192:195]\n"
"v_mfma_f32_16x16x32_bf16 v[196:199], v[80:83], v[52:55], v[196:199]\n"
"v_xor_b32 v24, 0x10000, v24\n"
"v_mfma_f32_16x16x32_bf16 v[196:199], v[64:67], v[36:39], v[196:199]\n"
"v_xor_b32 v25, 0x10000, v25\n"
"v_mfma_f32_16x16x32_bf16 v[200:203], v[64:67], v[40:43], v[200:203]\n"
"v_xor_b32 v26, 0x10000, v26\n"
"v_mfma_f32_16x16x32_bf16 v[200:203], v[80:83], v[56:59], v[200:203]\n"
"v_xor_b32 v27, 0x10000, v27\n"
"v_mfma_f32_16x16x32_bf16 v[204:207], v[80:83], v[60:63], v[204:207]\n"
"v_xor_b32 v28, 0x10000, v28\n"
"v_mfma_f32_16x16x32_bf16 v[204:207], v[64:67], v[44:47], v[204:207]\n"
"v_xor_b32 v29, 0x10000, v29\n"
"v_mfma_f32_16x16x32_bf16 v[220:223], v[68:71], v[44:47], v[220:223]\n"
"s_xor_b32 s46, s46, 0x10000\n"
"s_xor_b32 s49, s49, 0x10000\n"
"v_mfma_f32_16x16x32_bf16 v[220:223], v[84:87], v[60:63], v[220:223]\n"
"v_mfma_f32_16x16x32_bf16 v[216:219], v[84:87], v[56:59], v[216:219]\n"
"v_mfma_f32_16x16x32_bf16 v[216:219], v[68:71], v[40:43], v[216:219]\n"
"v_mfma_f32_16x16x32_bf16 v[212:215], v[68:71], v[36:39], v[212:215]\n"
"v_mfma_f32_16x16x32_bf16 v[212:215], v[84:87], v[52:55], v[212:215]\n"
"v_mfma_f32_16x16x32_bf16 v[208:211], v[84:87], v[48:51], v[208:211]\n"
"v_mfma_f32_16x16x32_bf16 v[208:211], v[68:71], v[32:35], v[208:211]\n"
"v_mfma_f32_16x16x32_bf16 v[224:227], v[72:75], v[32:35], v[224:227]\n"
"v_mfma_f32_16x16x32_bf16 v[224:227], v[88:91], v[48:51], v[224:227]\n"
"v_mfma_f32_16x16x32_bf16 v[228:231], v[88:91], v[52:55], v[228:231]\n"
"v_mfma_f32_16x16x32_bf16 v[228:231], v[72:75], v[36:39], v[228:231]\n"
"v_mfma_f32_16x16x32_bf16 v[232:235], v[72:75], v[40:43], v[232:235]\n"
"v_mfma_f32_16x16x32_bf16 v[232:235], v[88:91], v[56:59], v[232:235]\n"
"v_mfma_f32_16x16x32_bf16 v[236:239], v[88:91], v[60:63], v[236:239]\n"
"v_mfma_f32_16x16x32_bf16 v[236:239], v[72:75], v[44:47], v[236:239]\n"
"v_mfma_f32_16x16x32_bf16 v[252:255], v[76:79], v[44:47], v[252:255]\n"
"v_mfma_f32_16x16x32_bf16 v[252:255], v[92:95], v[60:63], v[252:255]\n"
"v_mfma_f32_16x16x32_bf16 v[248:251], v[92:95], v[56:59], v[248:251]\n"
"v_mfma_f32_16x16x32_bf16 v[248:251], v[76:79], v[40:43], v[248:251]\n"
"v_mfma_f32_16x16x32_bf16 v[244:247], v[76:79], v[36:39], v[244:247]\n"
"v_mfma_f32_16x16x32_bf16 v[244:247], v[92:95], v[52:55], v[244:247]\n"
"v_mfma_f32_16x16x32_bf16 v[240:243], v[92:95], v[48:51], v[240:243]\n"
"v_mfma_f32_16x16x32_bf16 v[240:243], v[76:79], v[32:35], v[240:243]\n"
"s_waitcnt vmcnt(4)\n"
"s_barrier\n"
"s_mov_b32 s48, 16384\n"
"s_add_u32 m0, s60, 32768\n"
"ds_read_b64_tr_b16 v[64:65], v26 offset:0\n"
"buffer_load_dwordx4 v124, s[36:39], s48 offen lds\n"
"ds_read_b64_tr_b16 v[66:67], v26 offset:512\n"
"ds_read_b64_tr_b16 v[68:69], v27 offset:0\n"
"ds_read_b64_tr_b16 v[70:71], v27 offset:512\n"
"ds_read_b64_tr_b16 v[72:73], v28 offset:0\n"
"ds_read_b64_tr_b16 v[74:75], v28 offset:512\n"
"s_add_u32 m0, s60, 33792\n"
"ds_read_b64_tr_b16 v[76:77], v29 offset:0\n"
"buffer_load_dwordx4 v125, s[36:39], s48 offen lds\n"
"ds_read_b64_tr_b16 v[78:79], v29 offset:512\n"
"ds_read_b64_tr_b16 v[80:81], v26 offset:4096\n"
"ds_read_b64_tr_b16 v[82:83], v26 offset:4608\n"
"ds_read_b64_tr_b16 v[84:85], v27 offset:4096\n"
"ds_read_b64_tr_b16 v[86:87], v27 offset:4608\n"
"s_add_u32 m0, s60, 34816\n"
"ds_read_b64_tr_b16 v[88:89], v28 offset:4096\n"
"buffer_load_dwordx4 v126, s[36:39], s48 offen lds\n"
"ds_read_b64_tr_b16 v[90:91], v28 offset:4608\n"
"ds_read_b64_tr_b16 v[92:93], v29 offset:4096\n"
"ds_read_b64_tr_b16 v[94:95], v29 offset:4608\n"
"ds_read_b128 v[32:35], v24 offset:0\n"
"ds_read_b128 v[36:39], v24 offset:2048\n"
"s_add_u32 m0, s60, 35840\n"
"ds_read_b128 v[40:43], v24 offset:4096\n"
"buffer_load_dwordx4 v127, s[36:39], s48 offen lds\n"
"ds_read_b128 v[44:47], v24 offset:6144\n"
"ds_read_b128 v[48:51], v25 offset:0\n"
"ds_read_b128 v[52:55], v25 offset:2048\n"
"ds_read_b128 v[56:59], v25 offset:4096\n"
"ds_read_b128 v[60:63], v25 offset:6144\n"
"s_waitcnt lgkmcnt(0)\n"
"s_barrier\n"
"v_mfma_f32_16x16x32_bf16 v[128:131], v[64:67], v[32:35], v[128:131]\n"
"v_mfma_f32_16x16x32_bf16 v[128:131], v[80:83], v[48:51], v[128:131]\n"
"v_mfma_f32_16x16x32_bf16 v[132:135], v[80:83], v[52:55], v[132:135]\n"
"v_mfma_f32_16x16x32_bf16 v[132:135], v[64:67], v[36:39], v[132:135]\n"
"v_mfma_f32_16x16x32_bf16 v[136:139], v[64:67], v[40:43], v[136:139]\n"
"v_mfma_f32_16x16x32_bf16 v[136:139], v[80:83], v[56:59], v[136:139]\n"
"v_mfma_f32_16x16x32_bf16 v[140:143], v[80:83], v[60:63], v[140:143]\n"
"v_mfma_f32_16x16x32_bf16 v[140:143], v[64:67], v[44:47], v[140:143]\n"
"v_mfma_f32_16x16x32_bf16 v[156:159], v[68:71], v[44:47], v[156:159]\n"
"v_mfma_f32_16x16x32_bf16 v[156:159], v[84:87], v[60:63], v[156:159]\n"
"v_mfma_f32_16x16x32_bf16 v[152:155], v[84:87], v[56:59], v[152:155]\n"
"v_mfma_f32_16x16x32_bf16 v[152:155], v[68:71], v[40:43], v[152:155]\n"
"v_mfma_f32_16x16x32_bf16 v[148:151], v[68:71], v[36:39], v[148:151]\n"
"v_mfma_f32_16x16x32_bf16 v[148:151], v[84:87], v[52:55], v[148:151]\n"
"v_mfma_f32_16x16x32_bf16 v[144:147], v[84:87], v[48:51], v[144:147]\n"
"v_mfma_f32_16x16x32_bf16 v[144:147], v[68:71], v[32:35], v[144:147]\n"
"v_mfma_f32_16x16x32_bf16 v[160:163], v[72:75], v[32:35], v[160:163]\n"
"v_mfma_f32_16x16x32_bf16 v[160:163], v[88:91], v[48:51], v[160:163]\n"
"v_mfma_f32_16x16x32_bf16 v[164:167], v[88:91], v[52:55], v[164:167]\n"
"v_mfma_f32_16x16x32_bf16 v[164:167], v[72:75], v[36:39], v[164:167]\n"
"v_mfma_f32_16x16x32_bf16 v[168:171], v[72:75], v[40:43], v[168:171]\n"
"v_mfma_f32_16x16x32_bf16 v[168:171], v[88:91], v[56:59], v[168:171]\n"
"v_mfma_f32_16x16x32_bf16 v[172:175], v[88:91], v[60:63], v[172:175]\n"
"v_mfma_f32_16x16x32_bf16 v[172:175], v[72:75], v[44:47], v[172:175]\n"
"v_mfma_f32_16x16x32_bf16 v[188:191], v[76:79], v[44:47], v[188:191]\n"
"v_mfma_f32_16x16x32_bf16 v[188:191], v[92:95], v[60:63], v[188:191]\n"
"v_mfma_f32_16x16x32_bf16 v[184:187], v[92:95], v[56:59], v[184:187]\n"
"v_mfma_f32_16x16x32_bf16 v[184:187], v[76:79], v[40:43], v[184:187]\n"
"v_mfma_f32_16x16x32_bf16 v[180:183], v[76:79], v[36:39], v[180:183]\n"
"v_mfma_f32_16x16x32_bf16 v[180:183], v[92:95], v[52:55], v[180:183]\n"
"v_mfma_f32_16x16x32_bf16 v[176:179], v[92:95], v[48:51], v[176:179]\n"
"v_mfma_f32_16x16x32_bf16 v[176:179], v[76:79], v[32:35], v[176:179]\n"
"s_barrier\n"
"s_mov_b32 s48, 32768\n"
"s_add_u32 m0, s60, 65536\n"
"ds_read_b64_tr_b16 v[64:65], v26 offset:16384\n"
"buffer_load_dwordx4 v124, s[36:39], s48 offen lds\n"
"ds_read_b64_tr_b16 v[66:67], v26 offset:16896\n"
"ds_read_b64_tr_b16 v[68:69], v27 offset:16384\n"
"ds_read_b64_tr_b16 v[70:71], v27 offset:16896\n"
"s_add_u32 m0, s60, 66560\n"
"ds_read_b64_tr_b16 v[72:73], v28 offset:16384\n"
"buffer_load_dwordx4 v125, s[36:39], s48 offen lds\n"
"ds_read_b64_tr_b16 v[74:75], v28 offset:16896\n"
"ds_read_b64_tr_b16 v[76:77], v29 offset:16384\n"
"ds_read_b64_tr_b16 v[78:79], v29 offset:16896\n"
"s_add_u32 m0, s60, 67584\n"
"ds_read_b64_tr_b16 v[80:81], v26 offset:20480\n"
"buffer_load_dwordx4 v126, s[36:39], s48 offen lds\n"
"ds_read_b64_tr_b16 v[82:83], v26 offset:20992\n"
"ds_read_b64_tr_b16 v[84:85], v27 offset:20480\n"
"ds_read_b64_tr_b16 v[86:87], v27 offset:20992\n"
"s_add_u32 m0, s60, 68608\n"
"ds_read_b64_tr_b16 v[88:89], v28 offset:20480\n"
"buffer_load_dwordx4 v127, s[36:39], s48 offen lds\n"
"ds_read_b64_tr_b16 v[90:91], v28 offset:20992\n"
"ds_read_b64_tr_b16 v[92:93], v29 offset:20480\n"
"ds_read_b64_tr_b16 v[94:95], v29 offset:20992\n"
"s_waitcnt lgkmcnt(0)\n"
"s_barrier\n"
"v_mfma_f32_16x16x32_bf16 v[192:195], v[64:67], v[32:35], v[192:195]\n"
"v_mfma_f32_16x16x32_bf16 v[192:195], v[80:83], v[48:51], v[192:195]\n"
"v_mfma_f32_16x16x32_bf16 v[196:199], v[80:83], v[52:55], v[196:199]\n"
"v_xor_b32 v24, 0x10000, v24\n"
"v_mfma_f32_16x16x32_bf16 v[196:199], v[64:67], v[36:39], v[196:199]\n"
"v_xor_b32 v25, 0x10000, v25\n"
"v_mfma_f32_16x16x32_bf16 v[200:203], v[64:67], v[40:43], v[200:203]\n"
"v_xor_b32 v26, 0x10000, v26\n"
"v_mfma_f32_16x16x32_bf16 v[200:203], v[80:83], v[56:59], v[200:203]\n"
"v_xor_b32 v27, 0x10000, v27\n"
"v_mfma_f32_16x16x32_bf16 v[204:207], v[80:83], v[60:63], v[204:207]\n"
"v_xor_b32 v28, 0x10000, v28\n"
"v_mfma_f32_16x16x32_bf16 v[204:207], v[64:67], v[44:47], v[204:207]\n"
"v_xor_b32 v29, 0x10000, v29\n"
"v_mfma_f32_16x16x32_bf16 v[220:223], v[68:71], v[44:47], v[220:223]\n"
"s_xor_b32 s46, s46, 0x10000\n"
"s_xor_b32 s49, s49, 0x10000\n"
"v_mfma_f32_16x16x32_bf16 v[220:223], v[84:87], v[60:63], v[220:223]\n"
"v_mfma_f32_16x16x32_bf16 v[216:219], v[84:87], v[56:59], v[216:219]\n"
"v_mfma_f32_16x16x32_bf16 v[216:219], v[68:71], v[40:43], v[216:219]\n"
"v_mfma_f32_16x16x32_bf16 v[212:215], v[68:71], v[36:39], v[212:215]\n"
"v_mfma_f32_16x16x32_bf16 v[212:215], v[84:87], v[52:55], v[212:215]\n"
"v_mfma_f32_16x16x32_bf16 v[208:211], v[84:87], v[48:51], v[208:211]\n"
"v_mfma_f32_16x16x32_bf16 v[208:211], v[68:71], v[32:35], v[208:211]\n"
"v_mfma_f32_16x16x32_bf16 v[224:227], v[72:75], v[32:35], v[224:227]\n"
"v_mfma_f32_16x16x32_bf16 v[224:227], v[88:91], v[48:51], v[224:227]\n"
"v_mfma_f32_16x16x32_bf16 v[228:231], v[88:91], v[52:55], v[228:231]\n"
"v_mfma_f32_16x16x32_bf16 v[228:231], v[72:75], v[36:39], v[228:231]\n"
"v_mfma_f32_16x16x32_bf16 v[232:235], v[72:75], v[40:43], v[232:235]\n"
"v_mfma_f32_16x16x32_bf16 v[232:235], v[88:91], v[56:59], v[232:235]\n"
"v_mfma_f32_16x16x32_bf16 v[236:239], v[88:91], v[60:63], v[236:239]\n"
"v_mfma_f32_16x16x32_bf16 v[236:239], v[72:75], v[44:47], v[236:239]\n"
"v_mfma_f32_16x16x32_bf16 v[252:255], v[76:79], v[44:47], v[252:255]\n"
"v_mfma_f32_16x16x32_bf16 v[252:255], v[92:95], v[60:63], v[252:255]\n"
"v_mfma_f32_16x16x32_bf16 v[248:251], v[92:95], v[56:59], v[248:251]\n"
"v_mfma_f32_16x16x32_bf16 v[248:251], v[76:79], v[40:43], v[248:251]\n"
"v_mfma_f32_16x16x32_bf16 v[244:247], v[76:79], v[36:39], v[244:247]\n"
"v_mfma_f32_16x16x32_bf16 v[244:247], v[92:95], v[52:55], v[244:247]\n"
"v_mfma_f32_16x16x32_bf16 v[240:243], v[92:95], v[48:51], v[240:243]\n"
"v_mfma_f32_16x16x32_bf16 v[240:243], v[76:79], v[32:35], v[240:243]\n"
"s_barrier\n"
"s_barrier\n"
"s_nop 7\n"
"s_mov_b32 s48, 49152\n"
"s_add_u32 m0, s60, 98304\n"
"s_nop 0\n"
"buffer_load_dwordx4 v124, s[36:39], s48 offen lds\n"
"s_add_u32 m0, s60, 99328\n"
"s_nop 0\n"
"buffer_load_dwordx4 v125, s[36:39], s48 offen lds\n"
"s_add_u32 m0, s60, 100352\n"
"s_nop 0\n"
"buffer_load_dwordx4 v126, s[36:39], s48 offen lds\n"
"s_add_u32 m0, s60, 101376\n"
"s_nop 0\n"
"buffer_load_dwordx4 v127, s[36:39], s48 offen lds\n"
"s_load_dwordx4 s[8:11], s[4:5], 48\n"
"s_mov_b32 s52, 0x3e0293ee\n"
"s_mov_b32 s53, 0x3e0293ee\n"
"s_mov_b32 s54, 0xbdd2d3e8\n"
"s_mov_b32 s55, 0xbdd2d3e8\n"
"v_pk_mul_f32 v[136:137], v[136:137], s[52:53]\n"
"v_pk_mul_f32 v[138:139], v[138:139], s[52:53]\n"
"v_pk_mul_f32 v[152:153], v[152:153], s[52:53]\n"
"v_pk_mul_f32 v[154:155], v[154:155], s[52:53]\n"
"v_cvt_pk_bf16_f32 v64, v136, v137\n"
"v_cvt_pk_bf16_f32 v65, v138, v139\n"
"v_cvt_pk_bf16_f32 v66, v152, v153\n"
"v_cvt_pk_bf16_f32 v67, v154, v155\n"
"v_pk_mul_f32 v[168:169], v[168:169], s[52:53]\n"
"v_pk_mul_f32 v[170:171], v[170:171], s[52:53]\n"
"v_pk_mul_f32 v[184:185], v[184:185], s[52:53]\n"
"v_pk_mul_f32 v[186:187], v[186:187], s[52:53]\n"
"v_cvt_pk_bf16_f32 v68, v168, v169\n"
"v_cvt_pk_bf16_f32 v69, v170, v171\n"
"v_cvt_pk_bf16_f32 v70, v184, v185\n"
"v_cvt_pk_bf16_f32 v71, v186, v187\n"
"v_pk_mul_f32 v[140:141], v[140:141], s[52:53]\n"
"v_pk_mul_f32 v[142:143], v[142:143], s[52:53]\n"
"v_pk_mul_f32 v[156:157], v[156:157], s[52:53]\n"
"v_pk_mul_f32 v[158:159], v[158:159], s[52:53]\n"
"v_cvt_pk_bf16_f32 v72, v140, v141\n"
"v_cvt_pk_bf16_f32 v73, v142, v143\n"
"v_cvt_pk_bf16_f32 v74, v156, v157\n"
"v_cvt_pk_bf16_f32 v75, v158, v159\n"
"v_pk_mul_f32 v[172:173], v[172:173], s[52:53]\n"
"v_pk_mul_f32 v[174:175], v[174:175], s[52:53]\n"
"v_pk_mul_f32 v[188:189], v[188:189], s[52:53]\n"
"v_pk_mul_f32 v[190:191], v[190:191], s[52:53]\n"
"v_cvt_pk_bf16_f32 v76, v172, v173\n"
"v_cvt_pk_bf16_f32 v77, v174, v175\n"
"v_cvt_pk_bf16_f32 v78, v188, v189\n"
"v_cvt_pk_bf16_f32 v79, v190, v191\n"
"v_pk_mul_f32 v[128:129], v[128:129], s[52:53]\n"
"v_pk_mul_f32 v[130:131], v[130:131], s[52:53]\n"
"v_pk_mul_f32 v[144:145], v[144:145], s[52:53]\n"
"v_pk_mul_f32 v[146:147], v[146:147], s[52:53]\n"
"v_cvt_pk_bf16_f32 v32, v128, v129\n"
"v_cvt_pk_bf16_f32 v33, v130, v131\n"
"v_cvt_pk_bf16_f32 v34, v144, v145\n"
"v_cvt_pk_bf16_f32 v35, v146, v147\n"
"v_pk_mul_f32 v[160:161], v[160:161], s[52:53]\n"
"v_pk_mul_f32 v[162:163], v[162:163], s[52:53]\n"
"v_pk_mul_f32 v[176:177], v[176:177], s[52:53]\n"
"v_pk_mul_f32 v[178:179], v[178:179], s[52:53]\n"
"v_cvt_pk_bf16_f32 v36, v160, v161\n"
"v_cvt_pk_bf16_f32 v37, v162, v163\n"
"v_cvt_pk_bf16_f32 v38, v176, v177\n"
"v_cvt_pk_bf16_f32 v39, v178, v179\n"
"v_pk_mul_f32 v[132:133], v[132:133], s[52:53]\n"
"v_pk_mul_f32 v[134:135], v[134:135], s[52:53]\n"
"v_pk_mul_f32 v[148:149], v[148:149], s[52:53]\n"
"v_pk_mul_f32 v[150:151], v[150:151], s[52:53]\n"
"v_cvt_pk_bf16_f32 v48, v132, v133\n"
"v_cvt_pk_bf16_f32 v49, v134, v135\n"
"v_cvt_pk_bf16_f32 v50, v148, v149\n"
"v_cvt_pk_bf16_f32 v51, v150, v151\n"
"v_pk_mul_f32 v[164:165], v[164:165], s[52:53]\n"
"v_pk_mul_f32 v[166:167], v[166:167], s[52:53]\n"
"v_pk_mul_f32 v[180:181], v[180:181], s[52:53]\n"
"v_pk_mul_f32 v[182:183], v[182:183], s[52:53]\n"
"v_cvt_pk_bf16_f32 v52, v164, v165\n"
"v_cvt_pk_bf16_f32 v53, v166, v167\n"
"v_cvt_pk_bf16_f32 v54, v180, v181\n"
"v_cvt_pk_bf16_f32 v55, v182, v183\n"
"s_nop 1\n"
"v_permlane16_swap_b32 v64, v66\n"
"v_permlane16_swap_b32 v65, v67\n"
"v_permlane16_swap_b32 v68, v70\n"
"v_permlane16_swap_b32 v69, v71\n"
"v_permlane16_swap_b32 v72, v74\n"
"v_permlane16_swap_b32 v73, v75\n"
"v_permlane16_swap_b32 v76, v78\n"
"v_permlane16_swap_b32 v77, v79\n"
"v_permlane16_swap_b32 v32, v34\n"
"v_permlane16_swap_b32 v33, v35\n"
"v_permlane16_swap_b32 v36, v38\n"
"v_permlane16_swap_b32 v37, v39\n"
"v_permlane16_swap_b32 v48, v50\n"
"v_permlane16_swap_b32 v49, v51\n"
"v_permlane16_swap_b32 v52, v54\n"
"v_permlane16_swap_b32 v53, v55\n"
"v_lshlrev_b32 v2, 4, v1\n"
"s_lshl_b32 s50, s21, 12\n"
"s_add_u32 s50, s50, 131072\n"
"v_add_u32 v2, s50, v2\n"
"ds_write_b128 v2, v[64:67] offset:0\n"
"ds_write_b128 v2, v[68:71] offset:1024\n"
"ds_write_b128 v2, v[72:75] offset:2048\n"
"ds_write_b128 v2, v[76:79] offset:3072\n"
"v_lshlrev_b32 v128, 16, v8\n"
"v_and_b32 v129, 0xffff0000, v8\n"
"v_lshlrev_b32 v130, 16, v9\n"
"v_and_b32 v131, 0xffff0000, v9\n"
"v_lshlrev_b32 v132, 16, v10\n"
"v_and_b32 v133, 0xffff0000, v10\n"
"v_lshlrev_b32 v134, 16, v11\n"
"v_and_b32 v135, 0xffff0000, v11\n"
"v_lshlrev_b32 v136, 16, v12\n"
"v_and_b32 v137, 0xffff0000, v12\n"
"v_lshlrev_b32 v138, 16, v13\n"
"v_and_b32 v139, 0xffff0000, v13\n"
"v_lshlrev_b32 v140, 16, v14\n"
"v_and_b32 v141, 0xffff0000, v14\n"
"v_lshlrev_b32 v142, 16, v15\n"
"v_and_b32 v143, 0xffff0000, v15\n"
"v_mov_b32 v144, 0xc0135761\n"
"v_mov_b32 v145, 0xc0135761\n"
"v_mov_b32 v146, 1.0\n"
"v_mov_b32 v147, 1.0\n"
"s_lshl_b32 s56, s20, 3\n"
"s_add_u32 s56, s56, s21\n"
"s_lshl_b32 s57, s56, 12\n"
"s_mul_i32 s58, s56, 3584\n"
"v_lshlrev_b32 v2, 4, v1\n"
"v_add_u32 v24, s57, v2\n"
"v_add_u32 v25, s58, v2\n"
"v_add_u32 v26, 3072, v25\n"
"v_min_u32 v26, 7340016, v26\n"
"s_waitcnt lgkmcnt(0)\n"
"s_mul_i32 s50, s22, 1835008\n"
"s_mul_i32 s51, s23, 224\n"
"s_add_u32 s50, s50, s51\n"
"s_add_u32 s40, s8, s50\n"
"s_addc_u32 s41, s9, 0\n"
"s_and_b32 s41, s41, 0xffff\n"
"s_sub_u32 s42, 1835008, s51\n"
"s_mov_b32 s43, 0x20000\n"
"v_and_b32 v3, 15, v1\n"
"s_lshl_b32 s50, s34, 6\n"
"v_add_u32 v3, s50, v3\n"
"v_mul_u32_u24 v3, 7168, v3\n"
"v_lshrrev_b32 v4, 4, v1\n"
"v_and_b32 v5, 1, v4\n"
"v_lshlrev_b32 v5, 5, v5\n"
"v_lshrrev_b32 v6, 1, v4\n"
"v_lshlrev_b32 v6, 4, v6\n"
"v_add_u32 v5, v5, v6\n"
"v_add_u32 v5, 0, v5\n"
"v_add_u32 v148, v3, v5\n"
"v_pk_add_f32 v[192:193], v[192:193], v[128:129]\n"
"v_pk_add_f32 v[194:195], v[194:195], v[130:131]\n"
"v_pk_add_f32 v[208:209], v[208:209], v[132:133]\n"
"v_pk_add_f32 v[210:211], v[210:211], v[134:135]\n"
"v_pk_add_f32 v[224:225], v[224:225], v[136:137]\n"
"v_pk_add_f32 v[226:227], v[226:227], v[138:139]\n"
"v_pk_add_f32 v[240:241], v[240:241], v[140:141]\n"
"v_pk_add_f32 v[242:243], v[242:243], v[142:143]\n"
"v_pk_mul_f32 v[64:65], v[192:193], v[192:193]\n"
"v_pk_mul_f32 v[66:67], v[194:195], v[194:195]\n"
"v_pk_mul_f32 v[68:69], v[208:209], v[208:209]\n"
"v_pk_mul_f32 v[70:71], v[210:211], v[210:211]\n"
"v_pk_mul_f32 v[72:73], v[224:225], v[224:225]\n"
"v_pk_mul_f32 v[74:75], v[226:227], v[226:227]\n"
"v_pk_mul_f32 v[76:77], v[240:241], v[240:241]\n"
"v_pk_mul_f32 v[78:79], v[242:243], v[242:243]\n"
"v_pk_fma_f32 v[64:65], v[64:65], s[54:55], v[144:145]\n"
"v_pk_fma_f32 v[66:67], v[66:67], s[54:55], v[144:145]\n"
"v_pk_fma_f32 v[68:69], v[68:69], s[54:55], v[144:145]\n"
"v_pk_fma_f32 v[70:71], v[70:71], s[54:55], v[144:145]\n"
"v_pk_fma_f32 v[72:73], v[72:73], s[54:55], v[144:145]\n"
"v_pk_fma_f32 v[74:75], v[74:75], s[54:55], v[144:145]\n"
"v_pk_fma_f32 v[76:77], v[76:77], s[54:55], v[144:145]\n"
"v_pk_fma_f32 v[78:79], v[78:79], s[54:55], v[144:145]\n"
"v_pk_mul_f32 v[64:65], v[192:193], v[64:65]\n"
"v_pk_mul_f32 v[66:67], v[194:195], v[66:67]\n"
"v_pk_mul_f32 v[68:69], v[208:209], v[68:69]\n"
"v_pk_mul_f32 v[70:71], v[210:211], v[70:71]\n"
"v_pk_mul_f32 v[72:73], v[224:225], v[72:73]\n"
"v_pk_mul_f32 v[74:75], v[226:227], v[74:75]\n"
"v_pk_mul_f32 v[76:77], v[240:241], v[76:77]\n"
"v_pk_mul_f32 v[78:79], v[242:243], v[78:79]\n"
"v_exp_f32 v64, v64\n"
"v_exp_f32 v65, v65\n"
"v_exp_f32 v66, v66\n"
"v_exp_f32 v67, v67\n"
"v_exp_f32 v68, v68\n"
"v_exp_f32 v69, v69\n"
"v_exp_f32 v70, v70\n"
"v_exp_f32 v71, v71\n"
"v_exp_f32 v72, v72\n"
"v_exp_f32 v73, v73\n"
"v_exp_f32 v74, v74\n"
"v_exp_f32 v75, v75\n"
"v_exp_f32 v76, v76\n"
"v_exp_f32 v77, v77\n"
"v_exp_f32 v78, v78\n"
"v_exp_f32 v79, v79\n"
"v_pk_add_f32 v[64:65], v[64:65], v[146:147]\n"
"v_pk_add_f32 v[66:67], v[66:67], v[146:147]\n"
"v_pk_add_f32 v[68:69], v[68:69], v[146:147]\n"
"v_pk_add_f32 v[70:71], v[70:71], v[146:147]\n"
"v_pk_add_f32 v[72:73], v[72:73], v[146:147]\n"
"v_pk_add_f32 v[74:75], v[74:75], v[146:147]\n"
"v_pk_add_f32 v[76:77], v[76:77], v[146:147]\n"
"v_pk_add_f32 v[78:79], v[78:79], v[146:147]\n"
"v_rcp_f32 v64, v64\n"
"v_rcp_f32 v65, v65\n"
"v_rcp_f32 v66, v66\n"
"v_rcp_f32 v67, v67\n"
"v_rcp_f32 v68, v68\n"
"v_rcp_f32 v69, v69\n"
"v_rcp_f32 v70, v70\n"
"v_rcp_f32 v71, v71\n"
"v_rcp_f32 v72, v72\n"
"v_rcp_f32 v73, v73\n"
"v_rcp_f32 v74, v74\n"
"v_rcp_f32 v75, v75\n"
"v_rcp_f32 v76, v76\n"
"v_rcp_f32 v77, v77\n"
"v_rcp_f32 v78, v78\n"
"v_rcp_f32 v79, v79\n"
"v_pk_mul_f32 v[192:193], v[192:193], v[64:65]\n"
"v_pk_mul_f32 v[194:195], v[194:195], v[66:67]\n"
"v_pk_mul_f32 v[208:209], v[208:209], v[68:69]\n"
"v_pk_mul_f32 v[210:211], v[210:211], v[70:71]\n"
"v_pk_mul_f32 v[224:225], v[224:225], v[72:73]\n"
"v_pk_mul_f32 v[226:227], v[226:227], v[74:75]\n"
"v_pk_mul_f32 v[240:241], v[240:241], v[76:77]\n"
"v_pk_mul_f32 v[242:243], v[242:243], v[78:79]\n"
"v_cvt_pk_bf16_f32 v152, v192, v193\n"
"v_cvt_pk_bf16_f32 v153, v194, v195\n"
"v_cvt_pk_bf16_f32 v154, v208, v209\n"
"v_cvt_pk_bf16_f32 v155, v210, v211\n"
"v_cvt_pk_bf16_f32 v156, v224, v225\n"
"v_cvt_pk_bf16_f32 v157, v226, v227\n"
"v_cvt_pk_bf16_f32 v158, v240, v241\n"
"v_cvt_pk_bf16_f32 v159, v242, v243\n"
"s_nop 1\n"
"v_permlane16_swap_b32 v152, v154\n"
"v_permlane16_swap_b32 v153, v155\n"
"v_permlane16_swap_b32 v156, v158\n"
"v_permlane16_swap_b32 v157, v159\n"
"s_mov_b32 s50, 0\n"
"buffer_store_dwordx4 v[152:155], v148, s[40:43], s50 offen sc1\n"
"s_mov_b32 s50, 64\n"
"buffer_store_dwordx4 v[156:159], v148, s[40:43], s50 offen sc1\n"
"v_pk_add_f32 v[196:197], v[196:197], v[128:129]\n"
"v_pk_add_f32 v[198:199], v[198:199], v[130:131]\n"
"v_pk_add_f32 v[212:213], v[212:213], v[132:133]\n"
"v_pk_add_f32 v[214:215], v[214:215], v[134:135]\n"
"v_pk_add_f32 v[228:229], v[228:229], v[136:137]\n"
"v_pk_add_f32 v[230:231], v[230:231], v[138:139]\n"
"v_pk_add_f32 v[244:245], v[244:245], v[140:141]\n"
"v_pk_add_f32 v[246:247], v[246:247], v[142:143]\n"
"v_pk_mul_f32 v[64:65], v[196:197], v[196:197]\n"
"v_pk_mul_f32 v[66:67], v[198:199], v[198:199]\n"
"v_pk_mul_f32 v[68:69], v[212:213], v[212:213]\n"
"v_pk_mul_f32 v[70:71], v[214:215], v[214:215]\n"
"v_pk_mul_f32 v[72:73], v[228:229], v[228:229]\n"
"v_pk_mul_f32 v[74:75], v[230:231], v[230:231]\n"
"v_pk_mul_f32 v[76:77], v[244:245], v[244:245]\n"
"v_pk_mul_f32 v[78:79], v[246:247], v[246:247]\n"
"v_pk_fma_f32 v[64:65], v[64:65], s[54:55], v[144:145]\n"
"v_pk_fma_f32 v[66:67], v[66:67], s[54:55], v[144:145]\n"
"v_pk_fma_f32 v[68:69], v[68:69], s[54:55], v[144:145]\n"
"v_pk_fma_f32 v[70:71], v[70:71], s[54:55], v[144:145]\n"
"v_pk_fma_f32 v[72:73], v[72:73], s[54:55], v[144:145]\n"
"v_pk_fma_f32 v[74:75], v[74:75], s[54:55], v[144:145]\n"
"v_pk_fma_f32 v[76:77], v[76:77], s[54:55], v[144:145]\n"
"v_pk_fma_f32 v[78:79], v[78:79], s[54:55], v[144:145]\n"
"v_pk_mul_f32 v[64:65], v[196:197], v[64:65]\n"
"v_pk_mul_f32 v[66:67], v[198:199], v[66:67]\n"
"v_pk_mul_f32 v[68:69], v[212:213], v[68:69]\n"
"v_pk_mul_f32 v[70:71], v[214:215], v[70:71]\n"
"v_pk_mul_f32 v[72:73], v[228:229], v[72:73]\n"
"v_pk_mul_f32 v[74:75], v[230:231], v[74:75]\n"
"v_pk_mul_f32 v[76:77], v[244:245], v[76:77]\n"
"v_pk_mul_f32 v[78:79], v[246:247], v[78:79]\n"
"v_exp_f32 v64, v64\n"
"v_exp_f32 v65, v65\n"
"v_exp_f32 v66, v66\n"
"v_exp_f32 v67, v67\n"
"v_exp_f32 v68, v68\n"
"v_exp_f32 v69, v69\n"
"v_exp_f32 v70, v70\n"
"v_exp_f32 v71, v71\n"
"v_exp_f32 v72, v72\n"
"v_exp_f32 v73, v73\n"
"v_exp_f32 v74, v74\n"
"v_exp_f32 v75, v75\n"
"v_exp_f32 v76, v76\n"
"v_exp_f32 v77, v77\n"
"v_exp_f32 v78, v78\n"
"v_exp_f32 v79, v79\n"
"v_pk_add_f32 v[64:65], v[64:65], v[146:147]\n"
"v_pk_add_f32 v[66:67], v[66:67], v[146:147]\n"
"v_pk_add_f32 v[68:69], v[68:69], v[146:147]\n"
"v_pk_add_f32 v[70:71], v[70:71], v[146:147]\n"
"v_pk_add_f32 v[72:73], v[72:73], v[146:147]\n"
"v_pk_add_f32 v[74:75], v[74:75], v[146:147]\n"
"v_pk_add_f32 v[76:77], v[76:77], v[146:147]\n"
"v_pk_add_f32 v[78:79], v[78:79], v[146:147]\n"
"v_rcp_f32 v64, v64\n"
"v_rcp_f32 v65, v65\n"
"v_rcp_f32 v66, v66\n"
"v_rcp_f32 v67, v67\n"
"v_rcp_f32 v68, v68\n"
"v_rcp_f32 v69, v69\n"
"v_rcp_f32 v70, v70\n"
"v_rcp_f32 v71, v71\n"
"v_rcp_f32 v72, v72\n"
"v_rcp_f32 v73, v73\n"
"v_rcp_f32 v74, v74\n"
"v_rcp_f32 v75, v75\n"
"v_rcp_f32 v76, v76\n"
"v_rcp_f32 v77, v77\n"
"v_rcp_f32 v78, v78\n"
"v_rcp_f32 v79, v79\n"
"v_pk_mul_f32 v[196:197], v[196:197], v[64:65]\n"
"v_pk_mul_f32 v[198:199], v[198:199], v[66:67]\n"
"v_pk_mul_f32 v[212:213], v[212:213], v[68:69]\n"
"v_pk_mul_f32 v[214:215], v[214:215], v[70:71]\n"
"v_pk_mul_f32 v[228:229], v[228:229], v[72:73]\n"
"v_pk_mul_f32 v[230:231], v[230:231], v[74:75]\n"
"v_pk_mul_f32 v[244:245], v[244:245], v[76:77]\n"
"v_pk_mul_f32 v[246:247], v[246:247], v[78:79]\n"
"v_cvt_pk_bf16_f32 v160, v196, v197\n"
"v_cvt_pk_bf16_f32 v161, v198, v199\n"
"v_cvt_pk_bf16_f32 v162, v212, v213\n"
"v_cvt_pk_bf16_f32 v163, v214, v215\n"
"v_cvt_pk_bf16_f32 v164, v228, v229\n"
"v_cvt_pk_bf16_f32 v165, v230, v231\n"
"v_cvt_pk_bf16_f32 v166, v244, v245\n"
"v_cvt_pk_bf16_f32 v167, v246, v247\n"
"s_nop 1\n"
"v_permlane16_swap_b32 v160, v162\n"
"v_permlane16_swap_b32 v161, v163\n"
"v_permlane16_swap_b32 v164, v166\n"
"v_permlane16_swap_b32 v165, v167\n"
"s_mov_b32 s50, 114688\n"
"buffer_store_dwordx4 v[160:163], v148, s[40:43], s50 offen sc1\n"
"s_mov_b32 s50, 114752\n"
"buffer_store_dwordx4 v[164:167], v148, s[40:43], s50 offen sc1\n"
"v_pk_add_f32 v[200:201], v[200:201], v[128:129]\n"
"v_pk_add_f32 v[202:203], v[202:203], v[130:131]\n"
"v_pk_add_f32 v[216:217], v[216:217], v[132:133]\n"
"v_pk_add_f32 v[218:219], v[218:219], v[134:135]\n"
"v_pk_add_f32 v[232:233], v[232:233], v[136:137]\n"
"v_pk_add_f32 v[234:235], v[234:235], v[138:139]\n"
"v_pk_add_f32 v[248:249], v[248:249], v[140:141]\n"
"v_pk_add_f32 v[250:251], v[250:251], v[142:143]\n"
"v_pk_mul_f32 v[64:65], v[200:201], v[200:201]\n"
"v_pk_mul_f32 v[66:67], v[202:203], v[202:203]\n"
"v_pk_mul_f32 v[68:69], v[216:217], v[216:217]\n"
"v_pk_mul_f32 v[70:71], v[218:219], v[218:219]\n"
"v_pk_mul_f32 v[72:73], v[232:233], v[232:233]\n"
"v_pk_mul_f32 v[74:75], v[234:235], v[234:235]\n"
"v_pk_mul_f32 v[76:77], v[248:249], v[248:249]\n"
"v_pk_mul_f32 v[78:79], v[250:251], v[250:251]\n"
"v_pk_fma_f32 v[64:65], v[64:65], s[54:55], v[144:145]\n"
"v_pk_fma_f32 v[66:67], v[66:67], s[54:55], v[144:145]\n"
"v_pk_fma_f32 v[68:69], v[68:69], s[54:55], v[144:145]\n"
"v_pk_fma_f32 v[70:71], v[70:71], s[54:55], v[144:145]\n"
"v_pk_fma_f32 v[72:73], v[72:73], s[54:55], v[144:145]\n"
"v_pk_fma_f32 v[74:75], v[74:75], s[54:55], v[144:145]\n"
"v_pk_fma_f32 v[76:77], v[76:77], s[54:55], v[144:145]\n"
"v_pk_fma_f32 v[78:79], v[78:79], s[54:55], v[144:145]\n"
"v_pk_mul_f32 v[64:65], v[200:201], v[64:65]\n"
"v_pk_mul_f32 v[66:67], v[202:203], v[66:67]\n"
"v_pk_mul_f32 v[68:69], v[216:217], v[68:69]\n"
"v_pk_mul_f32 v[70:71], v[218:219], v[70:71]\n"
"v_pk_mul_f32 v[72:73], v[232:233], v[72:73]\n"
"v_pk_mul_f32 v[74:75], v[234:235], v[74:75]\n"
"v_pk_mul_f32 v[76:77], v[248:249], v[76:77]\n"
"v_pk_mul_f32 v[78:79], v[250:251], v[78:79]\n"
"v_exp_f32 v64, v64\n"
"v_exp_f32 v65, v65\n"
"v_exp_f32 v66, v66\n"
"v_exp_f32 v67, v67\n"
"v_exp_f32 v68, v68\n"
"v_exp_f32 v69, v69\n"
"v_exp_f32 v70, v70\n"
"v_exp_f32 v71, v71\n"
"v_exp_f32 v72, v72\n"
"v_exp_f32 v73, v73\n"
"v_exp_f32 v74, v74\n"
"v_exp_f32 v75, v75\n"
"v_exp_f32 v76, v76\n"
"v_exp_f32 v77, v77\n"
"v_exp_f32 v78, v78\n"
"v_exp_f32 v79, v79\n"
"v_pk_add_f32 v[64:65], v[64:65], v[146:147]\n"
"v_pk_add_f32 v[66:67], v[66:67], v[146:147]\n"
"v_pk_add_f32 v[68:69], v[68:69], v[146:147]\n"
"v_pk_add_f32 v[70:71], v[70:71], v[146:147]\n"
"v_pk_add_f32 v[72:73], v[72:73], v[146:147]\n"
"v_pk_add_f32 v[74:75], v[74:75], v[146:147]\n"
"v_pk_add_f32 v[76:77], v[76:77], v[146:147]\n"
"v_pk_add_f32 v[78:79], v[78:79], v[146:147]\n"
"v_rcp_f32 v64, v64\n"
"v_rcp_f32 v65, v65\n"
"v_rcp_f32 v66, v66\n"
"v_rcp_f32 v67, v67\n"
"v_rcp_f32 v68, v68\n"
"v_rcp_f32 v69, v69\n"
"v_rcp_f32 v70, v70\n"
"v_rcp_f32 v71, v71\n"
"v_rcp_f32 v72, v72\n"
"v_rcp_f32 v73, v73\n"
"v_rcp_f32 v74, v74\n"
"v_rcp_f32 v75, v75\n"
"v_rcp_f32 v76, v76\n"
"v_rcp_f32 v77, v77\n"
"v_rcp_f32 v78, v78\n"
"v_rcp_f32 v79, v79\n"
"v_pk_mul_f32 v[200:201], v[200:201], v[64:65]\n"
"v_pk_mul_f32 v[202:203], v[202:203], v[66:67]\n"
"v_pk_mul_f32 v[216:217], v[216:217], v[68:69]\n"
"v_pk_mul_f32 v[218:219], v[218:219], v[70:71]\n"
"v_pk_mul_f32 v[232:233], v[232:233], v[72:73]\n"
"v_pk_mul_f32 v[234:235], v[234:235], v[74:75]\n"
"v_pk_mul_f32 v[248:249], v[248:249], v[76:77]\n"
"v_pk_mul_f32 v[250:251], v[250:251], v[78:79]\n"
"v_cvt_pk_bf16_f32 v168, v200, v201\n"
"v_cvt_pk_bf16_f32 v169, v202, v203\n"
"v_cvt_pk_bf16_f32 v170, v216, v217\n"
"v_cvt_pk_bf16_f32 v171, v218, v219\n"
"v_cvt_pk_bf16_f32 v172, v232, v233\n"
"v_cvt_pk_bf16_f32 v173, v234, v235\n"
"v_cvt_pk_bf16_f32 v174, v248, v249\n"
"v_cvt_pk_bf16_f32 v175, v250, v251\n"
"s_nop 1\n"
"v_permlane16_swap_b32 v168, v170\n"
"v_permlane16_swap_b32 v169, v171\n"
"v_permlane16_swap_b32 v172, v174\n"
"v_permlane16_swap_b32 v173, v175\n"
"s_mov_b32 s50, 229376\n"
"buffer_store_dwordx4 v[168:171], v148, s[40:43], s50 offen sc1\n"
"s_mov_b32 s50, 229440\n"
"buffer_store_dwordx4 v[172:175], v148, s[40:43], s50 offen sc1\n"
"v_pk_add_f32 v[204:205], v[204:205], v[128:129]\n"
"v_pk_add_f32 v[206:207], v[206:207], v[130:131]\n"
"v_pk_add_f32 v[220:221], v[220:221], v[132:133]\n"
"v_pk_add_f32 v[222:223], v[222:223], v[134:135]\n"
"v_pk_add_f32 v[236:237], v[236:237], v[136:137]\n"
"v_pk_add_f32 v[238:239], v[238:239], v[138:139]\n"
"v_pk_add_f32 v[252:253], v[252:253], v[140:141]\n"
"v_pk_add_f32 v[254:255], v[254:255], v[142:143]\n"
"v_pk_mul_f32 v[64:65], v[204:205], v[204:205]\n"
"v_pk_mul_f32 v[66:67], v[206:207], v[206:207]\n"
"v_pk_mul_f32 v[68:69], v[220:221], v[220:221]\n"
"v_pk_mul_f32 v[70:71], v[222:223], v[222:223]\n"
"v_pk_mul_f32 v[72:73], v[236:237], v[236:237]\n"
"v_pk_mul_f32 v[74:75], v[238:239], v[238:239]\n"
"v_pk_mul_f32 v[76:77], v[252:253], v[252:253]\n"
"v_pk_mul_f32 v[78:79], v[254:255], v[254:255]\n"
"v_pk_fma_f32 v[64:65], v[64:65], s[54:55], v[144:145]\n"
"v_pk_fma_f32 v[66:67], v[66:67], s[54:55], v[144:145]\n"
"v_pk_fma_f32 v[68:69], v[68:69], s[54:55], v[144:145]\n"
"v_pk_fma_f32 v[70:71], v[70:71], s[54:55], v[144:145]\n"
"v_pk_fma_f32 v[72:73], v[72:73], s[54:55], v[144:145]\n"
"v_pk_fma_f32 v[74:75], v[74:75], s[54:55], v[144:145]\n"
"v_pk_fma_f32 v[76:77], v[76:77], s[54:55], v[144:145]\n"
"v_pk_fma_f32 v[78:79], v[78:79], s[54:55], v[144:145]\n"
"v_pk_mul_f32 v[64:65], v[204:205], v[64:65]\n"
"v_pk_mul_f32 v[66:67], v[206:207], v[66:67]\n"
"v_pk_mul_f32 v[68:69], v[220:221], v[68:69]\n"
"v_pk_mul_f32 v[70:71], v[222:223], v[70:71]\n"
"v_pk_mul_f32 v[72:73], v[236:237], v[72:73]\n"
"v_pk_mul_f32 v[74:75], v[238:239], v[74:75]\n"
"v_pk_mul_f32 v[76:77], v[252:253], v[76:77]\n"
"v_pk_mul_f32 v[78:79], v[254:255], v[78:79]\n"
"v_exp_f32 v64, v64\n"
"v_exp_f32 v65, v65\n"
"v_exp_f32 v66, v66\n"
"v_exp_f32 v67, v67\n"
"v_exp_f32 v68, v68\n"
"v_exp_f32 v69, v69\n"
"v_exp_f32 v70, v70\n"
"v_exp_f32 v71, v71\n"
"v_exp_f32 v72, v72\n"
"v_exp_f32 v73, v73\n"
"v_exp_f32 v74, v74\n"
"v_exp_f32 v75, v75\n"
"v_exp_f32 v76, v76\n"
"v_exp_f32 v77, v77\n"
"v_exp_f32 v78, v78\n"
"v_exp_f32 v79, v79\n"
"v_pk_add_f32 v[64:65], v[64:65], v[146:147]\n"
"v_pk_add_f32 v[66:67], v[66:67], v[146:147]\n"
"v_pk_add_f32 v[68:69], v[68:69], v[146:147]\n"
"v_pk_add_f32 v[70:71], v[70:71], v[146:147]\n"
"v_pk_add_f32 v[72:73], v[72:73], v[146:147]\n"
"v_pk_add_f32 v[74:75], v[74:75], v[146:147]\n"
"v_pk_add_f32 v[76:77], v[76:77], v[146:147]\n"
"v_pk_add_f32 v[78:79], v[78:79], v[146:147]\n"
"v_rcp_f32 v64, v64\n"
"v_rcp_f32 v65, v65\n"
"v_rcp_f32 v66, v66\n"
"v_rcp_f32 v67, v67\n"
"v_rcp_f32 v68, v68\n"
"v_rcp_f32 v69, v69\n"
"v_rcp_f32 v70, v70\n"
"v_rcp_f32 v71, v71\n"
"v_rcp_f32 v72, v72\n"
"v_rcp_f32 v73, v73\n"
"v_rcp_f32 v74, v74\n"
"v_rcp_f32 v75, v75\n"
"v_rcp_f32 v76, v76\n"
"v_rcp_f32 v77, v77\n"
"v_rcp_f32 v78, v78\n"
"v_rcp_f32 v79, v79\n"
"v_pk_mul_f32 v[204:205], v[204:205], v[64:65]\n"
"v_pk_mul_f32 v[206:207], v[206:207], v[66:67]\n"
"v_pk_mul_f32 v[220:221], v[220:221], v[68:69]\n"
"v_pk_mul_f32 v[222:223], v[222:223], v[70:71]\n"
"v_pk_mul_f32 v[236:237], v[236:237], v[72:73]\n"
"v_pk_mul_f32 v[238:239], v[238:239], v[74:75]\n"
"v_pk_mul_f32 v[252:253], v[252:253], v[76:77]\n"
"v_pk_mul_f32 v[254:255], v[254:255], v[78:79]\n"
"v_cvt_pk_bf16_f32 v176, v204, v205\n"
"v_cvt_pk_bf16_f32 v177, v206, v207\n"
"v_cvt_pk_bf16_f32 v178, v220, v221\n"
"v_cvt_pk_bf16_f32 v179, v222, v223\n"
"v_cvt_pk_bf16_f32 v180, v236, v237\n"
"v_cvt_pk_bf16_f32 v181, v238, v239\n"
"v_cvt_pk_bf16_f32 v182, v252, v253\n"
"v_cvt_pk_bf16_f32 v183, v254, v255\n"
"s_nop 1\n"
"v_permlane16_swap_b32 v176, v178\n"
"v_permlane16_swap_b32 v177, v179\n"
"v_permlane16_swap_b32 v180, v182\n"
"v_permlane16_swap_b32 v181, v183\n"
"s_mov_b32 s50, 344064\n"
"buffer_store_dwordx4 v[176:179], v148, s[40:43], s50 offen sc1\n"
"s_mov_b32 s50, 344128\n"
"buffer_store_dwordx4 v[180:183], v148, s[40:43], s50 offen sc1\n"
"s_waitcnt vmcnt(14) lgkmcnt(0)\n"
"s_barrier\n"
"v_lshlrev_b32 v2, 4, v1\n"
"s_xor_b32 s50, s21, 4\n"
"s_lshl_b32 s50, s50, 12\n"
"s_add_u32 s50, s50, 131072\n"
"v_add_u32 v2, s50, v2\n"
"ds_read_b128 v[40:43], v2 offset:0\n"
"ds_read_b128 v[44:47], v2 offset:1024\n"
"ds_read_b128 v[56:59], v2 offset:2048\n"
"ds_read_b128 v[60:63], v2 offset:3072\n"
"s_branch P1_attn\n"
"P1_grpB:\n"
"s_barrier\n"
"s_add_u32 s45, s45, s98\n"
"s_add_u32 m0, s49, 32768\n"
"ds_read_b64_tr_b16 v[64:65], v26 offset:16384\n"
"buffer_load_dwordx4 v20, s[28:31], s45 offen lds\n"
"ds_read_b64_tr_b16 v[66:67], v26 offset:16896\n"
"ds_read_b64_tr_b16 v[68:69], v27 offset:16384\n"
"ds_read_b64_tr_b16 v[70:71], v27 offset:16896\n"
"ds_read_b64_tr_b16 v[72:73], v28 offset:16384\n"
"s_add_u32 m0, s49, 33792\n"
"ds_read_b64_tr_b16 v[74:75], v28 offset:16896\n"
"buffer_load_dwordx4 v21, s[28:31], s45 offen lds\n"
"ds_read_b64_tr_b16 v[80:81], v26 offset:20480\n"
"ds_read_b64_tr_b16 v[82:83], v26 offset:20992\n"
"ds_read_b64_tr_b16 v[84:85], v27 offset:20480\n"
"ds_read_b64_tr_b16 v[86:87], v27 offset:20992\n"
"s_add_u32 m0, s49, 34816\n"
"ds_read_b64_tr_b16 v[88:89], v28 offset:20480\n"
"buffer_load_dwordx4 v22, s[28:31], s45 offen lds\n"
"ds_read_b64_tr_b16 v[90:91], v28 offset:20992\n"
"ds_read_b128 v[32:35], v24 offset:0\n"
"ds_read_b128 v[36:39], v24 offset:2048\n"
"ds_read_b128 v[40:43], v24 offset:4096\n"
"s_add_u32 m0, s49, 35840\n"
"ds_read_b128 v[44:47], v24 offset:6144\n"
"buffer_load_dwordx4 v23, s[28:31], s45 offen lds\n"
"ds_read_b128 v[48:51], v25 offset:0\n"
"ds_read_b128 v[52:55], v25 offset:2048\n"
"ds_read_b128 v[56:59], v25 offset:4096\n"
"ds_read_b128 v[60:63], v25 offset:6144\n"
"s_waitcnt lgkmcnt(0)\n"
"s_barrier\n"
"v_mfma_f32_16x16x32_bf16 v[192:195], v[64:67], v[32:35], 0\n"
"v_mfma_f32_16x16x32_bf16 v[192:195], v[80:83], v[48:51], v[192:195]\n"
"v_mfma_f32_16x16x32_bf16 v[196:199], v[80:83], v[52:55], 0\n"
"v_mfma_f32_16x16x32_bf16 v[196:199], v[64:67], v[36:39], v[196:199]\n"
"v_mfma_f32_16x16x32_bf16 v[200:203], v[64:67], v[40:43], 0\n"
"v_mfma_f32_16x16x32_bf16 v[200:203], v[80:83], v[56:59], v[200:203]\n"
"v_mfma_f32_16x16x32_bf16 v[204:207], v[80:83], v[60:63], 0\n"
"v_mfma_f32_16x16x32_bf16 v[204:207], v[64:67], v[44:47], v[204:207]\n"
"v_mfma_f32_16x16x32_bf16 v[220:223], v[68:71], v[44:47], 0\n"
"v_mfma_f32_16x16x32_bf16 v[220:223], v[84:87], v[60:63], v[220:223]\n"
"v_mfma_f32_16x16x32_bf16 v[216:219], v[84:87], v[56:59], 0\n"
"v_mfma_f32_16x16x32_bf16 v[216:219], v[68:71], v[40:43], v[216:219]\n"
"v_mfma_f32_16x16x32_bf16 v[212:215], v[68:71], v[36:39], 0\n"
"v_mfma_f32_16x16x32_bf16 v[212:215], v[84:87], v[52:55], v[212:215]\n"
"v_mfma_f32_16x16x32_bf16 v[208:211], v[84:87], v[48:51], 0\n"
"v_mfma_f32_16x16x32_bf16 v[208:211], v[68:71], v[32:35], v[208:211]\n"
"v_mfma_f32_16x16x32_bf16 v[224:227], v[72:75], v[32:35], 0\n"
"v_mfma_f32_16x16x32_bf16 v[224:227], v[88:91], v[48:51], v[224:227]\n"
"v_mfma_f32_16x16x32_bf16 v[228:231], v[88:91], v[52:55], 0\n"
"v_mfma_f32_16x16x32_bf16 v[228:231], v[72:75], v[36:39], v[228:231]\n"
"v_mfma_f32_16x16x32_bf16 v[232:235], v[72:75], v[40:43], 0\n"
"v_mfma_f32_16x16x32_bf16 v[232:235], v[88:91], v[56:59], v[232:235]\n"
"v_mfma_f32_16x16x32_bf16 v[236:239], v[88:91], v[60:63], 0\n"
"v_mfma_f32_16x16x32_bf16 v[236:239], v[72:75], v[44:47], v[236:239]\n"
"s_barrier\n"
"s_add_u32 s44, s44, 128\n"
"s_add_u32 m0, s46, 0\n"
"ds_read_b64_tr_b16 v[64:65], v26 offset:0\n"
"buffer_load_dwordx4 v16, s[24:27], s44 offen lds\n"
"ds_read_b64_tr_b16 v[66:67], v26 offset:512\n"
"ds_read_b64_tr_b16 v[68:69], v27 offset:0\n"
"ds_read_b64_tr_b16 v[70:71], v27 offset:512\n"
"s_add_u32 m0, s46, 1024\n"
"ds_read_b64_tr_b16 v[72:73], v28 offset:0\n"
"buffer_load_dwordx4 v17, s[24:27], s44 offen lds\n"
"ds_read_b64_tr_b16 v[74:75], v28 offset:512\n"
"ds_read_b64_tr_b16 v[76:77], v29 offset:0\n"
"ds_read_b64_tr_b16 v[78:79], v29 offset:512\n"
"s_add_u32 m0, s46, 2048\n"
"ds_read_b64_tr_b16 v[80:81], v26 offset:4096\n"
"buffer_load_dwordx4 v18, s[24:27], s44 offen lds\n"
"ds_read_b64_tr_b16 v[82:83], v26 offset:4608\n"
"ds_read_b64_tr_b16 v[84:85], v27 offset:4096\n"
"ds_read_b64_tr_b16 v[86:87], v27 offset:4608\n"
"s_add_u32 m0, s46, 3072\n"
"ds_read_b64_tr_b16 v[88:89], v28 offset:4096\n"
"buffer_load_dwordx4 v19, s[24:27], s44 offen lds\n"
"ds_read_b64_tr_b16 v[90:91], v28 offset:4608\n"
"ds_read_b64_tr_b16 v[92:93], v29 offset:4096\n"
"ds_read_b64_tr_b16 v[94:95], v29 offset:4608\n"
"s_waitcnt vmcnt(8) lgkmcnt(0)\n"
"s_barrier\n"
"v_mfma_f32_16x16x32_bf16 v[128:131], v[64:67], v[32:35], 0\n"
"v_mfma_f32_16x16x32_bf16 v[128:131], v[80:83], v[48:51], v[128:131]\n"
"v_mfma_f32_16x16x32_bf16 v[132:135], v[80:83], v[52:55], 0\n"
"v_xor_b32 v24, 0x10000, v24\n"
"v_mfma_f32_16x16x32_bf16 v[132:135], v[64:67], v[36:39], v[132:135]\n"
"v_xor_b32 v25, 0x10000, v25\n"
"v_mfma_f32_16x16x32_bf16 v[136:139], v[64:67], v[40:43], 0\n"
"v_xor_b32 v26, 0x10000, v26\n"
"v_mfma_f32_16x16x32_bf16 v[136:139], v[80:83], v[56:59], v[136:139]\n"
"v_xor_b32 v27, 0x10000, v27\n"
"v_mfma_f32_16x16x32_bf16 v[140:143], v[80:83], v[60:63], 0\n"
"v_xor_b32 v28, 0x10000, v28\n"
"v_mfma_f32_16x16x32_bf16 v[140:143], v[64:67], v[44:47], v[140:143]\n"
"v_xor_b32 v29, 0x10000, v29\n"
"v_mfma_f32_16x16x32_bf16 v[156:159], v[68:71], v[44:47], 0\n"
"s_xor_b32 s46, s46, 0x10000\n"
"s_xor_b32 s49, s49, 0x10000\n"
"v_mfma_f32_16x16x32_bf16 v[156:159], v[84:87], v[60:63], v[156:159]\n"
"v_mfma_f32_16x16x32_bf16 v[152:155], v[84:87], v[56:59], 0\n"
"v_mfma_f32_16x16x32_bf16 v[152:155], v[68:71], v[40:43], v[152:155]\n"
"v_mfma_f32_16x16x32_bf16 v[148:151], v[68:71], v[36:39], 0\n"
"v_mfma_f32_16x16x32_bf16 v[148:151], v[84:87], v[52:55], v[148:151]\n"
"v_mfma_f32_16x16x32_bf16 v[144:147], v[84:87], v[48:51], 0\n"
"v_mfma_f32_16x16x32_bf16 v[144:147], v[68:71], v[32:35], v[144:147]\n"
"v_mfma_f32_16x16x32_bf16 v[160:163], v[72:75], v[32:35], 0\n"
"v_mfma_f32_16x16x32_bf16 v[160:163], v[88:91], v[48:51], v[160:163]\n"
"v_mfma_f32_16x16x32_bf16 v[164:167], v[88:91], v[52:55], 0\n"
"v_mfma_f32_16x16x32_bf16 v[164:167], v[72:75], v[36:39], v[164:167]\n"
"v_mfma_f32_16x16x32_bf16 v[168:171], v[72:75], v[40:43], 0\n"
"v_mfma_f32_16x16x32_bf16 v[168:171], v[88:91], v[56:59], v[168:171]\n"
"v_mfma_f32_16x16x32_bf16 v[172:175], v[88:91], v[60:63], 0\n"
"v_mfma_f32_16x16x32_bf16 v[172:175], v[72:75], v[44:47], v[172:175]\n"
"v_mfma_f32_16x16x32_bf16 v[188:191], v[76:79], v[44:47], 0\n"
"v_mfma_f32_16x16x32_bf16 v[188:191], v[92:95], v[60:63], v[188:191]\n"
"v_mfma_f32_16x16x32_bf16 v[184:187], v[92:95], v[56:59], 0\n"
"v_mfma_f32_16x16x32_bf16 v[184:187], v[76:79], v[40:43], v[184:187]\n"
"v_mfma_f32_16x16x32_bf16 v[180:183], v[76:79], v[36:39], 0\n"
"v_mfma_f32_16x16x32_bf16 v[180:183], v[92:95], v[52:55], v[180:183]\n"
"v_mfma_f32_16x16x32_bf16 v[176:179], v[92:95], v[48:51], 0\n"
"v_mfma_f32_16x16x32_bf16 v[176:179], v[76:79], v[32:35], v[176:179]\n"
"s_waitcnt vmcnt(4)\n"
"s_barrier\n"
"s_mov_b32 s47, 13\n"
"P1_loopB:\n"
"s_add_u32 s45, s45, s98\n"
"s_add_u32 m0, s49, 32768\n"
"ds_read_b64_tr_b16 v[64:65], v26 offset:16384\n"
"buffer_load_dwordx4 v20, s[28:31], s45 offen lds\n"
"ds_read_b64_tr_b16 v[66:67], v26 offset:16896\n"
"ds_read_b64_tr_b16 v[68:69], v27 offset:16384\n"
"ds_read_b64_tr_b16 v[70:71], v27 offset:16896\n"
"ds_read_b64_tr_b16 v[72:73], v28 offset:16384\n"
"s_add_u32 m0, s49, 33792\n"
"ds_read_b64_tr_b16 v[74:75], v28 offset:16896\n"
"buffer_load_dwordx4 v21, s[28:31], s45 offen lds\n"
"ds_read_b64_tr_b16 v[80:81], v26 offset:20480\n"
"ds_read_b64_tr_b16 v[82:83], v26 offset:20992\n"
"ds_read_b64_tr_b16 v[84:85], v27 offset:20480\n"
"ds_read_b64_tr_b16 v[86:87], v27 offset:20992\n"
"s_add_u32 m0, s49, 34816\n"
"ds_read_b64_tr_b16 v[88:89], v28 offset:20480\n"
"buffer_load_dwordx4 v22, s[28:31], s45 offen lds\n"
"ds_read_b64_tr_b16 v[90:91], v28 offset:20992\n"
"ds_read_b128 v[32:35], v24 offset:0\n"
"ds_read_b128 v[36:39], v24 offset:2048\n"
"ds_read_b128 v[40:43], v24 offset:4096\n"
"s_add_u32 m0, s49, 35840\n"
"ds_read_b128 v[44:47], v24 offset:6144\n"
"buffer_load_dwordx4 v23, s[28:31], s45 offen lds\n"
"ds_read_b128 v[48:51], v25 offset:0\n"
"ds_read_b128 v[52:55], v25 offset:2048\n"
"ds_read_b128 v[56:59], v25 offset:4096\n"
"ds_read_b128 v[60:63], v25 offset:6144\n"
"s_waitcnt lgkmcnt(0)\n"
"s_barrier\n"
"v_mfma_f32_16x16x32_bf16 v[192:195], v[64:67], v[32:35], v[192:195]\n"
"v_mfma_f32_16x16x32_bf16 v[192:195], v[80:83], v[48:51], v[192:195]\n"
"v_mfma_f32_16x16x32_bf16 v[196:199], v[80:83], v[52:55], v[196:199]\n"
"v_mfma_f32_16x16x32_bf16 v[196:199], v[64:67], v[36:39], v[196:199]\n"
"v_mfma_f32_16x16x32_bf16 v[200:203], v[64:67], v[40:43], v[200:203]\n"
"v_mfma_f32_16x16x32_bf16 v[200:203], v[80:83], v[56:59], v[200:203]\n"
"v_mfma_f32_16x16x32_bf16 v[204:207], v[80:83], v[60:63], v[204:207]\n"
"v_mfma_f32_16x16x32_bf16 v[204:207], v[64:67], v[44:47], v[204:207]\n"
"v_mfma_f32_16x16x32_bf16 v[220:223], v[68:71], v[44:47], v[220:223]\n"
"v_mfma_f32_16x16x32_bf16 v[220:223], v[84:87], v[60:63], v[220:223]\n"
"v_mfma_f32_16x16x32_bf16 v[216:219], v[84:87], v[56:59], v[216:219]\n"
"v_mfma_f32_16x16x32_bf16 v[216:219], v[68:71], v[40:43], v[216:219]\n"
"v_mfma_f32_16x16x32_bf16 v[212:215], v[68:71], v[36:39], v[212:215]\n"
"v_mfma_f32_16x16x32_bf16 v[212:215], v[84:87], v[52:55], v[212:215]\n"
"v_mfma_f32_16x16x32_bf16 v[208:211], v[84:87], v[48:51], v[208:211]\n"
"v_mfma_f32_16x16x32_bf16 v[208:211], v[68:71], v[32:35], v[208:211]\n"
"v_mfma_f32_16x16x32_bf16 v[224:227], v[72:75], v[32:35], v[224:227]\n"
"v_mfma_f32_16x16x32_bf16 v[224:227], v[88:91], v[48:51], v[224:227]\n"
"v_mfma_f32_16x16x32_bf16 v[228:231], v[88:91], v[52:55], v[228:231]\n"
"v_mfma_f32_16x16x32_bf16 v[228:231], v[72:75], v[36:39], v[228:231]\n"
"v_mfma_f32_16x16x32_bf16 v[232:235], v[72:75], v[40:43], v[232:235]\n"
"v_mfma_f32_16x16x32_bf16 v[232:235], v[88:91], v[56:59], v[232:235]\n"
"v_mfma_f32_16x16x32_bf16 v[236:239], v[88:91], v[60:63], v[236:239]\n"
"v_mfma_f32_16x16x32_bf16 v[236:239], v[72:75], v[44:47], v[236:239]\n"
"s_barrier\n"
"s_add_u32 s44, s44, 128\n"
"s_add_u32 m0, s46, 0\n"
"ds_read_b64_tr_b16 v[64:65], v26 offset:0\n"
"buffer_load_dwordx4 v16, s[24:27], s44 offen lds\n"
"ds_read_b64_tr_b16 v[66:67], v26 offset:512\n"
"ds_read_b64_tr_b16 v[68:69], v27 offset:0\n"
"ds_read_b64_tr_b16 v[70:71], v27 offset:512\n"
"s_add_u32 m0, s46, 1024\n"
"ds_read_b64_tr_b16 v[72:73], v28 offset:0\n"
"buffer_load_dwordx4 v17, s[24:27], s44 offen lds\n"
"ds_read_b64_tr_b16 v[74:75], v28 offset:512\n"
"ds_read_b64_tr_b16 v[76:77], v29 offset:0\n"
"ds_read_b64_tr_b16 v[78:79], v29 offset:512\n"
"s_add_u32 m0, s46, 2048\n"
"ds_read_b64_tr_b16 v[80:81], v26 offset:4096\n"
"buffer_load_dwordx4 v18, s[24:27], s44 offen lds\n"
"ds_read_b64_tr_b16 v[82:83], v26 offset:4608\n"
"ds_read_b64_tr_b16 v[84:85], v27 offset:4096\n"
"ds_read_b64_tr_b16 v[86:87], v27 offset:4608\n"
"s_add_u32 m0, s46, 3072\n"
"ds_read_b64_tr_b16 v[88:89], v28 offset:4096\n"
"buffer_load_dwordx4 v19, s[24:27], s44 offen lds\n"
"ds_read_b64_tr_b16 v[90:91], v28 offset:4608\n"
"ds_read_b64_tr_b16 v[92:93], v29 offset:4096\n"
"ds_read_b64_tr_b16 v[94:95], v29 offset:4608\n"
"s_waitcnt vmcnt(8) lgkmcnt(0)\n"
"s_barrier\n"
"v_mfma_f32_16x16x32_bf16 v[128:131], v[64:67], v[32:35], v[128:131]\n"
"v_mfma_f32_16x16x32_bf16 v[128:131], v[80:83], v[48:51], v[128:131]\n"
"v_mfma_f32_16x16x32_bf16 v[132:135], v[80:83], v[52:55], v[132:135]\n"
"v_xor_b32 v24, 0x10000, v24\n"
"v_mfma_f32_16x16x32_bf16 v[132:135], v[64:67], v[36:39], v[132:135]\n"
"v_xor_b32 v25, 0x10000, v25\n"
"v_mfma_f32_16x16x32_bf16 v[136:139], v[64:67], v[40:43], v[136:139]\n"
"v_xor_b32 v26, 0x10000, v26\n"
"v_mfma_f32_16x16x32_bf16 v[136:139], v[80:83], v[56:59], v[136:139]\n"
"v_xor_b32 v27, 0x10000, v27\n"
"v_mfma_f32_16x16x32_bf16 v[140:143], v[80:83], v[60:63], v[140:143]\n"
"v_xor_b32 v28, 0x10000, v28\n"
"v_mfma_f32_16x16x32_bf16 v[140:143], v[64:67], v[44:47], v[140:143]\n"
"v_xor_b32 v29, 0x10000, v29\n"
"v_mfma_f32_16x16x32_bf16 v[156:159], v[68:71], v[44:47], v[156:159]\n"
"s_xor_b32 s46, s46, 0x10000\n"
"s_xor_b32 s49, s49, 0x10000\n"
"v_mfma_f32_16x16x32_bf16 v[156:159], v[84:87], v[60:63], v[156:159]\n"
"v_mfma_f32_16x16x32_bf16 v[152:155], v[84:87], v[56:59], v[152:155]\n"
"v_mfma_f32_16x16x32_bf16 v[152:155], v[68:71], v[40:43], v[152:155]\n"
"v_mfma_f32_16x16x32_bf16 v[148:151], v[68:71], v[36:39], v[148:151]\n"
"v_mfma_f32_16x16x32_bf16 v[148:151], v[84:87], v[52:55], v[148:151]\n"
"v_mfma_f32_16x16x32_bf16 v[144:147], v[84:87], v[48:51], v[144:147]\n"
"v_mfma_f32_16x16x32_bf16 v[144:147], v[68:71], v[32:35], v[144:147]\n"
"v_mfma_f32_16x16x32_bf16 v[160:163], v[72:75], v[32:35], v[160:163]\n"
"v_mfma_f32_16x16x32_bf16 v[160:163], v[88:91], v[48:51], v[160:163]\n"
"v_mfma_f32_16x16x32_bf16 v[164:167], v[88:91], v[52:55], v[164:167]\n"
"v_mfma_f32_16x16x32_bf16 v[164:167], v[72:75], v[36:39], v[164:167]\n"
"v_mfma_f32_16x16x32_bf16 v[168:171], v[72:75], v[40:43], v[168:171]\n"
"v_mfma_f32_16x16x32_bf16 v[168:171], v[88:91], v[56:59], v[168:171]\n"
"v_mfma_f32_16x16x32_bf16 v[172:175], v[88:91], v[60:63], v[172:175]\n"
"v_mfma_f32_16x16x32_bf16 v[172:175], v[72:75], v[44:47], v[172:175]\n"
"v_mfma_f32_16x16x32_bf16 v[188:191], v[76:79], v[44:47], v[188:191]\n"
"v_mfma_f32_16x16x32_bf16 v[188:191], v[92:95], v[60:63], v[188:191]\n"
"v_mfma_f32_16x16x32_bf16 v[184:187], v[92:95], v[56:59], v[184:187]\n"
"v_mfma_f32_16x16x32_bf16 v[184:187], v[76:79], v[40:43], v[184:187]\n"
"v_mfma_f32_16x16x32_bf16 v[180:183], v[76:79], v[36:39], v[180:183]\n"
"v_mfma_f32_16x16x32_bf16 v[180:183], v[92:95], v[52:55], v[180:183]\n"
"v_mfma_f32_16x16x32_bf16 v[176:179], v[92:95], v[48:51], v[176:179]\n"
"v_mfma_f32_16x16x32_bf16 v[176:179], v[76:79], v[32:35], v[176:179]\n"
"s_waitcnt vmcnt(4)\n"
"s_barrier\n"
"s_sub_u32 s47, s47, 1\n"
"s_cmp_lg_u32 s47, 0\n"
"s_cbranch_scc1 P1_loopB\n"
"s_add_u32 s45, s45, s98\n"
"s_add_u32 m0, s49, 32768\n"
"ds_read_b64_tr_b16 v[64:65], v26 offset:16384\n"
"buffer_load_dwordx4 v20, s[28:31], s45 offen lds\n"
"ds_read_b64_tr_b16 v[66:67], v26 offset:16896\n"
"ds_read_b64_tr_b16 v[68:69], v27 offset:16384\n"
"ds_read_b64_tr_b16 v[70:71], v27 offset:16896\n"
"ds_read_b64_tr_b16 v[72:73], v28 offset:16384\n"
"s_add_u32 m0, s49, 33792\n"
"ds_read_b64_tr_b16 v[74:75], v28 offset:16896\n"
"buffer_load_dwordx4 v21, s[28:31], s45 offen lds\n"
"ds_read_b64_tr_b16 v[80:81], v26 offset:20480\n"
"ds_read_b64_tr_b16 v[82:83], v26 offset:20992\n"
"ds_read_b64_tr_b16 v[84:85], v27 offset:20480\n"
"ds_read_b64_tr_b16 v[86:87], v27 offset:20992\n"
"s_add_u32 m0, s49, 34816\n"
"ds_read_b64_tr_b16 v[88:89], v28 offset:20480\n"
"buffer_load_dwordx4 v22, s[28:31], s45 offen lds\n"
"ds_read_b64_tr_b16 v[90:91], v28 offset:20992\n"
"ds_read_b128 v[32:35], v24 offset:0\n"
"ds_read_b128 v[36:39], v24 offset:2048\n"
"ds_read_b128 v[40:43], v24 offset:4096\n"
"s_add_u32 m0, s49, 35840\n"
"ds_read_b128 v[44:47], v24 offset:6144\n"
"buffer_load_dwordx4 v23, s[28:31], s45 offen lds\n"
"ds_read_b128 v[48:51], v25 offset:0\n"
"ds_read_b128 v[52:55], v25 offset:2048\n"
"ds_read_b128 v[56:59], v25 offset:4096\n"
"ds_read_b128 v[60:63], v25 offset:6144\n"
"s_waitcnt lgkmcnt(0)\n"
"s_barrier\n"
"v_mfma_f32_16x16x32_bf16 v[192:195], v[64:67], v[32:35], v[192:195]\n"
"v_mfma_f32_16x16x32_bf16 v[192:195], v[80:83], v[48:51], v[192:195]\n"
"v_mfma_f32_16x16x32_bf16 v[196:199], v[80:83], v[52:55], v[196:199]\n"
"v_mfma_f32_16x16x32_bf16 v[196:199], v[64:67], v[36:39], v[196:199]\n"
"v_mfma_f32_16x16x32_bf16 v[200:203], v[64:67], v[40:43], v[200:203]\n"
"v_mfma_f32_16x16x32_bf16 v[200:203], v[80:83], v[56:59], v[200:203]\n"
"v_mfma_f32_16x16x32_bf16 v[204:207], v[80:83], v[60:63], v[204:207]\n"
"v_mfma_f32_16x16x32_bf16 v[204:207], v[64:67], v[44:47], v[204:207]\n"
"v_mfma_f32_16x16x32_bf16 v[220:223], v[68:71], v[44:47], v[220:223]\n"
"v_mfma_f32_16x16x32_bf16 v[220:223], v[84:87], v[60:63], v[220:223]\n"
"v_mfma_f32_16x16x32_bf16 v[216:219], v[84:87], v[56:59], v[216:219]\n"
"v_mfma_f32_16x16x32_bf16 v[216:219], v[68:71], v[40:43], v[216:219]\n"
"v_mfma_f32_16x16x32_bf16 v[212:215], v[68:71], v[36:39], v[212:215]\n"
"v_mfma_f32_16x16x32_bf16 v[212:215], v[84:87], v[52:55], v[212:215]\n"
"v_mfma_f32_16x16x32_bf16 v[208:211], v[84:87], v[48:51], v[208:211]\n"
"v_mfma_f32_16x16x32_bf16 v[208:211], v[68:71], v[32:35], v[208:211]\n"
"v_mfma_f32_16x16x32_bf16 v[224:227], v[72:75], v[32:35], v[224:227]\n"
"v_mfma_f32_16x16x32_bf16 v[224:227], v[88:91], v[48:51], v[224:227]\n"
"v_mfma_f32_16x16x32_bf16 v[228:231], v[88:91], v[52:55], v[228:231]\n"
"v_mfma_f32_16x16x32_bf16 v[228:231], v[72:75], v[36:39], v[228:231]\n"
"v_mfma_f32_16x16x32_bf16 v[232:235], v[72:75], v[40:43], v[232:235]\n"
"v_mfma_f32_16x16x32_bf16 v[232:235], v[88:91], v[56:59], v[232:235]\n"
"v_mfma_f32_16x16x32_bf16 v[236:239], v[88:91], v[60:63], v[236:239]\n"
"v_mfma_f32_16x16x32_bf16 v[236:239], v[72:75], v[44:47], v[236:239]\n"
"s_barrier\n"
"s_mov_b32 s48, 0\n"
"s_add_u32 m0, s60, 0\n"
"ds_read_b64_tr_b16 v[64:65], v26 offset:0\n"
"buffer_load_dwordx4 v124, s[36:39], s48 offen lds\n"
"ds_read_b64_tr_b16 v[66:67], v26 offset:512\n"
"ds_read_b64_tr_b16 v[68:69], v27 offset:0\n"
"ds_read_b64_tr_b16 v[70:71], v27 offset:512\n"
"s_add_u32 m0, s60, 1024\n"
"ds_read_b64_tr_b16 v[72:73], v28 offset:0\n"
"buffer_load_dwordx4 v125, s[36:39], s48 offen lds\n"
"ds_read_b64_tr_b16 v[74:75], v28 offset:512\n"
"ds_read_b64_tr_b16 v[76:77], v29 offset:0\n"
"ds_read_b64_tr_b16 v[78:79], v29 offset:512\n"
"s_add_u32 m0, s60, 2048\n"
"ds_read_b64_tr_b16 v[80:81], v26 offset:4096\n"
"buffer_load_dwordx4 v126, s[36:39], s48 offen lds\n"
"ds_read_b64_tr_b16 v[82:83], v26 offset:4608\n"
"ds_read_b64_tr_b16 v[84:85], v27 offset:4096\n"
"ds_read_b64_tr_b16 v[86:87], v27 offset:4608\n"
"s_add_u32 m0, s60, 3072\n"
"ds_read_b64_tr_b16 v[88:89], v28 offset:4096\n"
"buffer_load_dwordx4 v127, s[36:39], s48 offen lds\n"
"ds_read_b64_tr_b16 v[90:91], v28 offset:4608\n"
"ds_read_b64_tr_b16 v[92:93], v29 offset:4096\n"
"ds_read_b64_tr_b16 v[94:95], v29 offset:4608\n"
"s_waitcnt vmcnt(8) lgkmcnt(0)\n"
"s_barrier\n"
"v_mfma_f32_16x16x32_bf16 v[128:131], v[64:67], v[32:35], v[128:131]\n"
"v_mfma_f32_16x16x32_bf16 v[128:131], v[80:83], v[48:51], v[128:131]\n"
"v_mfma_f32_16x16x32_bf16 v[132:135], v[80:83], v[52:55], v[132:135]\n"
"v_xor_b32 v24, 0x10000, v24\n"
"v_mfma_f32_16x16x32_bf16 v[132:135], v[64:67], v[36:39], v[132:135]\n"
"v_xor_b32 v25, 0x10000, v25\n"
"v_mfma_f32_16x16x32_bf16 v[136:139], v[64:67], v[40:43], v[136:139]\n"
"v_xor_b32 v26, 0x10000, v26\n"
"v_mfma_f32_16x16x32_bf16 v[136:139], v[80:83], v[56:59], v[136:139]\n"
"v_xor_b32 v27, 0x10000, v27\n"
"v_mfma_f32_16x16x32_bf16 v[140:143], v[80:83], v[60:63], v[140:143]\n"
"v_xor_b32 v28, 0x10000, v28\n"
"v_mfma_f32_16x16x32_bf16 v[140:143], v[64:67], v[44:47], v[140:143]\n"
"v_xor_b32 v29, 0x10000, v29\n"
"v_mfma_f32_16x16x32_bf16 v[156:159], v[68:71], v[44:47], v[156:159]\n"
"s_xor_b32 s46, s46, 0x10000\n"
"s_xor_b32 s49, s49, 0x10000\n"
"v_mfma_f32_16x16x32_bf16 v[156:159], v[84:87], v[60:63], v[156:159]\n"
"v_mfma_f32_16x16x32_bf16 v[152:155], v[84:87], v[56:59], v[152:155]\n"
"v_mfma_f32_16x16x32_bf16 v[152:155], v[68:71], v[40:43], v[152:155]\n"
"v_mfma_f32_16x16x32_bf16 v[148:151], v[68:71], v[36:39], v[148:151]\n"
"v_mfma_f32_16x16x32_bf16 v[148:151], v[84:87], v[52:55], v[148:151]\n"
"v_mfma_f32_16x16x32_bf16 v[144:147], v[84:87], v[48:51], v[144:147]\n"
"v_mfma_f32_16x16x32_bf16 v[144:147], v[68:71], v[32:35], v[144:147]\n"
"v_mfma_f32_16x16x32_bf16 v[160:163], v[72:75], v[32:35], v[160:163]\n"
"v_mfma_f32_16x16x32_bf16 v[160:163], v[88:91], v[48:51], v[160:163]\n"
"v_mfma_f32_16x16x32_bf16 v[164:167], v[88:91], v[52:55], v[164:167]\n"
"v_mfma_f32_16x16x32_bf16 v[164:167], v[72:75], v[36:39], v[164:167]\n"
"v_mfma_f32_16x16x32_bf16 v[168:171], v[72:75], v[40:43], v[168:171]\n"
"v_mfma_f32_16x16x32_bf16 v[168:171], v[88:91], v[56:59], v[168:171]\n"
"v_mfma_f32_16x16x32_bf16 v[172:175], v[88:91], v[60:63], v[172:175]\n"
"v_mfma_f32_16x16x32_bf16 v[172:175], v[72:75], v[44:47], v[172:175]\n"
"v_mfma_f32_16x16x32_bf16 v[188:191], v[76:79], v[44:47], v[188:191]\n"
"v_mfma_f32_16x16x32_bf16 v[188:191], v[92:95], v[60:63], v[188:191]\n"
"v_mfma_f32_16x16x32_bf16 v[184:187], v[92:95], v[56:59], v[184:187]\n"
"v_mfma_f32_16x16x32_bf16 v[184:187], v[76:79], v[40:43], v[184:187]\n"
"v_mfma_f32_16x16x32_bf16 v[180:183], v[76:79], v[36:39], v[180:183]\n"
"v_mfma_f32_16x16x32_bf16 v[180:183], v[92:95], v[52:55], v[180:183]\n"
"v_mfma_f32_16x16x32_bf16 v[176:179], v[92:95], v[48:51], v[176:179]\n"
"v_mfma_f32_16x16x32_bf16 v[176:179], v[76:79], v[32:35], v[176:179]\n"
"s_waitcnt vmcnt(4)\n"
"s_barrier\n"
"s_mov_b32 s48, 16384\n"
"s_add_u32 m0, s60, 32768\n"
"ds_read_b64_tr_b16 v[64:65], v26 offset:16384\n"
"buffer_load_dwordx4 v124, s[36:39], s48 offen lds\n"
"ds_read_b64_tr_b16 v[66:67], v26 offset:16896\n"
"ds_read_b64_tr_b16 v[68:69], v27 offset:16384\n"
"ds_read_b64_tr_b16 v[70:71], v27 offset:16896\n"
"ds_read_b64_tr_b16 v[72:73], v28 offset:16384\n"
"s_add_u32 m0, s60, 33792\n"
"ds_read_b64_tr_b16 v[74:75], v28 offset:16896\n"
"buffer_load_dwordx4 v125, s[36:39], s48 offen lds\n"
"ds_read_b64_tr_b16 v[80:81], v26 offset:20480\n"
"ds_read_b64_tr_b16 v[82:83], v26 offset:20992\n"
"ds_read_b64_tr_b16 v[84:85], v27 offset:20480\n"
"ds_read_b64_tr_b16 v[86:87], v27 offset:20992\n"
"s_add_u32 m0, s60, 34816\n"
"ds_read_b64_tr_b16 v[88:89], v28 offset:20480\n"
"buffer_load_dwordx4 v126, s[36:39], s48 offen lds\n"
"ds_read_b64_tr_b16 v[90:91], v28 offset:20992\n"
"ds_read_b128 v[32:35], v24 offset:0\n"
"ds_read_b128 v[36:39], v24 offset:2048\n"
"ds_read_b128 v[40:43], v24 offset:4096\n"
"s_add_u32 m0, s60, 35840\n"
"ds_read_b128 v[44:47], v24 offset:6144\n"
"buffer_load_dwordx4 v127, s[36:39], s48 offen lds\n"
"ds_read_b128 v[48:51], v25 offset:0\n"
"ds_read_b128 v[52:55], v25 offset:2048\n"
"ds_read_b128 v[56:59], v25 offset:4096\n"
"ds_read_b128 v[60:63], v25 offset:6144\n"
"s_waitcnt lgkmcnt(0)\n"
"s_barrier\n"
"v_mfma_f32_16x16x32_bf16 v[192:195], v[64:67], v[32:35], v[192:195]\n"
"v_mfma_f32_16x16x32_bf16 v[192:195], v[80:83], v[48:51], v[192:195]\n"
"v_mfma_f32_16x16x32_bf16 v[196:199], v[80:83], v[52:55], v[196:199]\n"
"v_mfma_f32_16x16x32_bf16 v[196:199], v[64:67], v[36:39], v[196:199]\n"
"v_mfma_f32_16x16x32_bf16 v[200:203], v[64:67], v[40:43], v[200:203]\n"
"v_mfma_f32_16x16x32_bf16 v[200:203], v[80:83], v[56:59], v[200:203]\n"
"v_mfma_f32_16x16x32_bf16 v[204:207], v[80:83], v[60:63], v[204:207]\n"
"v_mfma_f32_16x16x32_bf16 v[204:207], v[64:67], v[44:47], v[204:207]\n"
"v_mfma_f32_16x16x32_bf16 v[220:223], v[68:71], v[44:47], v[220:223]\n"
"v_mfma_f32_16x16x32_bf16 v[220:223], v[84:87], v[60:63], v[220:223]\n"
"v_mfma_f32_16x16x32_bf16 v[216:219], v[84:87], v[56:59], v[216:219]\n"
"v_mfma_f32_16x16x32_bf16 v[216:219], v[68:71], v[40:43], v[216:219]\n"
"v_mfma_f32_16x16x32_bf16 v[212:215], v[68:71], v[36:39], v[212:215]\n"
"v_mfma_f32_16x16x32_bf16 v[212:215], v[84:87], v[52:55], v[212:215]\n"
"v_mfma_f32_16x16x32_bf16 v[208:211], v[84:87], v[48:51], v[208:211]\n"
"v_mfma_f32_16x16x32_bf16 v[208:211], v[68:71], v[32:35], v[208:211]\n"
"v_mfma_f32_16x16x32_bf16 v[224:227], v[72:75], v[32:35], v[224:227]\n"
"v_mfma_f32_16x16x32_bf16 v[224:227], v[88:91], v[48:51], v[224:227]\n"
"v_mfma_f32_16x16x32_bf16 v[228:231], v[88:91], v[52:55], v[228:231]\n"
"v_mfma_f32_16x16x32_bf16 v[228:231], v[72:75], v[36:39], v[228:231]\n"
"v_mfma_f32_16x16x32_bf16 v[232:235], v[72:75], v[40:43], v[232:235]\n"
"v_mfma_f32_16x16x32_bf16 v[232:235], v[88:91], v[56:59], v[232:235]\n"
"v_mfma_f32_16x16x32_bf16 v[236:239], v[88:91], v[60:63], v[236:239]\n"
"v_mfma_f32_16x16x32_bf16 v[236:239], v[72:75], v[44:47], v[236:239]\n"
"s_barrier\n"
"s_mov_b32 s48, 32768\n"
"s_add_u32 m0, s60, 65536\n"
"ds_read_b64_tr_b16 v[64:65], v26 offset:0\n"
"buffer_load_dwordx4 v124, s[36:39], s48 offen lds\n"
"ds_read_b64_tr_b16 v[66:67], v26 offset:512\n"
"ds_read_b64_tr_b16 v[68:69], v27 offset:0\n"
"ds_read_b64_tr_b16 v[70:71], v27 offset:512\n"
"s_add_u32 m0, s60, 66560\n"
"ds_read_b64_tr_b16 v[72:73], v28 offset:0\n"
"buffer_load_dwordx4 v125, s[36:39], s48 offen lds\n"
"ds_read_b64_tr_b16 v[74:75], v28 offset:512\n"
"ds_read_b64_tr_b16 v[76:77], v29 offset:0\n"
"ds_read_b64_tr_b16 v[78:79], v29 offset:512\n"
"s_add_u32 m0, s60, 67584\n"
"ds_read_b64_tr_b16 v[80:81], v26 offset:4096\n"
"buffer_load_dwordx4 v126, s[36:39], s48 offen lds\n"
"ds_read_b64_tr_b16 v[82:83], v26 offset:4608\n"
"ds_read_b64_tr_b16 v[84:85], v27 offset:4096\n"
"ds_read_b64_tr_b16 v[86:87], v27 offset:4608\n"
"s_add_u32 m0, s60, 68608\n"
"ds_read_b64_tr_b16 v[88:89], v28 offset:4096\n"
"buffer_load_dwordx4 v127, s[36:39], s48 offen lds\n"
"ds_read_b64_tr_b16 v[90:91], v28 offset:4608\n"
"ds_read_b64_tr_b16 v[92:93], v29 offset:4096\n"
"ds_read_b64_tr_b16 v[94:95], v29 offset:4608\n"
"s_waitcnt lgkmcnt(0)\n"
"s_barrier\n"
"v_mfma_f32_16x16x32_bf16 v[128:131], v[64:67], v[32:35], v[128:131]\n"
"v_mfma_f32_16x16x32_bf16 v[128:131], v[80:83], v[48:51], v[128:131]\n"
"v_mfma_f32_16x16x32_bf16 v[132:135], v[80:83], v[52:55], v[132:135]\n"
"v_xor_b32 v24, 0x10000, v24\n"
"v_mfma_f32_16x16x32_bf16 v[132:135], v[64:67], v[36:39], v[132:135]\n"
"v_xor_b32 v25, 0x10000, v25\n"
"v_mfma_f32_16x16x32_bf16 v[136:139], v[64:67], v[40:43], v[136:139]\n"
"v_xor_b32 v26, 0x10000, v26\n"
"v_mfma_f32_16x16x32_bf16 v[136:139], v[80:83], v[56:59], v[136:139]\n"
"v_xor_b32 v27, 0x10000, v27\n"
"v_mfma_f32_16x16x32_bf16 v[140:143], v[80:83], v[60:63], v[140:143]\n"
"v_xor_b32 v28, 0x10000, v28\n"
"v_mfma_f32_16x16x32_bf16 v[140:143], v[64:67], v[44:47], v[140:143]\n"
"v_xor_b32 v29, 0x10000, v29\n"
"v_mfma_f32_16x16x32_bf16 v[156:159], v[68:71], v[44:47], v[156:159]\n"
"s_xor_b32 s46, s46, 0x10000\n"
"s_xor_b32 s49, s49, 0x10000\n"
"v_mfma_f32_16x16x32_bf16 v[156:159], v[84:87], v[60:63], v[156:159]\n"
"v_mfma_f32_16x16x32_bf16 v[152:155], v[84:87], v[56:59], v[152:155]\n"
"v_mfma_f32_16x16x32_bf16 v[152:155], v[68:71], v[40:43], v[152:155]\n"
"v_mfma_f32_16x16x32_bf16 v[148:151], v[68:71], v[36:39], v[148:151]\n"
"v_mfma_f32_16x16x32_bf16 v[148:151], v[84:87], v[52:55], v[148:151]\n"
"v_mfma_f32_16x16x32_bf16 v[144:147], v[84:87], v[48:51], v[144:147]\n"
"v_mfma_f32_16x16x32_bf16 v[144:147], v[68:71], v[32:35], v[144:147]\n"
"v_mfma_f32_16x16x32_bf16 v[160:163], v[72:75], v[32:35], v[160:163]\n"
"v_mfma_f32_16x16x32_bf16 v[160:163], v[88:91], v[48:51], v[160:163]\n"
"v_mfma_f32_16x16x32_bf16 v[164:167], v[88:91], v[52:55], v[164:167]\n"
"v_mfma_f32_16x16x32_bf16 v[164:167], v[72:75], v[36:39], v[164:167]\n"
"v_mfma_f32_16x16x32_bf16 v[168:171], v[72:75], v[40:43], v[168:171]\n"
"v_mfma_f32_16x16x32_bf16 v[168:171], v[88:91], v[56:59], v[168:171]\n"
"v_mfma_f32_16x16x32_bf16 v[172:175], v[88:91], v[60:63], v[172:175]\n"
"v_mfma_f32_16x16x32_bf16 v[172:175], v[72:75], v[44:47], v[172:175]\n"
"v_mfma_f32_16x16x32_bf16 v[188:191], v[76:79], v[44:47], v[188:191]\n"
"v_mfma_f32_16x16x32_bf16 v[188:191], v[92:95], v[60:63], v[188:191]\n"
"v_mfma_f32_16x16x32_bf16 v[184:187], v[92:95], v[56:59], v[184:187]\n"
"v_mfma_f32_16x16x32_bf16 v[184:187], v[76:79], v[40:43], v[184:187]\n"
"v_mfma_f32_16x16x32_bf16 v[180:183], v[76:79], v[36:39], v[180:183]\n"
"v_mfma_f32_16x16x32_bf16 v[180:183], v[92:95], v[52:55], v[180:183]\n"
"v_mfma_f32_16x16x32_bf16 v[176:179], v[92:95], v[48:51], v[176:179]\n"
"v_mfma_f32_16x16x32_bf16 v[176:179], v[76:79], v[32:35], v[176:179]\n"
"s_barrier\n"
"s_nop 7\n"
"s_mov_b32 s48, 49152\n"
"s_add_u32 m0, s60, 98304\n"
"s_nop 0\n"
"buffer_load_dwordx4 v124, s[36:39], s48 offen lds\n"
"s_add_u32 m0, s60, 99328\n"
"s_nop 0\n"
"buffer_load_dwordx4 v125, s[36:39], s48 offen lds\n"
"s_add_u32 m0, s60, 100352\n"
"s_nop 0\n"
"buffer_load_dwordx4 v126, s[36:39], s48 offen lds\n"
"s_add_u32 m0, s60, 101376\n"
"s_nop 0\n"
"buffer_load_dwordx4 v127, s[36:39], s48 offen lds\n"
"s_load_dwordx4 s[8:11], s[4:5], 48\n"
"s_mov_b32 s52, 0x3e0293ee\n"
"s_mov_b32 s53, 0x3e0293ee\n"
"s_mov_b32 s54, 0xbdd2d3e8\n"
"s_mov_b32 s55, 0xbdd2d3e8\n"
"v_pk_mul_f32 v[128:129], v[128:129], s[52:53]\n"
"v_pk_mul_f32 v[130:131], v[130:131], s[52:53]\n"
"v_pk_mul_f32 v[144:145], v[144:145], s[52:53]\n"
"v_pk_mul_f32 v[146:147], v[146:147], s[52:53]\n"
"v_cvt_pk_bf16_f32 v64, v128, v129\n"
"v_cvt_pk_bf16_f32 v65, v130, v131\n"
"v_cvt_pk_bf16_f32 v66, v144, v145\n"
"v_cvt_pk_bf16_f32 v67, v146, v147\n"
"v_pk_mul_f32 v[160:161], v[160:161], s[52:53]\n"
"v_pk_mul_f32 v[162:163], v[162:163], s[52:53]\n"
"v_pk_mul_f32 v[176:177], v[176:177], s[52:53]\n"
"v_pk_mul_f32 v[178:179], v[178:179], s[52:53]\n"
"v_cvt_pk_bf16_f32 v68, v160, v161\n"
"v_cvt_pk_bf16_f32 v69, v162, v163\n"
"v_cvt_pk_bf16_f32 v70, v176, v177\n"
"v_cvt_pk_bf16_f32 v71, v178, v179\n"
"v_pk_mul_f32 v[132:133], v[132:133], s[52:53]\n"
"v_pk_mul_f32 v[134:135], v[134:135], s[52:53]\n"
"v_pk_mul_f32 v[148:149], v[148:149], s[52:53]\n"
"v_pk_mul_f32 v[150:151], v[150:151], s[52:53]\n"
"v_cvt_pk_bf16_f32 v72, v132, v133\n"
"v_cvt_pk_bf16_f32 v73, v134, v135\n"
"v_cvt_pk_bf16_f32 v74, v148, v149\n"
"v_cvt_pk_bf16_f32 v75, v150, v151\n"
"v_pk_mul_f32 v[164:165], v[164:165], s[52:53]\n"
"v_pk_mul_f32 v[166:167], v[166:167], s[52:53]\n"
"v_pk_mul_f32 v[180:181], v[180:181], s[52:53]\n"
"v_pk_mul_f32 v[182:183], v[182:183], s[52:53]\n"
"v_cvt_pk_bf16_f32 v76, v164, v165\n"
"v_cvt_pk_bf16_f32 v77, v166, v167\n"
"v_cvt_pk_bf16_f32 v78, v180, v181\n"
"v_cvt_pk_bf16_f32 v79, v182, v183\n"
"v_pk_mul_f32 v[136:137], v[136:137], s[52:53]\n"
"v_pk_mul_f32 v[138:139], v[138:139], s[52:53]\n"
"v_pk_mul_f32 v[152:153], v[152:153], s[52:53]\n"
"v_pk_mul_f32 v[154:155], v[154:155], s[52:53]\n"
"v_cvt_pk_bf16_f32 v40, v136, v137\n"
"v_cvt_pk_bf16_f32 v41, v138, v139\n"
"v_cvt_pk_bf16_f32 v42, v152, v153\n"
"v_cvt_pk_bf16_f32 v43, v154, v155\n"
"v_pk_mul_f32 v[168:169], v[168:169], s[52:53]\n"
"v_pk_mul_f32 v[170:171], v[170:171], s[52:53]\n"
"v_pk_mul_f32 v[184:185], v[184:185], s[52:53]\n"
"v_pk_mul_f32 v[186:187], v[186:187], s[52:53]\n"
"v_cvt_pk_bf16_f32 v44, v168, v169\n"
"v_cvt_pk_bf16_f32 v45, v170, v171\n"
"v_cvt_pk_bf16_f32 v46, v184, v185\n"
"v_cvt_pk_bf16_f32 v47, v186, v187\n"
"v_pk_mul_f32 v[140:141], v[140:141], s[52:53]\n"
"v_pk_mul_f32 v[142:143], v[142:143], s[52:53]\n"
"v_pk_mul_f32 v[156:157], v[156:157], s[52:53]\n"
"v_pk_mul_f32 v[158:159], v[158:159], s[52:53]\n"
"v_cvt_pk_bf16_f32 v56, v140, v141\n"
"v_cvt_pk_bf16_f32 v57, v142, v143\n"
"v_cvt_pk_bf16_f32 v58, v156, v157\n"
"v_cvt_pk_bf16_f32 v59, v158, v159\n"
"v_pk_mul_f32 v[172:173], v[172:173], s[52:53]\n"
"v_pk_mul_f32 v[174:175], v[174:175], s[52:53]\n"
"v_pk_mul_f32 v[188:189], v[188:189], s[52:53]\n"
"v_pk_mul_f32 v[190:191], v[190:191], s[52:53]\n"
"v_cvt_pk_bf16_f32 v60, v172, v173\n"
"v_cvt_pk_bf16_f32 v61, v174, v175\n"
"v_cvt_pk_bf16_f32 v62, v188, v189\n"
"v_cvt_pk_bf16_f32 v63, v190, v191\n"
"s_nop 1\n"
"v_permlane16_swap_b32 v64, v66\n"
"v_permlane16_swap_b32 v65, v67\n"
"v_permlane16_swap_b32 v68, v70\n"
"v_permlane16_swap_b32 v69, v71\n"
"v_permlane16_swap_b32 v72, v74\n"
"v_permlane16_swap_b32 v73, v75\n"
"v_permlane16_swap_b32 v76, v78\n"
"v_permlane16_swap_b32 v77, v79\n"
"v_permlane16_swap_b32 v40, v42\n"
"v_permlane16_swap_b32 v41, v43\n"
"v_permlane16_swap_b32 v44, v46\n"
"v_permlane16_swap_b32 v45, v47\n"
"v_permlane16_swap_b32 v56, v58\n"
"v_permlane16_swap_b32 v57, v59\n"
"v_permlane16_swap_b32 v60, v62\n"
"v_permlane16_swap_b32 v61, v63\n"
"v_lshlrev_b32 v2, 4, v1\n"
"s_lshl_b32 s50, s21, 12\n"
"s_add_u32 s50, s50, 131072\n"
"v_add_u32 v2, s50, v2\n"
"ds_write_b128 v2, v[64:67] offset:0\n"
"ds_write_b128 v2, v[68:71] offset:1024\n"
"ds_write_b128 v2, v[72:75] offset:2048\n"
"ds_write_b128 v2, v[76:79] offset:3072\n"
"v_lshlrev_b32 v128, 16, v8\n"
"v_and_b32 v129, 0xffff0000, v8\n"
"v_lshlrev_b32 v130, 16, v9\n"
"v_and_b32 v131, 0xffff0000, v9\n"
"v_lshlrev_b32 v132, 16, v10\n"
"v_and_b32 v133, 0xffff0000, v10\n"
"v_lshlrev_b32 v134, 16, v11\n"
"v_and_b32 v135, 0xffff0000, v11\n"
"v_lshlrev_b32 v136, 16, v12\n"
"v_and_b32 v137, 0xffff0000, v12\n"
"v_lshlrev_b32 v138, 16, v13\n"
"v_and_b32 v139, 0xffff0000, v13\n"
"v_mov_b32 v144, 0xc0135761\n"
"v_mov_b32 v145, 0xc0135761\n"
"v_mov_b32 v146, 1.0\n"
"v_mov_b32 v147, 1.0\n"
"s_lshl_b32 s56, s20, 3\n"
"s_add_u32 s56, s56, s21\n"
"s_lshl_b32 s57, s56, 12\n"
"s_mul_i32 s58, s56, 3584\n"
"v_lshlrev_b32 v2, 4, v1\n"
"v_add_u32 v24, s57, v2\n"
"v_add_u32 v25, s58, v2\n"
"v_add_u32 v26, 3072, v25\n"
"v_min_u32 v26, 7340016, v26\n"
"s_waitcnt lgkmcnt(0)\n"
"s_mul_i32 s50, s22, 1835008\n"
"s_mul_i32 s51, s23, 224\n"
"s_add_u32 s50, s50, s51\n"
"s_add_u32 s40, s8, s50\n"
"s_addc_u32 s41, s9, 0\n"
"s_and_b32 s41, s41, 0xffff\n"
"s_sub_u32 s42, 1835008, s51\n"
"s_mov_b32 s43, 0x20000\n"
"v_and_b32 v3, 15, v1\n"
"s_lshl_b32 s50, s34, 6\n"
"v_add_u32 v3, s50, v3\n"
"v_mul_u32_u24 v3, 7168, v3\n"
"v_lshrrev_b32 v4, 4, v1\n"
"v_and_b32 v5, 1, v4\n"
"v_lshlrev_b32 v5, 5, v5\n"
"v_lshrrev_b32 v6, 1, v4\n"
"v_lshlrev_b32 v6, 4, v6\n"
"v_add_u32 v5, v5, v6\n"
"v_add_u32 v5, 128, v5\n"
"v_add_u32 v148, v3, v5\n"
"v_and_b32 v5, 1, v4\n"
"v_mul_u32_u24 v5, 114688, v5\n"
"v_add_u32 v5, v5, v6\n"
"v_add_u32 v5, 192, v5\n"
"v_add_u32 v149, v3, v5\n"
"v_pk_add_f32 v[192:193], v[192:193], v[128:129]\n"
"v_pk_add_f32 v[194:195], v[194:195], v[130:131]\n"
"v_pk_add_f32 v[208:209], v[208:209], v[132:133]\n"
"v_pk_add_f32 v[210:211], v[210:211], v[134:135]\n"
"v_pk_add_f32 v[196:197], v[196:197], v[128:129]\n"
"v_pk_add_f32 v[198:199], v[198:199], v[130:131]\n"
"v_pk_add_f32 v[212:213], v[212:213], v[132:133]\n"
"v_pk_add_f32 v[214:215], v[214:215], v[134:135]\n"
"v_pk_mul_f32 v[64:65], v[192:193], v[192:193]\n"
"v_pk_mul_f32 v[66:67], v[194:195], v[194:195]\n"
"v_pk_mul_f32 v[68:69], v[208:209], v[208:209]\n"
"v_pk_mul_f32 v[70:71], v[210:211], v[210:211]\n"
"v_pk_mul_f32 v[72:73], v[196:197], v[196:197]\n"
"v_pk_mul_f32 v[74:75], v[198:199], v[198:199]\n"
"v_pk_mul_f32 v[76:77], v[212:213], v[212:213]\n"
"v_pk_mul_f32 v[78:79], v[214:215], v[214:215]\n"
"v_pk_fma_f32 v[64:65], v[64:65], s[54:55], v[144:145]\n"
"v_pk_fma_f32 v[66:67], v[66:67], s[54:55], v[144:145]\n"
"v_pk_fma_f32 v[68:69], v[68:69], s[54:55], v[144:145]\n"
"v_pk_fma_f32 v[70:71], v[70:71], s[54:55], v[144:145]\n"
"v_pk_fma_f32 v[72:73], v[72:73], s[54:55], v[144:145]\n"
"v_pk_fma_f32 v[74:75], v[74:75], s[54:55], v[144:145]\n"
"v_pk_fma_f32 v[76:77], v[76:77], s[54:55], v[144:145]\n"
"v_pk_fma_f32 v[78:79], v[78:79], s[54:55], v[144:145]\n"
"v_pk_mul_f32 v[64:65], v[192:193], v[64:65]\n"
"v_pk_mul_f32 v[66:67], v[194:195], v[66:67]\n"
"v_pk_mul_f32 v[68:69], v[208:209], v[68:69]\n"
"v_pk_mul_f32 v[70:71], v[210:211], v[70:71]\n"
"v_pk_mul_f32 v[72:73], v[196:197], v[72:73]\n"
"v_pk_mul_f32 v[74:75], v[198:199], v[74:75]\n"
"v_pk_mul_f32 v[76:77], v[212:213], v[76:77]\n"
"v_pk_mul_f32 v[78:79], v[214:215], v[78:79]\n"
"v_exp_f32 v64, v64\n"
"v_exp_f32 v65, v65\n"
"v_exp_f32 v66, v66\n"
"v_exp_f32 v67, v67\n"
"v_exp_f32 v68, v68\n"
"v_exp_f32 v69, v69\n"
"v_exp_f32 v70, v70\n"
"v_exp_f32 v71, v71\n"
"v_exp_f32 v72, v72\n"
"v_exp_f32 v73, v73\n"
"v_exp_f32 v74, v74\n"
"v_exp_f32 v75, v75\n"
"v_exp_f32 v76, v76\n"
"v_exp_f32 v77, v77\n"
"v_exp_f32 v78, v78\n"
"v_exp_f32 v79, v79\n"
"v_pk_add_f32 v[64:65], v[64:65], v[146:147]\n"
"v_pk_add_f32 v[66:67], v[66:67], v[146:147]\n"
"v_pk_add_f32 v[68:69], v[68:69], v[146:147]\n"
"v_pk_add_f32 v[70:71], v[70:71], v[146:147]\n"
"v_pk_add_f32 v[72:73], v[72:73], v[146:147]\n"
"v_pk_add_f32 v[74:75], v[74:75], v[146:147]\n"
"v_pk_add_f32 v[76:77], v[76:77], v[146:147]\n"
"v_pk_add_f32 v[78:79], v[78:79], v[146:147]\n"
"v_rcp_f32 v64, v64\n"
"v_rcp_f32 v65, v65\n"
"v_rcp_f32 v66, v66\n"
"v_rcp_f32 v67, v67\n"
"v_rcp_f32 v68, v68\n"
"v_rcp_f32 v69, v69\n"
"v_rcp_f32 v70, v70\n"
"v_rcp_f32 v71, v71\n"
"v_rcp_f32 v72, v72\n"
"v_rcp_f32 v73, v73\n"
"v_rcp_f32 v74, v74\n"
"v_rcp_f32 v75, v75\n"
"v_rcp_f32 v76, v76\n"
"v_rcp_f32 v77, v77\n"
"v_rcp_f32 v78, v78\n"
"v_rcp_f32 v79, v79\n"
"v_pk_mul_f32 v[192:193], v[192:193], v[64:65]\n"
"v_pk_mul_f32 v[194:195], v[194:195], v[66:67]\n"
"v_pk_mul_f32 v[208:209], v[208:209], v[68:69]\n"
"v_pk_mul_f32 v[210:211], v[210:211], v[70:71]\n"
"v_pk_mul_f32 v[196:197], v[196:197], v[72:73]\n"
"v_pk_mul_f32 v[198:199], v[198:199], v[74:75]\n"
"v_pk_mul_f32 v[212:213], v[212:213], v[76:77]\n"
"v_pk_mul_f32 v[214:215], v[214:215], v[78:79]\n"
"v_cvt_pk_bf16_f32 v152, v192, v193\n"
"v_cvt_pk_bf16_f32 v153, v194, v195\n"
"v_cvt_pk_bf16_f32 v154, v208, v209\n"
"v_cvt_pk_bf16_f32 v155, v210, v211\n"
"v_cvt_pk_bf16_f32 v156, v196, v197\n"
"v_cvt_pk_bf16_f32 v157, v198, v199\n"
"v_cvt_pk_bf16_f32 v158, v212, v213\n"
"v_cvt_pk_bf16_f32 v159, v214, v215\n"
"s_nop 1\n"
"v_permlane16_swap_b32 v152, v154\n"
"v_permlane16_swap_b32 v153, v155\n"
"v_permlane16_swap_b32 v156, v158\n"
"v_permlane16_swap_b32 v157, v159\n"
"s_mov_b32 s50, 0\n"
"buffer_store_dwordx4 v[152:155], v148, s[40:43], s50 offen sc1\n"
"s_mov_b32 s50, 114688\n"
"buffer_store_dwordx4 v[156:159], v148, s[40:43], s50 offen sc1\n"
"v_pk_add_f32 v[200:201], v[200:201], v[128:129]\n"
"v_pk_add_f32 v[202:203], v[202:203], v[130:131]\n"
"v_pk_add_f32 v[216:217], v[216:217], v[132:133]\n"
"v_pk_add_f32 v[218:219], v[218:219], v[134:135]\n"
"v_pk_add_f32 v[204:205], v[204:205], v[128:129]\n"
"v_pk_add_f32 v[206:207], v[206:207], v[130:131]\n"
"v_pk_add_f32 v[220:221], v[220:221], v[132:133]\n"
"v_pk_add_f32 v[222:223], v[222:223], v[134:135]\n"
"v_pk_mul_f32 v[64:65], v[200:201], v[200:201]\n"
"v_pk_mul_f32 v[66:67], v[202:203], v[202:203]\n"
"v_pk_mul_f32 v[68:69], v[216:217], v[216:217]\n"
"v_pk_mul_f32 v[70:71], v[218:219], v[218:219]\n"
"v_pk_mul_f32 v[72:73], v[204:205], v[204:205]\n"
"v_pk_mul_f32 v[74:75], v[206:207], v[206:207]\n"
"v_pk_mul_f32 v[76:77], v[220:221], v[220:221]\n"
"v_pk_mul_f32 v[78:79], v[222:223], v[222:223]\n"
"v_pk_fma_f32 v[64:65], v[64:65], s[54:55], v[144:145]\n"
"v_pk_fma_f32 v[66:67], v[66:67], s[54:55], v[144:145]\n"
"v_pk_fma_f32 v[68:69], v[68:69], s[54:55], v[144:145]\n"
"v_pk_fma_f32 v[70:71], v[70:71], s[54:55], v[144:145]\n"
"v_pk_fma_f32 v[72:73], v[72:73], s[54:55], v[144:145]\n"
"v_pk_fma_f32 v[74:75], v[74:75], s[54:55], v[144:145]\n"
"v_pk_fma_f32 v[76:77], v[76:77], s[54:55], v[144:145]\n"
"v_pk_fma_f32 v[78:79], v[78:79], s[54:55], v[144:145]\n"
"v_pk_mul_f32 v[64:65], v[200:201], v[64:65]\n"
"v_pk_mul_f32 v[66:67], v[202:203], v[66:67]\n"
"v_pk_mul_f32 v[68:69], v[216:217], v[68:69]\n"
"v_pk_mul_f32 v[70:71], v[218:219], v[70:71]\n"
"v_pk_mul_f32 v[72:73], v[204:205], v[72:73]\n"
"v_pk_mul_f32 v[74:75], v[206:207], v[74:75]\n"
"v_pk_mul_f32 v[76:77], v[220:221], v[76:77]\n"
"v_pk_mul_f32 v[78:79], v[222:223], v[78:79]\n"
"v_exp_f32 v64, v64\n"
"v_exp_f32 v65, v65\n"
"v_exp_f32 v66, v66\n"
"v_exp_f32 v67, v67\n"
"v_exp_f32 v68, v68\n"
"v_exp_f32 v69, v69\n"
"v_exp_f32 v70, v70\n"
"v_exp_f32 v71, v71\n"
"v_exp_f32 v72, v72\n"
"v_exp_f32 v73, v73\n"
"v_exp_f32 v74, v74\n"
"v_exp_f32 v75, v75\n"
"v_exp_f32 v76, v76\n"
"v_exp_f32 v77, v77\n"
"v_exp_f32 v78, v78\n"
"v_exp_f32 v79, v79\n"
"v_pk_add_f32 v[64:65], v[64:65], v[146:147]\n"
"v_pk_add_f32 v[66:67], v[66:67], v[146:147]\n"
"v_pk_add_f32 v[68:69], v[68:69], v[146:147]\n"
"v_pk_add_f32 v[70:71], v[70:71], v[146:147]\n"
"v_pk_add_f32 v[72:73], v[72:73], v[146:147]\n"
"v_pk_add_f32 v[74:75], v[74:75], v[146:147]\n"
"v_pk_add_f32 v[76:77], v[76:77], v[146:147]\n"
"v_pk_add_f32 v[78:79], v[78:79], v[146:147]\n"
"v_rcp_f32 v64, v64\n"
"v_rcp_f32 v65, v65\n"
"v_rcp_f32 v66, v66\n"
"v_rcp_f32 v67, v67\n"
"v_rcp_f32 v68, v68\n"
"v_rcp_f32 v69, v69\n"
"v_rcp_f32 v70, v70\n"
"v_rcp_f32 v71, v71\n"
"v_rcp_f32 v72, v72\n"
"v_rcp_f32 v73, v73\n"
"v_rcp_f32 v74, v74\n"
"v_rcp_f32 v75, v75\n"
"v_rcp_f32 v76, v76\n"
"v_rcp_f32 v77, v77\n"
"v_rcp_f32 v78, v78\n"
"v_rcp_f32 v79, v79\n"
"v_pk_mul_f32 v[200:201], v[200:201], v[64:65]\n"
"v_pk_mul_f32 v[202:203], v[202:203], v[66:67]\n"
"v_pk_mul_f32 v[216:217], v[216:217], v[68:69]\n"
"v_pk_mul_f32 v[218:219], v[218:219], v[70:71]\n"
"v_pk_mul_f32 v[204:205], v[204:205], v[72:73]\n"
"v_pk_mul_f32 v[206:207], v[206:207], v[74:75]\n"
"v_pk_mul_f32 v[220:221], v[220:221], v[76:77]\n"
"v_pk_mul_f32 v[222:223], v[222:223], v[78:79]\n"
"v_cvt_pk_bf16_f32 v160, v200, v201\n"
"v_cvt_pk_bf16_f32 v161, v202, v203\n"
"v_cvt_pk_bf16_f32 v162, v216, v217\n"
"v_cvt_pk_bf16_f32 v163, v218, v219\n"
"v_cvt_pk_bf16_f32 v164, v204, v205\n"
"v_cvt_pk_bf16_f32 v165, v206, v207\n"
"v_cvt_pk_bf16_f32 v166, v220, v221\n"
"v_cvt_pk_bf16_f32 v167, v222, v223\n"
"s_nop 1\n"
"v_permlane16_swap_b32 v160, v162\n"
"v_permlane16_swap_b32 v161, v163\n"
"v_permlane16_swap_b32 v164, v166\n"
"v_permlane16_swap_b32 v165, v167\n"
"s_mov_b32 s50, 229376\n"
"buffer_store_dwordx4 v[160:163], v148, s[40:43], s50 offen sc1\n"
"s_mov_b32 s50, 344064\n"
"buffer_store_dwordx4 v[164:167], v148, s[40:43], s50 offen sc1\n"
"v_pk_add_f32 v[224:225], v[224:225], v[136:137]\n"
"v_pk_add_f32 v[226:227], v[226:227], v[138:139]\n"
"v_pk_add_f32 v[228:229], v[228:229], v[136:137]\n"
"v_pk_add_f32 v[230:231], v[230:231], v[138:139]\n"
"v_pk_add_f32 v[232:233], v[232:233], v[136:137]\n"
"v_pk_add_f32 v[234:235], v[234:235], v[138:139]\n"
"v_pk_add_f32 v[236:237], v[236:237], v[136:137]\n"
"v_pk_add_f32 v[238:239], v[238:239], v[138:139]\n"
"v_pk_mul_f32 v[64:65], v[224:225], v[224:225]\n"
"v_pk_mul_f32 v[66:67], v[226:227], v[226:227]\n"
"v_pk_mul_f32 v[68:69], v[228:229], v[228:229]\n"
"v_pk_mul_f32 v[70:71], v[230:231], v[230:231]\n"
"v_pk_mul_f32 v[72:73], v[232:233], v[232:233]\n"
"v_pk_mul_f32 v[74:75], v[234:235], v[234:235]\n"
"v_pk_mul_f32 v[76:77], v[236:237], v[236:237]\n"
"v_pk_mul_f32 v[78:79], v[238:239], v[238:239]\n"
"v_pk_fma_f32 v[64:65], v[64:65], s[54:55], v[144:145]\n"
"v_pk_fma_f32 v[66:67], v[66:67], s[54:55], v[144:145]\n"
"v_pk_fma_f32 v[68:69], v[68:69], s[54:55], v[144:145]\n"
"v_pk_fma_f32 v[70:71], v[70:71], s[54:55], v[144:145]\n"
"v_pk_fma_f32 v[72:73], v[72:73], s[54:55], v[144:145]\n"
"v_pk_fma_f32 v[74:75], v[74:75], s[54:55], v[144:145]\n"
"v_pk_fma_f32 v[76:77], v[76:77], s[54:55], v[144:145]\n"
"v_pk_fma_f32 v[78:79], v[78:79], s[54:55], v[144:145]\n"
"v_pk_mul_f32 v[64:65], v[224:225], v[64:65]\n"
"v_pk_mul_f32 v[66:67], v[226:227], v[66:67]\n"
"v_pk_mul_f32 v[68:69], v[228:229], v[68:69]\n"
"v_pk_mul_f32 v[70:71], v[230:231], v[70:71]\n"
"v_pk_mul_f32 v[72:73], v[232:233], v[72:73]\n"
"v_pk_mul_f32 v[74:75], v[234:235], v[74:75]\n"
"v_pk_mul_f32 v[76:77], v[236:237], v[76:77]\n"
"v_pk_mul_f32 v[78:79], v[238:239], v[78:79]\n"
"v_exp_f32 v64, v64\n"
"v_exp_f32 v65, v65\n"
"v_exp_f32 v66, v66\n"
"v_exp_f32 v67, v67\n"
"v_exp_f32 v68, v68\n"
"v_exp_f32 v69, v69\n"
"v_exp_f32 v70, v70\n"
"v_exp_f32 v71, v71\n"
"v_exp_f32 v72, v72\n"
"v_exp_f32 v73, v73\n"
"v_exp_f32 v74, v74\n"
"v_exp_f32 v75, v75\n"
"v_exp_f32 v76, v76\n"
"v_exp_f32 v77, v77\n"
"v_exp_f32 v78, v78\n"
"v_exp_f32 v79, v79\n"
"v_pk_add_f32 v[64:65], v[64:65], v[146:147]\n"
"v_pk_add_f32 v[66:67], v[66:67], v[146:147]\n"
"v_pk_add_f32 v[68:69], v[68:69], v[146:147]\n"
"v_pk_add_f32 v[70:71], v[70:71], v[146:147]\n"
"v_pk_add_f32 v[72:73], v[72:73], v[146:147]\n"
"v_pk_add_f32 v[74:75], v[74:75], v[146:147]\n"
"v_pk_add_f32 v[76:77], v[76:77], v[146:147]\n"
"v_pk_add_f32 v[78:79], v[78:79], v[146:147]\n"
"v_rcp_f32 v64, v64\n"
"v_rcp_f32 v65, v65\n"
"v_rcp_f32 v66, v66\n"
"v_rcp_f32 v67, v67\n"
"v_rcp_f32 v68, v68\n"
"v_rcp_f32 v69, v69\n"
"v_rcp_f32 v70, v70\n"
"v_rcp_f32 v71, v71\n"
"v_rcp_f32 v72, v72\n"
"v_rcp_f32 v73, v73\n"
"v_rcp_f32 v74, v74\n"
"v_rcp_f32 v75, v75\n"
"v_rcp_f32 v76, v76\n"
"v_rcp_f32 v77, v77\n"
"v_rcp_f32 v78, v78\n"
"v_rcp_f32 v79, v79\n"
"v_pk_mul_f32 v[224:225], v[224:225], v[64:65]\n"
"v_pk_mul_f32 v[226:227], v[226:227], v[66:67]\n"
"v_pk_mul_f32 v[228:229], v[228:229], v[68:69]\n"
"v_pk_mul_f32 v[230:231], v[230:231], v[70:71]\n"
"v_pk_mul_f32 v[232:233], v[232:233], v[72:73]\n"
"v_pk_mul_f32 v[234:235], v[234:235], v[74:75]\n"
"v_pk_mul_f32 v[236:237], v[236:237], v[76:77]\n"
"v_pk_mul_f32 v[238:239], v[238:239], v[78:79]\n"
"v_cvt_pk_bf16_f32 v168, v224, v225\n"
"v_cvt_pk_bf16_f32 v169, v226, v227\n"
"v_cvt_pk_bf16_f32 v170, v228, v229\n"
"v_cvt_pk_bf16_f32 v171, v230, v231\n"
"v_cvt_pk_bf16_f32 v172, v232, v233\n"
"v_cvt_pk_bf16_f32 v173, v234, v235\n"
"v_cvt_pk_bf16_f32 v174, v236, v237\n"
"v_cvt_pk_bf16_f32 v175, v238, v239\n"
"s_nop 1\n"
"v_permlane16_swap_b32 v168, v170\n"
"v_permlane16_swap_b32 v169, v171\n"
"v_permlane16_swap_b32 v172, v174\n"
"v_permlane16_swap_b32 v173, v175\n"
"s_mov_b32 s50, 0\n"
"buffer_store_dwordx4 v[168:171], v149, s[40:43], s50 offen sc1\n"
"s_mov_b32 s50, 229376\n"
"buffer_store_dwordx4 v[172:175], v149, s[40:43], s50 offen sc1\n"
"s_waitcnt vmcnt(14) lgkmcnt(0)\n"
"s_barrier\n"
"v_lshlrev_b32 v2, 4, v1\n"
"s_xor_b32 s50, s21, 4\n"
"s_lshl_b32 s50, s50, 12\n"
"s_add_u32 s50, s50, 131072\n"
"v_add_u32 v2, s50, v2\n"
"ds_read_b128 v[32:35], v2 offset:0\n"
"ds_read_b128 v[36:39], v2 offset:1024\n"
"ds_read_b128 v[48:51], v2 offset:2048\n"
"ds_read_b128 v[52:55], v2 offset:3072\n"
"P1_attn:\n"
"v_and_b32 v2, 15, v1\n"
"v_lshrrev_b32 v3, 4, v1\n"
"v_and_b32 v4, 1, v3\n"
"v_lshlrev_b32 v4, 1, v4\n"
"v_lshrrev_b32 v5, 1, v3\n"
"v_or_b32 v4, v4, v5\n"
"v_xor_b32 v4, v4, v2\n"
"v_lshlrev_b32 v4, 4, v4\n"
"v_lshlrev_b32 v5, 8, v2\n"
"v_add_u32 v16, v4, v5\n"
"v_xor_b32 v17, 64, v16\n"
"v_xor_b32 v18, 128, v16\n"
"v_xor_b32 v19, 192, v16\n"
"v_lshrrev_b32 v4, 2, v2\n"
"v_and_b32 v5, 3, v1\n"
"v_lshlrev_b32 v6, 2, v3\n"
"v_add_u32 v6, v6, v4\n"
"v_lshlrev_b32 v6, 7, v6\n"
"v_and_b32 v3, 1, v3\n"
"v_lshlrev_b32 v3, 1, v3\n"
"v_lshrrev_b32 v4, 1, v4\n"
"v_add_u32 v3, v3, v4\n"
"v_lshlrev_b32 v3, 1, v3\n"
"v_lshrrev_b32 v4, 1, v5\n"
"v_xor_b32 v3, v3, v4\n"
"v_lshlrev_b32 v3, 4, v3\n"
"v_and_b32 v5, 1, v5\n"
"v_lshlrev_b32 v5, 3, v5\n"
"v_add_u32 v6, v6, v3\n"
"v_add_u32 v6, v6, v5\n"
"v_add_u32 v20, 16384, v6\n"
"v_xor_b32 v21, 32, v20\n"
"v_xor_b32 v22, 64, v20\n"
"v_xor_b32 v23, 96, v20\n"
"v_mov_b32 v120, 0x3f803f80\n"
"v_mov_b32 v121, 0x3f803f80\n"
"v_mov_b32 v122, 0x3f803f80\n"
"v_mov_b32 v123, 0x3f803f80\n"
"s_waitcnt lgkmcnt(0)\n"
"ds_read_b128 v[160:163], v16 offset:0\n"
"ds_read_b128 v[164:167], v16 offset:4096\n"
"ds_read_b128 v[168:171], v16 offset:8192\n"
"ds_read_b128 v[172:175], v16 offset:12288\n"
"ds_read_b128 v[176:179], v17 offset:0\n"
"ds_read_b128 v[180:183], v17 offset:4096\n"
"s_waitcnt lgkmcnt(4)\n"
"v_mfma_f32_16x16x32_bf16 v[64:67], v[160:163], v[32:35], 0\n"
"v_mfma_f32_16x16x32_bf16 v[80:83], v[160:163], v[48:51], 0\n"
"ds_read_b128 v[184:187], v17 offset:8192\n"
"v_mfma_f32_16x16x32_bf16 v[68:71], v[164:167], v[32:35], 0\n"
"v_mfma_f32_16x16x32_bf16 v[84:87], v[164:167], v[48:51], 0\n"
"ds_read_b128 v[188:191], v17 offset:12288\n"
"s_waitcnt lgkmcnt(4)\n"
"v_mfma_f32_16x16x32_bf16 v[72:75], v[168:171], v[32:35], 0\n"
"v_mfma_f32_16x16x32_bf16 v[88:91], v[168:171], v[48:51], 0\n"
"ds_read_b128 v[160:163], v18 offset:0\n"
"v_mfma_f32_16x16x32_bf16 v[76:79], v[172:175], v[32:35], 0\n"
"v_mfma_f32_16x16x32_bf16 v[92:95], v[172:175], v[48:51], 0\n"
"ds_read_b128 v[164:167], v18 offset:4096\n"
"s_waitcnt lgkmcnt(4)\n"
"v_mfma_f32_16x16x32_bf16 v[64:67], v[176:179], v[36:39], v[64:67]\n"
"v_mfma_f32_16x16x32_bf16 v[80:83], v[176:179], v[52:55], v[80:83]\n"
"ds_read_b128 v[168:171], v18 offset:8192\n"
"v_mfma_f32_16x16x32_bf16 v[68:71], v[180:183], v[36:39], v[68:71]\n"
"v_mfma_f32_16x16x32_bf16 v[84:87], v[180:183], v[52:55], v[84:87]\n"
"ds_read_b128 v[172:175], v18 offset:12288\n"
"s_waitcnt lgkmcnt(4)\n"
"v_mfma_f32_16x16x32_bf16 v[72:75], v[184:187], v[36:39], v[72:75]\n"
"v_mfma_f32_16x16x32_bf16 v[88:91], v[184:187], v[52:55], v[88:91]\n"
"ds_read_b128 v[176:179], v19 offset:0\n"
"v_mfma_f32_16x16x32_bf16 v[76:79], v[188:191], v[36:39], v[76:79]\n"
"v_mfma_f32_16x16x32_bf16 v[92:95], v[188:191], v[52:55], v[92:95]\n"
"ds_read_b128 v[180:183], v19 offset:4096\n"
"s_waitcnt lgkmcnt(4)\n"
"v_mfma_f32_16x16x32_bf16 v[64:67], v[160:163], v[40:43], v[64:67]\n"
"v_mfma_f32_16x16x32_bf16 v[80:83], v[160:163], v[56:59], v[80:83]\n"
"ds_read_b128 v[184:187], v19 offset:8192\n"
"v_mfma_f32_16x16x32_bf16 v[68:71], v[164:167], v[40:43], v[68:71]\n"
"v_mfma_f32_16x16x32_bf16 v[84:87], v[164:167], v[56:59], v[84:87]\n"
"ds_read_b128 v[188:191], v19 offset:12288\n"
"s_waitcnt lgkmcnt(4)\n"
"v_mfma_f32_16x16x32_bf16 v[72:75], v[168:171], v[40:43], v[72:75]\n"
"v_mfma_f32_16x16x32_bf16 v[88:91], v[168:171], v[56:59], v[88:91]\n"
"ds_read_b128 v[160:163], v16 offset:32768\n"
"v_mfma_f32_16x16x32_bf16 v[76:79], v[172:175], v[40:43], v[76:79]\n"
"v_mfma_f32_16x16x32_bf16 v[92:95], v[172:175], v[56:59], v[92:95]\n"
"ds_read_b128 v[164:167], v16 offset:36864\n"
"s_waitcnt lgkmcnt(4)\n"
"v_mfma_f32_16x16x32_bf16 v[64:67], v[176:179], v[44:47], v[64:67]\n"
"v_mfma_f32_16x16x32_bf16 v[80:83], v[176:179], v[60:63], v[80:83]\n"
"ds_read_b128 v[168:171], v16 offset:40960\n"
"v_mfma_f32_16x16x32_bf16 v[68:71], v[180:183], v[44:47], v[68:71]\n"
"v_mfma_f32_16x16x32_bf16 v[84:87], v[180:183], v[60:63], v[84:87]\n"
"ds_read_b128 v[172:175], v16 offset:45056\n"
"s_waitcnt lgkmcnt(4)\n"
"v_mfma_f32_16x16x32_bf16 v[72:75], v[184:187], v[44:47], v[72:75]\n"
"v_mfma_f32_16x16x32_bf16 v[88:91], v[184:187], v[60:63], v[88:91]\n"
"ds_read_b128 v[176:179], v17 offset:32768\n"
"v_mfma_f32_16x16x32_bf16 v[76:79], v[188:191], v[44:47], v[76:79]\n"
"v_mfma_f32_16x16x32_bf16 v[92:95], v[188:191], v[60:63], v[92:95]\n"
"ds_read_b128 v[180:183], v17 offset:36864\n"
"s_nop 7\n"
"v_max3_f32 v2, v64, v65, v66\n"
"v_max3_f32 v2, v2, v67, v68\n"
"v_max3_f32 v2, v2, v69, v70\n"
"v_max3_f32 v2, v2, v71, v72\n"
"v_max3_f32 v2, v2, v73, v74\n"
"v_max3_f32 v2, v2, v75, v76\n"
"v_max3_f32 v2, v2, v77, v78\n"
"v_max_f32 v2, v2, v79\n"
"v_mov_b32 v3, v2\n"
"s_nop 1\n"
"v_permlane32_swap_b32 v2, v3\n"
"s_nop 1\n"
"v_max_f32 v2, v2, v3\n"
"v_mov_b32 v3, v2\n"
"s_nop 1\n"
"v_permlane16_swap_b32 v2, v3\n"
"s_nop 1\n"
"v_max_f32 v2, v2, v3\n"
"v_sub_f32 v8, 0, v2\n"
"v_sub_f32 v9, 0, v2\n"
"v_sub_f32 v10, 0, v2\n"
"v_sub_f32 v11, 0, v2\n"
"v_sub_f32 v64, v64, v2\n"
"v_sub_f32 v65, v65, v2\n"
"v_sub_f32 v66, v66, v2\n"
"v_sub_f32 v67, v67, v2\n"
"v_sub_f32 v68, v68, v2\n"
"v_sub_f32 v69, v69, v2\n"
"v_sub_f32 v70, v70, v2\n"
"v_sub_f32 v71, v71, v2\n"
"v_sub_f32 v72, v72, v2\n"
"v_sub_f32 v73, v73, v2\n"
"v_sub_f32 v74, v74, v2\n"
"v_sub_f32 v75, v75, v2\n"
"v_sub_f32 v76, v76, v2\n"
"v_sub_f32 v77, v77, v2\n"
"v_sub_f32 v78, v78, v2\n"
"v_sub_f32 v79, v79, v2\n"
"v_max3_f32 v4, v80, v81, v82\n"
"v_max3_f32 v4, v4, v83, v84\n"
"v_max3_f32 v4, v4, v85, v86\n"
"v_max3_f32 v4, v4, v87, v88\n"
"v_max3_f32 v4, v4, v89, v90\n"
"v_max3_f32 v4, v4, v91, v92\n"
"v_max3_f32 v4, v4, v93, v94\n"
"v_max_f32 v4, v4, v95\n"
"v_mov_b32 v5, v4\n"
"s_nop 1\n"
"v_permlane32_swap_b32 v4, v5\n"
"s_nop 1\n"
"v_max_f32 v4, v4, v5\n"
"v_mov_b32 v5, v4\n"
"s_nop 1\n"
"v_permlane16_swap_b32 v4, v5\n"
"s_nop 1\n"
"v_max_f32 v4, v4, v5\n"
"v_sub_f32 v12, 0, v4\n"
"v_sub_f32 v13, 0, v4\n"
"v_sub_f32 v14, 0, v4\n"
"v_sub_f32 v15, 0, v4\n"
"v_sub_f32 v80, v80, v4\n"
"v_sub_f32 v81, v81, v4\n"
"v_sub_f32 v82, v82, v4\n"
"v_sub_f32 v83, v83, v4\n"
"v_sub_f32 v84, v84, v4\n"
"v_sub_f32 v85, v85, v4\n"
"v_sub_f32 v86, v86, v4\n"
"v_sub_f32 v87, v87, v4\n"
"v_sub_f32 v88, v88, v4\n"
"v_sub_f32 v89, v89, v4\n"
"v_sub_f32 v90, v90, v4\n"
"v_sub_f32 v91, v91, v4\n"
"v_sub_f32 v92, v92, v4\n"
"v_sub_f32 v93, v93, v4\n"
"v_sub_f32 v94, v94, v4\n"
"v_sub_f32 v95, v95, v4\n"
"v_exp_f32 v64, v64\n"
"v_exp_f32 v65, v65\n"
"v_exp_f32 v66, v66\n"
"v_exp_f32 v67, v67\n"
"v_exp_f32 v80, v80\n"
"v_exp_f32 v81, v81\n"
"v_exp_f32 v82, v82\n"
"v_exp_f32 v83, v83\n"
"v_exp_f32 v68, v68\n"
"v_exp_f32 v69, v69\n"
"v_exp_f32 v70, v70\n"
"v_exp_f32 v71, v71\n"
"v_exp_f32 v84, v84\n"
"v_exp_f32 v85, v85\n"
"v_exp_f32 v86, v86\n"
"v_exp_f32 v87, v87\n"
"s_nop 1\n"
"s_waitcnt lgkmcnt(4)\n"
"v_mfma_f32_16x16x32_bf16 v[128:131], v[160:163], v[32:35], v[8:11]\n"
"v_cvt_pk_bf16_f32 v96, v64, v65\n"
"v_mfma_f32_16x16x32_bf16 v[144:147], v[160:163], v[48:51], v[12:15]\n"
"v_cvt_pk_bf16_f32 v97, v66, v67\n"
"ds_read_b128 v[184:187], v17 offset:40960\n"
"v_mfma_f32_16x16x32_bf16 v[132:135], v[164:167], v[32:35], v[8:11]\n"
"v_cvt_pk_bf16_f32 v98, v68, v69\n"
"v_mfma_f32_16x16x32_bf16 v[148:151], v[164:167], v[48:51], v[12:15]\n"
"v_cvt_pk_bf16_f32 v99, v70, v71\n"
"ds_read_b128 v[188:191], v17 offset:45056\n"
"s_waitcnt lgkmcnt(4)\n"
"v_mfma_f32_16x16x32_bf16 v[136:139], v[168:171], v[32:35], v[8:11]\n"
"v_cvt_pk_bf16_f32 v104, v80, v81\n"
"v_mfma_f32_16x16x32_bf16 v[152:155], v[168:171], v[48:51], v[12:15]\n"
"v_cvt_pk_bf16_f32 v105, v82, v83\n"
"ds_read_b128 v[160:163], v18 offset:32768\n"
"v_mfma_f32_16x16x32_bf16 v[140:143], v[172:175], v[32:35], v[8:11]\n"
"v_cvt_pk_bf16_f32 v106, v84, v85\n"
"v_mfma_f32_16x16x32_bf16 v[156:159], v[172:175], v[48:51], v[12:15]\n"
"v_cvt_pk_bf16_f32 v107, v86, v87\n"
"ds_read_b128 v[164:167], v18 offset:36864\n"
"s_waitcnt lgkmcnt(4)\n"
"v_mfma_f32_16x16x32_bf16 v[128:131], v[176:179], v[36:39], v[128:131]\n"
"v_exp_f32 v72, v72\n"
"v_mfma_f32_16x16x32_bf16 v[144:147], v[176:179], v[52:55], v[144:147]\n"
"v_exp_f32 v73, v73\n"
"ds_read_b128 v[168:171], v18 offset:40960\n"
"v_mfma_f32_16x16x32_bf16 v[132:135], v[180:183], v[36:39], v[132:135]\n"
"v_exp_f32 v74, v74\n"
"v_mfma_f32_16x16x32_bf16 v[148:151], v[180:183], v[52:55], v[148:151]\n"
"v_exp_f32 v75, v75\n"
"ds_read_b128 v[172:175], v18 offset:45056\n"
"s_waitcnt lgkmcnt(4)\n"
"v_mfma_f32_16x16x32_bf16 v[136:139], v[184:187], v[36:39], v[136:139]\n"
"v_exp_f32 v88, v88\n"
"v_mfma_f32_16x16x32_bf16 v[152:155], v[184:187], v[52:55], v[152:155]\n"
"v_exp_f32 v89, v89\n"
"ds_read_b128 v[176:179], v19 offset:32768\n"
"v_mfma_f32_16x16x32_bf16 v[140:143], v[188:191], v[36:39], v[140:143]\n"
"v_exp_f32 v90, v90\n"
"v_mfma_f32_16x16x32_bf16 v[156:159], v[188:191], v[52:55], v[156:159]\n"
"v_exp_f32 v91, v91\n"
"ds_read_b128 v[180:183], v19 offset:36864\n"
"s_waitcnt lgkmcnt(4)\n"
"v_mfma_f32_16x16x32_bf16 v[128:131], v[160:163], v[40:43], v[128:131]\n"
"v_exp_f32 v76, v76\n"
"v_mfma_f32_16x16x32_bf16 v[144:147], v[160:163], v[56:59], v[144:147]\n"
"v_exp_f32 v77, v77\n"
"ds_read_b128 v[184:187], v19 offset:40960\n"
"v_mfma_f32_16x16x32_bf16 v[132:135], v[164:167], v[40:43], v[132:135]\n"
"v_exp_f32 v78, v78\n"
"v_mfma_f32_16x16x32_bf16 v[148:151], v[164:167], v[56:59], v[148:151]\n"
"v_exp_f32 v79, v79\n"
"ds_read_b128 v[188:191], v19 offset:45056\n"
"s_waitcnt lgkmcnt(4)\n"
"v_mfma_f32_16x16x32_bf16 v[136:139], v[168:171], v[40:43], v[136:139]\n"
"v_exp_f32 v92, v92\n"
"v_mfma_f32_16x16x32_bf16 v[152:155], v[168:171], v[56:59], v[152:155]\n"
"v_exp_f32 v93, v93\n"
"ds_read_b64_tr_b16 v[160:161], v20 offset:0\n"
"ds_read_b64_tr_b16 v[162:163], v20 offset:2048\n"
"v_mfma_f32_16x16x32_bf16 v[140:143], v[172:175], v[40:43], v[140:143]\n"
"v_exp_f32 v94, v94\n"
"v_mfma_f32_16x16x32_bf16 v[156:159], v[172:175], v[56:59], v[156:159]\n"
"v_exp_f32 v95, v95\n"
"ds_read_b64_tr_b16 v[164:165], v21 offset:0\n"
"ds_read_b64_tr_b16 v[166:167], v21 offset:2048\n"
"s_waitcnt lgkmcnt(6)\n"
"v_mfma_f32_16x16x32_bf16 v[128:131], v[176:179], v[44:47], v[128:131]\n"
"v_cvt_pk_bf16_f32 v100, v72, v73\n"
"v_mfma_f32_16x16x32_bf16 v[144:147], v[176:179], v[60:63], v[144:147]\n"
"v_cvt_pk_bf16_f32 v101, v74, v75\n"
"ds_read_b64_tr_b16 v[168:169], v22 offset:0\n"
"ds_read_b64_tr_b16 v[170:171], v22 offset:2048\n"
"v_mfma_f32_16x16x32_bf16 v[132:135], v[180:183], v[44:47], v[132:135]\n"
"v_cvt_pk_bf16_f32 v102, v76, v77\n"
"v_mfma_f32_16x16x32_bf16 v[148:151], v[180:183], v[60:63], v[148:151]\n"
"v_cvt_pk_bf16_f32 v103, v78, v79\n"
"ds_read_b64_tr_b16 v[172:173], v23 offset:0\n"
"ds_read_b64_tr_b16 v[174:175], v23 offset:2048\n"
"s_waitcnt lgkmcnt(8)\n"
"v_mfma_f32_16x16x32_bf16 v[136:139], v[184:187], v[44:47], v[136:139]\n"
"v_cvt_pk_bf16_f32 v108, v88, v89\n"
"v_mfma_f32_16x16x32_bf16 v[152:155], v[184:187], v[60:63], v[152:155]\n"
"v_cvt_pk_bf16_f32 v109, v90, v91\n"
"ds_read_b64_tr_b16 v[176:177], v20 offset:8192\n"
"ds_read_b64_tr_b16 v[178:179], v20 offset:10240\n"
"v_mfma_f32_16x16x32_bf16 v[140:143], v[188:191], v[44:47], v[140:143]\n"
"v_cvt_pk_bf16_f32 v110, v92, v93\n"
"v_mfma_f32_16x16x32_bf16 v[156:159], v[188:191], v[60:63], v[156:159]\n"
"v_cvt_pk_bf16_f32 v111, v94, v95\n"
"ds_read_b64_tr_b16 v[180:181], v21 offset:8192\n"
"ds_read_b64_tr_b16 v[182:183], v21 offset:10240\n"
"s_nop 1\n"
"global_load_dwordx4 v[28:31], v24, s[92:93] offset:0\n"
"s_waitcnt lgkmcnt(8)\n"
"v_mfma_f32_16x16x32_bf16 v[192:195], v[160:163], v[96:99], 0\n"
"v_mfma_f32_16x16x32_bf16 v[196:199], v[160:163], v[104:107], 0\n"
"ds_read_b64_tr_b16 v[184:185], v22 offset:8192\n"
"ds_read_b64_tr_b16 v[186:187], v22 offset:10240\n"
"v_mfma_f32_16x16x32_bf16 v[200:203], v[164:167], v[96:99], 0\n"
"v_mfma_f32_16x16x32_bf16 v[204:207], v[164:167], v[104:107], 0\n"
"ds_read_b64_tr_b16 v[188:189], v23 offset:8192\n"
"ds_read_b64_tr_b16 v[190:191], v23 offset:10240\n"
"s_waitcnt lgkmcnt(8)\n"
"v_mfma_f32_16x16x32_bf16 v[208:211], v[168:171], v[96:99], 0\n"
"v_exp_f32 v128, v128\n"
"v_mfma_f32_16x16x32_bf16 v[212:215], v[168:171], v[104:107], 0\n"
"ds_read_b64_tr_b16 v[160:161], v20 offset:4096\n"
"ds_read_b64_tr_b16 v[162:163], v20 offset:6144\n"
"v_mfma_f32_16x16x32_bf16 v[216:219], v[172:175], v[96:99], 0\n"
"v_exp_f32 v129, v129\n"
"v_mfma_f32_16x16x32_bf16 v[220:223], v[172:175], v[104:107], 0\n"
"v_mfma_f32_16x16x32_bf16 v[112:115], v[120:123], v[96:99], 0\n"
"v_exp_f32 v130, v130\n"
"v_mfma_f32_16x16x32_bf16 v[116:119], v[120:123], v[104:107], 0\n"
"ds_read_b64_tr_b16 v[164:165], v21 offset:4096\n"
"ds_read_b64_tr_b16 v[166:167], v21 offset:6144\n"
"s_waitcnt lgkmcnt(8)\n"
"v_mfma_f32_16x16x32_bf16 v[224:227], v[176:179], v[96:99], 0\n"
"v_exp_f32 v131, v131\n"
"v_mfma_f32_16x16x32_bf16 v[228:231], v[176:179], v[104:107], 0\n"
"ds_read_b64_tr_b16 v[168:169], v22 offset:4096\n"
"ds_read_b64_tr_b16 v[170:171], v22 offset:6144\n"
"v_mfma_f32_16x16x32_bf16 v[232:235], v[180:183], v[96:99], 0\n"
"v_exp_f32 v144, v144\n"
"v_mfma_f32_16x16x32_bf16 v[236:239], v[180:183], v[104:107], 0\n"
"ds_read_b64_tr_b16 v[172:173], v23 offset:4096\n"
"ds_read_b64_tr_b16 v[174:175], v23 offset:6144\n"
"s_waitcnt lgkmcnt(8)\n"
"v_mfma_f32_16x16x32_bf16 v[240:243], v[184:187], v[96:99], 0\n"
"v_exp_f32 v145, v145\n"
"v_mfma_f32_16x16x32_bf16 v[244:247], v[184:187], v[104:107], 0\n"
"ds_read_b64_tr_b16 v[176:177], v20 offset:12288\n"
"ds_read_b64_tr_b16 v[178:179], v20 offset:14336\n"
"v_mfma_f32_16x16x32_bf16 v[248:251], v[188:191], v[96:99], 0\n"
"v_exp_f32 v146, v146\n"
"v_mfma_f32_16x16x32_bf16 v[252:255], v[188:191], v[104:107], 0\n"
"ds_read_b64_tr_b16 v[180:181], v21 offset:12288\n"
"ds_read_b64_tr_b16 v[182:183], v21 offset:14336\n"
"s_waitcnt lgkmcnt(8)\n"
"v_mfma_f32_16x16x32_bf16 v[192:195], v[160:163], v[100:103], v[192:195]\n"
"v_exp_f32 v147, v147\n"
"v_mfma_f32_16x16x32_bf16 v[196:199], v[160:163], v[108:111], v[196:199]\n"
"ds_read_b64_tr_b16 v[184:185], v22 offset:12288\n"
"ds_read_b64_tr_b16 v[186:187], v22 offset:14336\n"
"v_mfma_f32_16x16x32_bf16 v[200:203], v[164:167], v[100:103], v[200:203]\n"
"v_exp_f32 v132, v132\n"
"v_mfma_f32_16x16x32_bf16 v[204:207], v[164:167], v[108:111], v[204:207]\n"
"ds_read_b64_tr_b16 v[188:189], v23 offset:12288\n"
"ds_read_b64_tr_b16 v[190:191], v23 offset:14336\n"
"s_waitcnt lgkmcnt(8)\n"
"v_mfma_f32_16x16x32_bf16 v[208:211], v[168:171], v[100:103], v[208:211]\n"
"v_exp_f32 v133, v133\n"
"v_mfma_f32_16x16x32_bf16 v[212:215], v[168:171], v[108:111], v[212:215]\n"
"s_waitcnt vmcnt(11)\n"
"s_barrier\n"
"s_mov_b32 s48, 65536\n"
"s_add_u32 m0, s60, 131072\n"
"s_nop 0\n"
"buffer_load_dwordx4 v124, s[36:39], s48 offen lds\n"
"s_add_u32 m0, s60, 132096\n"
"s_nop 0\n"
"buffer_load_dwordx4 v125, s[36:39], s48 offen lds\n"
"s_add_u32 m0, s60, 133120\n"
"s_nop 0\n"
"buffer_load_dwordx4 v126, s[36:39], s48 offen lds\n"
"s_add_u32 m0, s60, 134144\n"
"s_nop 0\n"
"buffer_load_dwordx4 v127, s[36:39], s48 offen lds\n"
"v_xor_b32 v16, 0x10000, v16\n"
"v_xor_b32 v17, 0x10000, v17\n"
"v_xor_b32 v18, 0x10000, v18\n"
"v_xor_b32 v19, 0x10000, v19\n"
"ds_read_b128 v[160:163], v16 offset:0\n"
"v_mfma_f32_16x16x32_bf16 v[216:219], v[172:175], v[100:103], v[216:219]\n"
"v_exp_f32 v134, v134\n"
"v_mfma_f32_16x16x32_bf16 v[220:223], v[172:175], v[108:111], v[220:223]\n"
"v_mfma_f32_16x16x32_bf16 v[112:115], v[120:123], v[100:103], v[112:115]\n"
"v_exp_f32 v135, v135\n"
"v_mfma_f32_16x16x32_bf16 v[116:119], v[120:123], v[108:111], v[116:119]\n"
"ds_read_b128 v[164:167], v16 offset:4096\n"
"s_waitcnt lgkmcnt(6)\n"
"v_mfma_f32_16x16x32_bf16 v[224:227], v[176:179], v[100:103], v[224:227]\n"
"v_exp_f32 v148, v148\n"
"v_mfma_f32_16x16x32_bf16 v[228:231], v[176:179], v[108:111], v[228:231]\n"
"ds_read_b128 v[168:171], v16 offset:8192\n"
"v_mfma_f32_16x16x32_bf16 v[232:235], v[180:183], v[100:103], v[232:235]\n"
"v_exp_f32 v149, v149\n"
"v_mfma_f32_16x16x32_bf16 v[236:239], v[180:183], v[108:111], v[236:239]\n"
"ds_read_b128 v[172:175], v16 offset:12288\n"
"s_waitcnt lgkmcnt(4)\n"
"v_mfma_f32_16x16x32_bf16 v[240:243], v[184:187], v[100:103], v[240:243]\n"
"v_exp_f32 v150, v150\n"
"v_mfma_f32_16x16x32_bf16 v[244:247], v[184:187], v[108:111], v[244:247]\n"
"ds_read_b128 v[176:179], v17 offset:0\n"
"v_mfma_f32_16x16x32_bf16 v[248:251], v[188:191], v[100:103], v[248:251]\n"
"v_exp_f32 v151, v151\n"
"v_mfma_f32_16x16x32_bf16 v[252:255], v[188:191], v[108:111], v[252:255]\n"
"ds_read_b128 v[180:183], v17 offset:4096\n"
"s_waitcnt lgkmcnt(4)\n"
"v_mfma_f32_16x16x32_bf16 v[64:67], v[160:163], v[32:35], v[8:11]\n"
"v_cvt_pk_bf16_f32 v96, v128, v129\n"
"v_mfma_f32_16x16x32_bf16 v[80:83], v[160:163], v[48:51], v[12:15]\n"
"v_cvt_pk_bf16_f32 v97, v130, v131\n"
"ds_read_b128 v[184:187], v17 offset:8192\n"
"v_mfma_f32_16x16x32_bf16 v[68:71], v[164:167], v[32:35], v[8:11]\n"
"v_cvt_pk_bf16_f32 v98, v132, v133\n"
"v_mfma_f32_16x16x32_bf16 v[84:87], v[164:167], v[48:51], v[12:15]\n"
"v_cvt_pk_bf16_f32 v99, v134, v135\n"
"ds_read_b128 v[188:191], v17 offset:12288\n"
"s_waitcnt lgkmcnt(4)\n"
"v_mfma_f32_16x16x32_bf16 v[72:75], v[168:171], v[32:35], v[8:11]\n"
"v_cvt_pk_bf16_f32 v104, v144, v145\n"
"v_mfma_f32_16x16x32_bf16 v[88:91], v[168:171], v[48:51], v[12:15]\n"
"v_cvt_pk_bf16_f32 v105, v146, v147\n"
"ds_read_b128 v[160:163], v18 offset:0\n"
"v_mfma_f32_16x16x32_bf16 v[76:79], v[172:175], v[32:35], v[8:11]\n"
"v_cvt_pk_bf16_f32 v106, v148, v149\n"
"v_mfma_f32_16x16x32_bf16 v[92:95], v[172:175], v[48:51], v[12:15]\n"
"v_cvt_pk_bf16_f32 v107, v150, v151\n"
"ds_read_b128 v[164:167], v18 offset:4096\n"
"s_waitcnt lgkmcnt(4)\n"
"v_mfma_f32_16x16x32_bf16 v[64:67], v[176:179], v[36:39], v[64:67]\n"
"v_exp_f32 v136, v136\n"
"v_mfma_f32_16x16x32_bf16 v[80:83], v[176:179], v[52:55], v[80:83]\n"
"v_exp_f32 v137, v137\n"
"ds_read_b128 v[168:171], v18 offset:8192\n"
"v_mfma_f32_16x16x32_bf16 v[68:71], v[180:183], v[36:39], v[68:71]\n"
"v_exp_f32 v138, v138\n"
"v_mfma_f32_16x16x32_bf16 v[84:87], v[180:183], v[52:55], v[84:87]\n"
"v_exp_f32 v139, v139\n"
"ds_read_b128 v[172:175], v18 offset:12288\n"
"s_waitcnt lgkmcnt(4)\n"
"v_mfma_f32_16x16x32_bf16 v[72:75], v[184:187], v[36:39], v[72:75]\n"
"v_exp_f32 v152, v152\n"
"v_mfma_f32_16x16x32_bf16 v[88:91], v[184:187], v[52:55], v[88:91]\n"
"v_exp_f32 v153, v153\n"
"ds_read_b128 v[176:179], v19 offset:0\n"
"v_mfma_f32_16x16x32_bf16 v[76:79], v[188:191], v[36:39], v[76:79]\n"
"v_exp_f32 v154, v154\n"
"v_mfma_f32_16x16x32_bf16 v[92:95], v[188:191], v[52:55], v[92:95]\n"
"v_exp_f32 v155, v155\n"
"ds_read_b128 v[180:183], v19 offset:4096\n"
"s_waitcnt lgkmcnt(4)\n"
"v_mfma_f32_16x16x32_bf16 v[64:67], v[160:163], v[40:43], v[64:67]\n"
"v_exp_f32 v140, v140\n"
"v_mfma_f32_16x16x32_bf16 v[80:83], v[160:163], v[56:59], v[80:83]\n"
"v_exp_f32 v141, v141\n"
"ds_read_b128 v[184:187], v19 offset:8192\n"
"v_mfma_f32_16x16x32_bf16 v[68:71], v[164:167], v[40:43], v[68:71]\n"
"v_exp_f32 v142, v142\n"
"v_mfma_f32_16x16x32_bf16 v[84:87], v[164:167], v[56:59], v[84:87]\n"
"v_exp_f32 v143, v143\n"
"ds_read_b128 v[188:191], v19 offset:12288\n"
"s_waitcnt lgkmcnt(4)\n"
"v_mfma_f32_16x16x32_bf16 v[72:75], v[168:171], v[40:43], v[72:75]\n"
"v_exp_f32 v156, v156\n"
"v_mfma_f32_16x16x32_bf16 v[88:91], v[168:171], v[56:59], v[88:91]\n"
"v_exp_f32 v157, v157\n"
"ds_read_b64_tr_b16 v[160:161], v20 offset:32768\n"
"ds_read_b64_tr_b16 v[162:163], v20 offset:34816\n"
"v_mfma_f32_16x16x32_bf16 v[76:79], v[172:175], v[40:43], v[76:79]\n"
"v_exp_f32 v158, v158\n"
"v_mfma_f32_16x16x32_bf16 v[92:95], v[172:175], v[56:59], v[92:95]\n"
"v_exp_f32 v159, v159\n"
"ds_read_b64_tr_b16 v[164:165], v21 offset:32768\n"
"ds_read_b64_tr_b16 v[166:167], v21 offset:34816\n"
"s_waitcnt lgkmcnt(6)\n"
"v_mfma_f32_16x16x32_bf16 v[64:67], v[176:179], v[44:47], v[64:67]\n"
"v_cvt_pk_bf16_f32 v100, v136, v137\n"
"v_mfma_f32_16x16x32_bf16 v[80:83], v[176:179], v[60:63], v[80:83]\n"
"v_cvt_pk_bf16_f32 v101, v138, v139\n"
"ds_read_b64_tr_b16 v[168:169], v22 offset:32768\n"
"ds_read_b64_tr_b16 v[170:171], v22 offset:34816\n"
"v_mfma_f32_16x16x32_bf16 v[68:71], v[180:183], v[44:47], v[68:71]\n"
"v_cvt_pk_bf16_f32 v102, v140, v141\n"
"v_mfma_f32_16x16x32_bf16 v[84:87], v[180:183], v[60:63], v[84:87]\n"
"v_cvt_pk_bf16_f32 v103, v142, v143\n"
"ds_read_b64_tr_b16 v[172:173], v23 offset:32768\n"
"ds_read_b64_tr_b16 v[174:175], v23 offset:34816\n"
"s_waitcnt lgkmcnt(8)\n"
"v_mfma_f32_16x16x32_bf16 v[72:75], v[184:187], v[44:47], v[72:75]\n"
"v_cvt_pk_bf16_f32 v108, v152, v153\n"
"v_mfma_f32_16x16x32_bf16 v[88:91], v[184:187], v[60:63], v[88:91]\n"
"v_cvt_pk_bf16_f32 v109, v154, v155\n"
"ds_read_b64_tr_b16 v[176:177], v20 offset:40960\n"
"ds_read_b64_tr_b16 v[178:179], v20 offset:43008\n"
"v_mfma_f32_16x16x32_bf16 v[76:79], v[188:191], v[44:47], v[76:79]\n"
"v_cvt_pk_bf16_f32 v110, v156, v157\n"
"v_mfma_f32_16x16x32_bf16 v[92:95], v[188:191], v[60:63], v[92:95]\n"
"v_cvt_pk_bf16_f32 v111, v158, v159\n"
"ds_read_b64_tr_b16 v[180:181], v21 offset:40960\n"
"ds_read_b64_tr_b16 v[182:183], v21 offset:43008\n"
"s_nop 1\n"
"global_load_dwordx4 v[28:31], v25, s[94:95] offset:0\n"
"s_waitcnt lgkmcnt(8)\n"
"v_mfma_f32_16x16x32_bf16 v[192:195], v[160:163], v[96:99], v[192:195]\n"
"v_mfma_f32_16x16x32_bf16 v[196:199], v[160:163], v[104:107], v[196:199]\n"
"ds_read_b64_tr_b16 v[184:185], v22 offset:40960\n"
"ds_read_b64_tr_b16 v[186:187], v22 offset:43008\n"
"v_mfma_f32_16x16x32_bf16 v[200:203], v[164:167], v[96:99], v[200:203]\n"
"v_mfma_f32_16x16x32_bf16 v[204:207], v[164:167], v[104:107], v[204:207]\n"
"ds_read_b64_tr_b16 v[188:189], v23 offset:40960\n"
"ds_read_b64_tr_b16 v[190:191], v23 offset:43008\n"
"s_waitcnt lgkmcnt(8)\n"
"v_mfma_f32_16x16x32_bf16 v[208:211], v[168:171], v[96:99], v[208:211]\n"
"v_exp_f32 v64, v64\n"
"v_mfma_f32_16x16x32_bf16 v[212:215], v[168:171], v[104:107], v[212:215]\n"
"ds_read_b64_tr_b16 v[160:161], v20 offset:36864\n"
"ds_read_b64_tr_b16 v[162:163], v20 offset:38912\n"
"v_mfma_f32_16x16x32_bf16 v[216:219], v[172:175], v[96:99], v[216:219]\n"
"v_exp_f32 v65, v65\n"
"v_mfma_f32_16x16x32_bf16 v[220:223], v[172:175], v[104:107], v[220:223]\n"
"v_mfma_f32_16x16x32_bf16 v[112:115], v[120:123], v[96:99], v[112:115]\n"
"v_exp_f32 v66, v66\n"
"v_mfma_f32_16x16x32_bf16 v[116:119], v[120:123], v[104:107], v[116:119]\n"
"ds_read_b64_tr_b16 v[164:165], v21 offset:36864\n"
"ds_read_b64_tr_b16 v[166:167], v21 offset:38912\n"
"s_waitcnt lgkmcnt(8)\n"
"v_mfma_f32_16x16x32_bf16 v[224:227], v[176:179], v[96:99], v[224:227]\n"
"v_exp_f32 v67, v67\n"
"v_mfma_f32_16x16x32_bf16 v[228:231], v[176:179], v[104:107], v[228:231]\n"
"ds_read_b64_tr_b16 v[168:169], v22 offset:36864\n"
"ds_read_b64_tr_b16 v[170:171], v22 offset:38912\n"
"v_mfma_f32_16x16x32_bf16 v[232:235], v[180:183], v[96:99], v[232:235]\n"
"v_exp_f32 v80, v80\n"
"v_mfma_f32_16x16x32_bf16 v[236:239], v[180:183], v[104:107], v[236:239]\n"
"ds_read_b64_tr_b16 v[172:173], v23 offset:36864\n"
"ds_read_b64_tr_b16 v[174:175], v23 offset:38912\n"
"s_waitcnt lgkmcnt(8)\n"
"v_mfma_f32_16x16x32_bf16 v[240:243], v[184:187], v[96:99], v[240:243]\n"
"v_exp_f32 v81, v81\n"
"v_mfma_f32_16x16x32_bf16 v[244:247], v[184:187], v[104:107], v[244:247]\n"
"ds_read_b64_tr_b16 v[176:177], v20 offset:45056\n"
"ds_read_b64_tr_b16 v[178:179], v20 offset:47104\n"
"v_mfma_f32_16x16x32_bf16 v[248:251], v[188:191], v[96:99], v[248:251]\n"
"v_exp_f32 v82, v82\n"
"v_mfma_f32_16x16x32_bf16 v[252:255], v[188:191], v[104:107], v[252:255]\n"
"ds_read_b64_tr_b16 v[180:181], v21 offset:45056\n"
"ds_read_b64_tr_b16 v[182:183], v21 offset:47104\n"
"s_waitcnt lgkmcnt(8)\n"
"v_mfma_f32_16x16x32_bf16 v[192:195], v[160:163], v[100:103], v[192:195]\n"
"v_exp_f32 v83, v83\n"
"v_mfma_f32_16x16x32_bf16 v[196:199], v[160:163], v[108:111], v[196:199]\n"
"ds_read_b64_tr_b16 v[184:185], v22 offset:45056\n"
"ds_read_b64_tr_b16 v[186:187], v22 offset:47104\n"
"v_mfma_f32_16x16x32_bf16 v[200:203], v[164:167], v[100:103], v[200:203]\n"
"v_exp_f32 v68, v68\n"
"v_mfma_f32_16x16x32_bf16 v[204:207], v[164:167], v[108:111], v[204:207]\n"
"ds_read_b64_tr_b16 v[188:189], v23 offset:45056\n"
"ds_read_b64_tr_b16 v[190:191], v23 offset:47104\n"
"s_waitcnt lgkmcnt(8)\n"
"v_mfma_f32_16x16x32_bf16 v[208:211], v[168:171], v[100:103], v[208:211]\n"
"v_exp_f32 v69, v69\n"
"v_mfma_f32_16x16x32_bf16 v[212:215], v[168:171], v[108:111], v[212:215]\n"
"s_waitcnt vmcnt(12)\n"
"s_barrier\n"
"s_mov_b32 s48, 81920\n"
"s_add_u32 m0, s60, 0\n"
"s_nop 0\n"
"buffer_load_dwordx4 v124, s[36:39], s48 offen lds\n"
"s_add_u32 m0, s60, 1024\n"
"s_nop 0\n"
"buffer_load_dwordx4 v125, s[36:39], s48 offen lds\n"
"s_add_u32 m0, s60, 2048\n"
"s_nop 0\n"
"buffer_load_dwordx4 v126, s[36:39], s48 offen lds\n"
"s_add_u32 m0, s60, 3072\n"
"s_nop 0\n"
"buffer_load_dwordx4 v127, s[36:39], s48 offen lds\n"
"ds_read_b128 v[160:163], v16 offset:32768\n"
"v_mfma_f32_16x16x32_bf16 v[216:219], v[172:175], v[100:103], v[216:219]\n"
"v_exp_f32 v70, v70\n"
"v_mfma_f32_16x16x32_bf16 v[220:223], v[172:175], v[108:111], v[220:223]\n"
"v_mfma_f32_16x16x32_bf16 v[112:115], v[120:123], v[100:103], v[112:115]\n"
"v_exp_f32 v71, v71\n"
"v_mfma_f32_16x16x32_bf16 v[116:119], v[120:123], v[108:111], v[116:119]\n"
"ds_read_b128 v[164:167], v16 offset:36864\n"
"s_waitcnt lgkmcnt(6)\n"
"v_mfma_f32_16x16x32_bf16 v[224:227], v[176:179], v[100:103], v[224:227]\n"
"v_exp_f32 v84, v84\n"
"v_mfma_f32_16x16x32_bf16 v[228:231], v[176:179], v[108:111], v[228:231]\n"
"ds_read_b128 v[168:171], v16 offset:40960\n"
"v_mfma_f32_16x16x32_bf16 v[232:235], v[180:183], v[100:103], v[232:235]\n"
"v_exp_f32 v85, v85\n"
"v_mfma_f32_16x16x32_bf16 v[236:239], v[180:183], v[108:111], v[236:239]\n"
"ds_read_b128 v[172:175], v16 offset:45056\n"
"s_waitcnt lgkmcnt(4)\n"
"v_mfma_f32_16x16x32_bf16 v[240:243], v[184:187], v[100:103], v[240:243]\n"
"v_exp_f32 v86, v86\n"
"v_mfma_f32_16x16x32_bf16 v[244:247], v[184:187], v[108:111], v[244:247]\n"
"ds_read_b128 v[176:179], v17 offset:32768\n"
"v_mfma_f32_16x16x32_bf16 v[248:251], v[188:191], v[100:103], v[248:251]\n"
"v_exp_f32 v87, v87\n"
"v_mfma_f32_16x16x32_bf16 v[252:255], v[188:191], v[108:111], v[252:255]\n"
"ds_read_b128 v[180:183], v17 offset:36864\n"
"s_waitcnt lgkmcnt(4)\n"
"v_mfma_f32_16x16x32_bf16 v[128:131], v[160:163], v[32:35], v[8:11]\n"
"v_cvt_pk_bf16_f32 v96, v64, v65\n"
"v_mfma_f32_16x16x32_bf16 v[144:147], v[160:163], v[48:51], v[12:15]\n"
"v_cvt_pk_bf16_f32 v97, v66, v67\n"
"ds_read_b128 v[184:187], v17 offset:40960\n"
"v_mfma_f32_16x16x32_bf16 v[132:135], v[164:167], v[32:35], v[8:11]\n"
"v_cvt_pk_bf16_f32 v98, v68, v69\n"
"v_mfma_f32_16x16x32_bf16 v[148:151], v[164:167], v[48:51], v[12:15]\n"
"v_cvt_pk_bf16_f32 v99, v70, v71\n"
"ds_read_b128 v[188:191], v17 offset:45056\n"
"s_waitcnt lgkmcnt(4)\n"
"v_mfma_f32_16x16x32_bf16 v[136:139], v[168:171], v[32:35], v[8:11]\n"
"v_cvt_pk_bf16_f32 v104, v80, v81\n"
"v_mfma_f32_16x16x32_bf16 v[152:155], v[168:171], v[48:51], v[12:15]\n"
"v_cvt_pk_bf16_f32 v105, v82, v83\n"
"ds_read_b128 v[160:163], v18 offset:32768\n"
"v_mfma_f32_16x16x32_bf16 v[140:143], v[172:175], v[32:35], v[8:11]\n"
"v_cvt_pk_bf16_f32 v106, v84, v85\n"
"v_mfma_f32_16x16x32_bf16 v[156:159], v[172:175], v[48:51], v[12:15]\n"
"v_cvt_pk_bf16_f32 v107, v86, v87\n"
"ds_read_b128 v[164:167], v18 offset:36864\n"
"s_waitcnt lgkmcnt(4)\n"
"v_mfma_f32_16x16x32_bf16 v[128:131], v[176:179], v[36:39], v[128:131]\n"
"v_exp_f32 v72, v72\n"
"v_mfma_f32_16x16x32_bf16 v[144:147], v[176:179], v[52:55], v[144:147]\n"
"v_exp_f32 v73, v73\n"
"ds_read_b128 v[168:171], v18 offset:40960\n"
"v_mfma_f32_16x16x32_bf16 v[132:135], v[180:183], v[36:39], v[132:135]\n"
"v_exp_f32 v74, v74\n"
"v_mfma_f32_16x16x32_bf16 v[148:151], v[180:183], v[52:55], v[148:151]\n"
"v_exp_f32 v75, v75\n"
"ds_read_b128 v[172:175], v18 offset:45056\n"
"s_waitcnt lgkmcnt(4)\n"
"v_mfma_f32_16x16x32_bf16 v[136:139], v[184:187], v[36:39], v[136:139]\n"
"v_exp_f32 v88, v88\n"
"v_mfma_f32_16x16x32_bf16 v[152:155], v[184:187], v[52:55], v[152:155]\n"
"v_exp_f32 v89, v89\n"
"ds_read_b128 v[176:179], v19 offset:32768\n"
"v_mfma_f32_16x16x32_bf16 v[140:143], v[188:191], v[36:39], v[140:143]\n"
"v_exp_f32 v90, v90\n"
"v_mfma_f32_16x16x32_bf16 v[156:159], v[188:191], v[52:55], v[156:159]\n"
"v_exp_f32 v91, v91\n"
"ds_read_b128 v[180:183], v19 offset:36864\n"
"s_waitcnt lgkmcnt(4)\n"
"v_mfma_f32_16x16x32_bf16 v[128:131], v[160:163], v[40:43], v[128:131]\n"
"v_exp_f32 v76, v76\n"
"v_mfma_f32_16x16x32_bf16 v[144:147], v[160:163], v[56:59], v[144:147]\n"
"v_exp_f32 v77, v77\n"
"ds_read_b128 v[184:187], v19 offset:40960\n"
"v_mfma_f32_16x16x32_bf16 v[132:135], v[164:167], v[40:43], v[132:135]\n"
"v_exp_f32 v78, v78\n"
"v_mfma_f32_16x16x32_bf16 v[148:151], v[164:167], v[56:59], v[148:151]\n"
"v_exp_f32 v79, v79\n"
"ds_read_b128 v[188:191], v19 offset:45056\n"
"s_waitcnt lgkmcnt(4)\n"
"v_mfma_f32_16x16x32_bf16 v[136:139], v[168:171], v[40:43], v[136:139]\n"
"v_exp_f32 v92, v92\n"
"v_mfma_f32_16x16x32_bf16 v[152:155], v[168:171], v[56:59], v[152:155]\n"
"v_exp_f32 v93, v93\n"
"v_xor_b32 v20, 0x10000, v20\n"
"v_xor_b32 v21, 0x10000, v21\n"
"v_xor_b32 v22, 0x10000, v22\n"
"v_xor_b32 v23, 0x10000, v23\n"
"ds_read_b64_tr_b16 v[160:161], v20 offset:0\n"
"ds_read_b64_tr_b16 v[162:163], v20 offset:2048\n"
"v_mfma_f32_16x16x32_bf16 v[140:143], v[172:175], v[40:43], v[140:143]\n"
"v_exp_f32 v94, v94\n"
"v_mfma_f32_16x16x32_bf16 v[156:159], v[172:175], v[56:59], v[156:159]\n"
"v_exp_f32 v95, v95\n"
"ds_read_b64_tr_b16 v[164:165], v21 offset:0\n"
"ds_read_b64_tr_b16 v[166:167], v21 offset:2048\n"
"s_waitcnt lgkmcnt(6)\n"
"v_mfma_f32_16x16x32_bf16 v[128:131], v[176:179], v[44:47], v[128:131]\n"
"v_cvt_pk_bf16_f32 v100, v72, v73\n"
"v_mfma_f32_16x16x32_bf16 v[144:147], v[176:179], v[60:63], v[144:147]\n"
"v_cvt_pk_bf16_f32 v101, v74, v75\n"
"ds_read_b64_tr_b16 v[168:169], v22 offset:0\n"
"ds_read_b64_tr_b16 v[170:171], v22 offset:2048\n"
"v_mfma_f32_16x16x32_bf16 v[132:135], v[180:183], v[44:47], v[132:135]\n"
"v_cvt_pk_bf16_f32 v102, v76, v77\n"
"v_mfma_f32_16x16x32_bf16 v[148:151], v[180:183], v[60:63], v[148:151]\n"
"v_cvt_pk_bf16_f32 v103, v78, v79\n"
"ds_read_b64_tr_b16 v[172:173], v23 offset:0\n"
"ds_read_b64_tr_b16 v[174:175], v23 offset:2048\n"
"s_waitcnt lgkmcnt(8)\n"
"v_mfma_f32_16x16x32_bf16 v[136:139], v[184:187], v[44:47], v[136:139]\n"
"v_cvt_pk_bf16_f32 v108, v88, v89\n"
"v_mfma_f32_16x16x32_bf16 v[152:155], v[184:187], v[60:63], v[152:155]\n"
"v_cvt_pk_bf16_f32 v109, v90, v91\n"
"ds_read_b64_tr_b16 v[176:177], v20 offset:8192\n"
"ds_read_b64_tr_b16 v[178:179], v20 offset:10240\n"
"v_mfma_f32_16x16x32_bf16 v[140:143], v[188:191], v[44:47], v[140:143]\n"
"v_cvt_pk_bf16_f32 v110, v92, v93\n"
"v_mfma_f32_16x16x32_bf16 v[156:159], v[188:191], v[60:63], v[156:159]\n"
"v_cvt_pk_bf16_f32 v111, v94, v95\n"
"ds_read_b64_tr_b16 v[180:181], v21 offset:8192\n"
"ds_read_b64_tr_b16 v[182:183], v21 offset:10240\n"
"s_nop 1\n"
"global_load_dwordx4 v[28:31], v24, s[92:93] offset:1024\n"
"s_waitcnt lgkmcnt(8)\n"
"v_mfma_f32_16x16x32_bf16 v[192:195], v[160:163], v[96:99], v[192:195]\n"
"v_mfma_f32_16x16x32_bf16 v[196:199], v[160:163], v[104:107], v[196:199]\n"
"ds_read_b64_tr_b16 v[184:185], v22 offset:8192\n"
"ds_read_b64_tr_b16 v[186:187], v22 offset:10240\n"
"v_mfma_f32_16x16x32_bf16 v[200:203], v[164:167], v[96:99], v[200:203]\n"
"v_mfma_f32_16x16x32_bf16 v[204:207], v[164:167], v[104:107], v[204:207]\n"
"ds_read_b64_tr_b16 v[188:189], v23 offset:8192\n"
"ds_read_b64_tr_b16 v[190:191], v23 offset:10240\n"
"s_waitcnt lgkmcnt(8)\n"
"v_mfma_f32_16x16x32_bf16 v[208:211], v[168:171], v[96:99], v[208:211]\n"
"v_exp_f32 v128, v128\n"
"v_mfma_f32_16x16x32_bf16 v[212:215], v[168:171], v[104:107], v[212:215]\n"
"ds_read_b64_tr_b16 v[160:161], v20 offset:4096\n"
"ds_read_b64_tr_b16 v[162:163], v20 offset:6144\n"
"v_mfma_f32_16x16x32_bf16 v[216:219], v[172:175], v[96:99], v[216:219]\n"
"v_exp_f32 v129, v129\n"
"v_mfma_f32_16x16x32_bf16 v[220:223], v[172:175], v[104:107], v[220:223]\n"
"v_mfma_f32_16x16x32_bf16 v[112:115], v[120:123], v[96:99], v[112:115]\n"
"v_exp_f32 v130, v130\n"
"v_mfma_f32_16x16x32_bf16 v[116:119], v[120:123], v[104:107], v[116:119]\n"
"ds_read_b64_tr_b16 v[164:165], v21 offset:4096\n"
"ds_read_b64_tr_b16 v[166:167], v21 offset:6144\n"
"s_waitcnt lgkmcnt(8)\n"
"v_mfma_f32_16x16x32_bf16 v[224:227], v[176:179], v[96:99], v[224:227]\n"
"v_exp_f32 v131, v131\n"
"v_mfma_f32_16x16x32_bf16 v[228:231], v[176:179], v[104:107], v[228:231]\n"
"ds_read_b64_tr_b16 v[168:169], v22 offset:4096\n"
"ds_read_b64_tr_b16 v[170:171], v22 offset:6144\n"
"v_mfma_f32_16x16x32_bf16 v[232:235], v[180:183], v[96:99], v[232:235]\n"
"v_exp_f32 v144, v144\n"
"v_mfma_f32_16x16x32_bf16 v[236:239], v[180:183], v[104:107], v[236:239]\n"
"ds_read_b64_tr_b16 v[172:173], v23 offset:4096\n"
"ds_read_b64_tr_b16 v[174:175], v23 offset:6144\n"
"s_waitcnt lgkmcnt(8)\n"
"v_mfma_f32_16x16x32_bf16 v[240:243], v[184:187], v[96:99], v[240:243]\n"
"v_exp_f32 v145, v145\n"
"v_mfma_f32_16x16x32_bf16 v[244:247], v[184:187], v[104:107], v[244:247]\n"
"ds_read_b64_tr_b16 v[176:177], v20 offset:12288\n"
"ds_read_b64_tr_b16 v[178:179], v20 offset:14336\n"
"v_mfma_f32_16x16x32_bf16 v[248:251], v[188:191], v[96:99], v[248:251]\n"
"v_exp_f32 v146, v146\n"
"v_mfma_f32_16x16x32_bf16 v[252:255], v[188:191], v[104:107], v[252:255]\n"
"ds_read_b64_tr_b16 v[180:181], v21 offset:12288\n"
"ds_read_b64_tr_b16 v[182:183], v21 offset:14336\n"
"s_waitcnt lgkmcnt(8)\n"
"v_mfma_f32_16x16x32_bf16 v[192:195], v[160:163], v[100:103], v[192:195]\n"
"v_exp_f32 v147, v147\n"
"v_mfma_f32_16x16x32_bf16 v[196:199], v[160:163], v[108:111], v[196:199]\n"
"ds_read_b64_tr_b16 v[184:185], v22 offset:12288\n"
"ds_read_b64_tr_b16 v[186:187], v22 offset:14336\n"
"v_mfma_f32_16x16x32_bf16 v[200:203], v[164:167], v[100:103], v[200:203]\n"
"v_exp_f32 v132, v132\n"
"v_mfma_f32_16x16x32_bf16 v[204:207], v[164:167], v[108:111], v[204:207]\n"
"ds_read_b64_tr_b16 v[188:189], v23 offset:12288\n"
"ds_read_b64_tr_b16 v[190:191], v23 offset:14336\n"
"s_waitcnt lgkmcnt(8)\n"
"v_mfma_f32_16x16x32_bf16 v[208:211], v[168:171], v[100:103], v[208:211]\n"
"v_exp_f32 v133, v133\n"
"v_mfma_f32_16x16x32_bf16 v[212:215], v[168:171], v[108:111], v[212:215]\n"
"s_waitcnt vmcnt(6)\n"
"s_barrier\n"
"s_mov_b32 s48, 98304\n"
"s_add_u32 m0, s60, 32768\n"
"s_nop 0\n"
"buffer_load_dwordx4 v124, s[36:39], s48 offen lds\n"
"s_add_u32 m0, s60, 33792\n"
"s_nop 0\n"
"buffer_load_dwordx4 v125, s[36:39], s48 offen lds\n"
"s_add_u32 m0, s60, 34816\n"
"s_nop 0\n"
"buffer_load_dwordx4 v126, s[36:39], s48 offen lds\n"
"s_add_u32 m0, s60, 35840\n"
"s_nop 0\n"
"buffer_load_dwordx4 v127, s[36:39], s48 offen lds\n"
"s_cmp_eq_u32 s21, 0\n"
"s_cbranch_scc0 AT_nohf\n"
"s_lshl_b32 s50, s22, 7\n"
"s_lshl_b32 s51, s23, 2\n"
"s_add_u32 s50, s50, s51\n"
"s_add_u32 s50, s50, 0\n"
"v_mov_b32 v2, s50\n"
"v_mov_b32 v3, s61\n"
"s_mov_b64 exec, 1\n"
"global_store_dword v2, v3, s[6:7] sc1\n"
"s_mov_b64 exec, -1\n"
"AT_nohf:\n"
"v_xor_b32 v16, 0x30000, v16\n"
"v_xor_b32 v17, 0x30000, v17\n"
"v_xor_b32 v18, 0x30000, v18\n"
"v_xor_b32 v19, 0x30000, v19\n"
"ds_read_b128 v[160:163], v16 offset:0\n"
"v_mfma_f32_16x16x32_bf16 v[216:219], v[172:175], v[100:103], v[216:219]\n"
"v_exp_f32 v134, v134\n"
"v_mfma_f32_16x16x32_bf16 v[220:223], v[172:175], v[108:111], v[220:223]\n"
"v_mfma_f32_16x16x32_bf16 v[112:115], v[120:123], v[100:103], v[112:115]\n"
"v_exp_f32 v135, v135\n"
"v_mfma_f32_16x16x32_bf16 v[116:119], v[120:123], v[108:111], v[116:119]\n"
"ds_read_b128 v[164:167], v16 offset:4096\n"
"s_waitcnt lgkmcnt(6)\n"
"v_mfma_f32_16x16x32_bf16 v[224:227], v[176:179], v[100:103], v[224:227]\n"
"v_exp_f32 v148, v148\n"
"v_mfma_f32_16x16x32_bf16 v[228:231], v[176:179], v[108:111], v[228:231]\n"
"ds_read_b128 v[168:171], v16 offset:8192\n"
"v_mfma_f32_16x16x32_bf16 v[232:235], v[180:183], v[100:103], v[232:235]\n"
"v_exp_f32 v149, v149\n"
"v_mfma_f32_16x16x32_bf16 v[236:239], v[180:183], v[108:111], v[236:239]\n"
"ds_read_b128 v[172:175], v16 offset:12288\n"
"s_waitcnt lgkmcnt(4)\n"
"v_mfma_f32_16x16x32_bf16 v[240:243], v[184:187], v[100:103], v[240:243]\n"
"v_exp_f32 v150, v150\n"
"v_mfma_f32_16x16x32_bf16 v[244:247], v[184:187], v[108:111], v[244:247]\n"
"ds_read_b128 v[176:179], v17 offset:0\n"
"v_mfma_f32_16x16x32_bf16 v[248:251], v[188:191], v[100:103], v[248:251]\n"
"v_exp_f32 v151, v151\n"
"v_mfma_f32_16x16x32_bf16 v[252:255], v[188:191], v[108:111], v[252:255]\n"
"ds_read_b128 v[180:183], v17 offset:4096\n"
"s_waitcnt lgkmcnt(4)\n"
"v_mfma_f32_16x16x32_bf16 v[64:67], v[160:163], v[32:35], v[8:11]\n"
"v_cvt_pk_bf16_f32 v96, v128, v129\n"
"v_mfma_f32_16x16x32_bf16 v[80:83], v[160:163], v[48:51], v[12:15]\n"
"v_cvt_pk_bf16_f32 v97, v130, v131\n"
"ds_read_b128 v[184:187], v17 offset:8192\n"
"v_mfma_f32_16x16x32_bf16 v[68:71], v[164:167], v[32:35], v[8:11]\n"
"v_cvt_pk_bf16_f32 v98, v132, v133\n"
"v_mfma_f32_16x16x32_bf16 v[84:87], v[164:167], v[48:51], v[12:15]\n"
"v_cvt_pk_bf16_f32 v99, v134, v135\n"
"ds_read_b128 v[188:191], v17 offset:12288\n"
"s_waitcnt lgkmcnt(4)\n"
"v_mfma_f32_16x16x32_bf16 v[72:75], v[168:171], v[32:35], v[8:11]\n"
"v_cvt_pk_bf16_f32 v104, v144, v145\n"
"v_mfma_f32_16x16x32_bf16 v[88:91], v[168:171], v[48:51], v[12:15]\n"
"v_cvt_pk_bf16_f32 v105, v146, v147\n"
"ds_read_b128 v[160:163], v18 offset:0\n"
"v_mfma_f32_16x16x32_bf16 v[76:79], v[172:175], v[32:35], v[8:11]\n"
"v_cvt_pk_bf16_f32 v106, v148, v149\n"
"v_mfma_f32_16x16x32_bf16 v[92:95], v[172:175], v[48:51], v[12:15]\n"
"v_cvt_pk_bf16_f32 v107, v150, v151\n"
"ds_read_b128 v[164:167], v18 offset:4096\n"
"s_waitcnt lgkmcnt(4)\n"
"v_mfma_f32_16x16x32_bf16 v[64:67], v[176:179], v[36:39], v[64:67]\n"
"v_exp_f32 v136, v136\n"
"v_mfma_f32_16x16x32_bf16 v[80:83], v[176:179], v[52:55], v[80:83]\n"
"v_exp_f32 v137, v137\n"
"ds_read_b128 v[168:171], v18 offset:8192\n"
"v_mfma_f32_16x16x32_bf16 v[68:71], v[180:183], v[36:39], v[68:71]\n"
"v_exp_f32 v138, v138\n"
"v_mfma_f32_16x16x32_bf16 v[84:87], v[180:183], v[52:55], v[84:87]\n"
"v_exp_f32 v139, v139\n"
"ds_read_b128 v[172:175], v18 offset:12288\n"
"s_waitcnt lgkmcnt(4)\n"
"v_mfma_f32_16x16x32_bf16 v[72:75], v[184:187], v[36:39], v[72:75]\n"
"v_exp_f32 v152, v152\n"
"v_mfma_f32_16x16x32_bf16 v[88:91], v[184:187], v[52:55], v[88:91]\n"
"v_exp_f32 v153, v153\n"
"ds_read_b128 v[176:179], v19 offset:0\n"
"v_mfma_f32_16x16x32_bf16 v[76:79], v[188:191], v[36:39], v[76:79]\n"
"v_exp_f32 v154, v154\n"
"v_mfma_f32_16x16x32_bf16 v[92:95], v[188:191], v[52:55], v[92:95]\n"
"v_exp_f32 v155, v155\n"
"ds_read_b128 v[180:183], v19 offset:4096\n"
"s_waitcnt lgkmcnt(4)\n"
"v_mfma_f32_16x16x32_bf16 v[64:67], v[160:163], v[40:43], v[64:67]\n"
"v_exp_f32 v140, v140\n"
"v_mfma_f32_16x16x32_bf16 v[80:83], v[160:163], v[56:59], v[80:83]\n"
"v_exp_f32 v141, v141\n"
"ds_read_b128 v[184:187], v19 offset:8192\n"
"v_mfma_f32_16x16x32_bf16 v[68:71], v[164:167], v[40:43], v[68:71]\n"
"v_exp_f32 v142, v142\n"
"v_mfma_f32_16x16x32_bf16 v[84:87], v[164:167], v[56:59], v[84:87]\n"
"v_exp_f32 v143, v143\n"
"ds_read_b128 v[188:191], v19 offset:12288\n"
"s_waitcnt lgkmcnt(4)\n"
"v_mfma_f32_16x16x32_bf16 v[72:75], v[168:171], v[40:43], v[72:75]\n"
"v_exp_f32 v156, v156\n"
"v_mfma_f32_16x16x32_bf16 v[88:91], v[168:171], v[56:59], v[88:91]\n"
"v_exp_f32 v157, v157\n"
"ds_read_b64_tr_b16 v[160:161], v20 offset:32768\n"
"ds_read_b64_tr_b16 v[162:163], v20 offset:34816\n"
"v_mfma_f32_16x16x32_bf16 v[76:79], v[172:175], v[40:43], v[76:79]\n"
"v_exp_f32 v158, v158\n"
"v_mfma_f32_16x16x32_bf16 v[92:95], v[172:175], v[56:59], v[92:95]\n"
"v_exp_f32 v159, v159\n"
"ds_read_b64_tr_b16 v[164:165], v21 offset:32768\n"
"ds_read_b64_tr_b16 v[166:167], v21 offset:34816\n"
"s_waitcnt lgkmcnt(6)\n"
"v_mfma_f32_16x16x32_bf16 v[64:67], v[176:179], v[44:47], v[64:67]\n"
"v_cvt_pk_bf16_f32 v100, v136, v137\n"
"v_mfma_f32_16x16x32_bf16 v[80:83], v[176:179], v[60:63], v[80:83]\n"
"v_cvt_pk_bf16_f32 v101, v138, v139\n"
"ds_read_b64_tr_b16 v[168:169], v22 offset:32768\n"
"ds_read_b64_tr_b16 v[170:171], v22 offset:34816\n"
"v_mfma_f32_16x16x32_bf16 v[68:71], v[180:183], v[44:47], v[68:71]\n"
"v_cvt_pk_bf16_f32 v102, v140, v141\n"
"v_mfma_f32_16x16x32_bf16 v[84:87], v[180:183], v[60:63], v[84:87]\n"
"v_cvt_pk_bf16_f32 v103, v142, v143\n"
"ds_read_b64_tr_b16 v[172:173], v23 offset:32768\n"
"ds_read_b64_tr_b16 v[174:175], v23 offset:34816\n"
"s_waitcnt lgkmcnt(8)\n"
"v_mfma_f32_16x16x32_bf16 v[72:75], v[184:187], v[44:47], v[72:75]\n"
"v_cvt_pk_bf16_f32 v108, v152, v153\n"
"v_mfma_f32_16x16x32_bf16 v[88:91], v[184:187], v[60:63], v[88:91]\n"
"v_cvt_pk_bf16_f32 v109, v154, v155\n"
"ds_read_b64_tr_b16 v[176:177], v20 offset:40960\n"
"ds_read_b64_tr_b16 v[178:179], v20 offset:43008\n"
"v_mfma_f32_16x16x32_bf16 v[76:79], v[188:191], v[44:47], v[76:79]\n"
"v_cvt_pk_bf16_f32 v110, v156, v157\n"
"v_mfma_f32_16x16x32_bf16 v[92:95], v[188:191], v[60:63], v[92:95]\n"
"v_cvt_pk_bf16_f32 v111, v158, v159\n"
"ds_read_b64_tr_b16 v[180:181], v21 offset:40960\n"
"ds_read_b64_tr_b16 v[182:183], v21 offset:43008\n"
"s_nop 1\n"
"global_load_dwordx4 v[28:31], v25, s[94:95] offset:1024\n"
"s_waitcnt lgkmcnt(8)\n"
"v_mfma_f32_16x16x32_bf16 v[192:195], v[160:163], v[96:99], v[192:195]\n"
"v_mfma_f32_16x16x32_bf16 v[196:199], v[160:163], v[104:107], v[196:199]\n"
"ds_read_b64_tr_b16 v[184:185], v22 offset:40960\n"
"ds_read_b64_tr_b16 v[186:187], v22 offset:43008\n"
"v_mfma_f32_16x16x32_bf16 v[200:203], v[164:167], v[96:99], v[200:203]\n"
"v_mfma_f32_16x16x32_bf16 v[204:207], v[164:167], v[104:107], v[204:207]\n"
"ds_read_b64_tr_b16 v[188:189], v23 offset:40960\n"
"ds_read_b64_tr_b16 v[190:191], v23 offset:43008\n"
"s_waitcnt lgkmcnt(8)\n"
"v_mfma_f32_16x16x32_bf16 v[208:211], v[168:171], v[96:99], v[208:211]\n"
"v_exp_f32 v64, v64\n"
"v_mfma_f32_16x16x32_bf16 v[212:215], v[168:171], v[104:107], v[212:215]\n"
"ds_read_b64_tr_b16 v[160:161], v20 offset:36864\n"
"ds_read_b64_tr_b16 v[162:163], v20 offset:38912\n"
"v_mfma_f32_16x16x32_bf16 v[216:219], v[172:175], v[96:99], v[216:219]\n"
"v_exp_f32 v65, v65\n"
"v_mfma_f32_16x16x32_bf16 v[220:223], v[172:175], v[104:107], v[220:223]\n"
"v_mfma_f32_16x16x32_bf16 v[112:115], v[120:123], v[96:99], v[112:115]\n"
"v_exp_f32 v66, v66\n"
"v_mfma_f32_16x16x32_bf16 v[116:119], v[120:123], v[104:107], v[116:119]\n"
"ds_read_b64_tr_b16 v[164:165], v21 offset:36864\n"
"ds_read_b64_tr_b16 v[166:167], v21 offset:38912\n"
"s_waitcnt lgkmcnt(8)\n"
"v_mfma_f32_16x16x32_bf16 v[224:227], v[176:179], v[96:99], v[224:227]\n"
"v_exp_f32 v67, v67\n"
"v_mfma_f32_16x16x32_bf16 v[228:231], v[176:179], v[104:107], v[228:231]\n"
"ds_read_b64_tr_b16 v[168:169], v22 offset:36864\n"
"ds_read_b64_tr_b16 v[170:171], v22 offset:38912\n"
"v_mfma_f32_16x16x32_bf16 v[232:235], v[180:183], v[96:99], v[232:235]\n"
"v_exp_f32 v80, v80\n"
"v_mfma_f32_16x16x32_bf16 v[236:239], v[180:183], v[104:107], v[236:239]\n"
"ds_read_b64_tr_b16 v[172:173], v23 offset:36864\n"
"ds_read_b64_tr_b16 v[174:175], v23 offset:38912\n"
"s_waitcnt lgkmcnt(8)\n"
"v_mfma_f32_16x16x32_bf16 v[240:243], v[184:187], v[96:99], v[240:243]\n"
"v_exp_f32 v81, v81\n"
"v_mfma_f32_16x16x32_bf16 v[244:247], v[184:187], v[104:107], v[244:247]\n"
"ds_read_b64_tr_b16 v[176:177], v20 offset:45056\n"
"ds_read_b64_tr_b16 v[178:179], v20 offset:47104\n"
"v_mfma_f32_16x16x32_bf16 v[248:251], v[188:191], v[96:99], v[248:251]\n"
"v_exp_f32 v82, v82\n"
"v_mfma_f32_16x16x32_bf16 v[252:255], v[188:191], v[104:107], v[252:255]\n"
"ds_read_b64_tr_b16 v[180:181], v21 offset:45056\n"
"ds_read_b64_tr_b16 v[182:183], v21 offset:47104\n"
"s_waitcnt lgkmcnt(8)\n"
"v_mfma_f32_16x16x32_bf16 v[192:195], v[160:163], v[100:103], v[192:195]\n"
"v_exp_f32 v83, v83\n"
"v_mfma_f32_16x16x32_bf16 v[196:199], v[160:163], v[108:111], v[196:199]\n"
"ds_read_b64_tr_b16 v[184:185], v22 offset:45056\n"
"ds_read_b64_tr_b16 v[186:187], v22 offset:47104\n"
"v_mfma_f32_16x16x32_bf16 v[200:203], v[164:167], v[100:103], v[200:203]\n"
"v_exp_f32 v68, v68\n"
"v_mfma_f32_16x16x32_bf16 v[204:207], v[164:167], v[108:111], v[204:207]\n"
"ds_read_b64_tr_b16 v[188:189], v23 offset:45056\n"
"ds_read_b64_tr_b16 v[190:191], v23 offset:47104\n"
"s_waitcnt lgkmcnt(8)\n"
"v_mfma_f32_16x16x32_bf16 v[208:211], v[168:171], v[100:103], v[208:211]\n"
"v_exp_f32 v69, v69\n"
"v_mfma_f32_16x16x32_bf16 v[212:215], v[168:171], v[108:111], v[212:215]\n"
"s_waitcnt vmcnt(6)\n"
"s_barrier\n"
"s_mov_b32 s48, 114688\n"
"s_add_u32 m0, s60, 65536\n"
"s_nop 0\n"
"buffer_load_dwordx4 v124, s[36:39], s48 offen lds\n"
"s_add_u32 m0, s60, 66560\n"
"s_nop 0\n"
"buffer_load_dwordx4 v125, s[36:39], s48 offen lds\n"
"s_add_u32 m0, s60, 67584\n"
"s_nop 0\n"
"buffer_load_dwordx4 v126, s[36:39], s48 offen lds\n"
"s_add_u32 m0, s60, 68608\n"
"s_nop 0\n"
"buffer_load_dwordx4 v127, s[36:39], s48 offen lds\n"
"v_xor_b32 v16, 0x20000, v16\n"
"v_xor_b32 v17, 0x20000, v17\n"
"v_xor_b32 v18, 0x20000, v18\n"
"v_xor_b32 v19, 0x20000, v19\n"
"ds_read_b128 v[160:163], v16 offset:0\n"
"v_mfma_f32_16x16x32_bf16 v[216:219], v[172:175], v[100:103], v[216:219]\n"
"v_exp_f32 v70, v70\n"
"v_mfma_f32_16x16x32_bf16 v[220:223], v[172:175], v[108:111], v[220:223]\n"
"v_mfma_f32_16x16x32_bf16 v[112:115], v[120:123], v[100:103], v[112:115]\n"
"v_exp_f32 v71, v71\n"
"v_mfma_f32_16x16x32_bf16 v[116:119], v[120:123], v[108:111], v[116:119]\n"
"ds_read_b128 v[164:167], v16 offset:4096\n"
"s_waitcnt lgkmcnt(6)\n"
"v_mfma_f32_16x16x32_bf16 v[224:227], v[176:179], v[100:103], v[224:227]\n"
"v_exp_f32 v84, v84\n"
"v_mfma_f32_16x16x32_bf16 v[228:231], v[176:179], v[108:111], v[228:231]\n"
"ds_read_b128 v[168:171], v16 offset:8192\n"
"v_mfma_f32_16x16x32_bf16 v[232:235], v[180:183], v[100:103], v[232:235]\n"
"v_exp_f32 v85, v85\n"
"v_mfma_f32_16x16x32_bf16 v[236:239], v[180:183], v[108:111], v[236:239]\n"
"ds_read_b128 v[172:175], v16 offset:12288\n"
"s_waitcnt lgkmcnt(4)\n"
"v_mfma_f32_16x16x32_bf16 v[240:243], v[184:187], v[100:103], v[240:243]\n"
"v_exp_f32 v86, v86\n"
"v_mfma_f32_16x16x32_bf16 v[244:247], v[184:187], v[108:111], v[244:247]\n"
"ds_read_b128 v[176:179], v17 offset:0\n"
"v_mfma_f32_16x16x32_bf16 v[248:251], v[188:191], v[100:103], v[248:251]\n"
"v_exp_f32 v87, v87\n"
"v_mfma_f32_16x16x32_bf16 v[252:255], v[188:191], v[108:111], v[252:255]\n"
"ds_read_b128 v[180:183], v17 offset:4096\n"
"s_waitcnt lgkmcnt(4)\n"
"v_mfma_f32_16x16x32_bf16 v[128:131], v[160:163], v[32:35], v[8:11]\n"
"v_cvt_pk_bf16_f32 v96, v64, v65\n"
"v_mfma_f32_16x16x32_bf16 v[144:147], v[160:163], v[48:51], v[12:15]\n"
"v_cvt_pk_bf16_f32 v97, v66, v67\n"
"ds_read_b128 v[184:187], v17 offset:8192\n"
"v_mfma_f32_16x16x32_bf16 v[132:135], v[164:167], v[32:35], v[8:11]\n"
"v_cvt_pk_bf16_f32 v98, v68, v69\n"
"v_mfma_f32_16x16x32_bf16 v[148:151], v[164:167], v[48:51], v[12:15]\n"
"v_cvt_pk_bf16_f32 v99, v70, v71\n"
"ds_read_b128 v[188:191], v17 offset:12288\n"
"s_waitcnt lgkmcnt(4)\n"
"v_mfma_f32_16x16x32_bf16 v[136:139], v[168:171], v[32:35], v[8:11]\n"
"v_cvt_pk_bf16_f32 v104, v80, v81\n"
"v_mfma_f32_16x16x32_bf16 v[152:155], v[168:171], v[48:51], v[12:15]\n"
"v_cvt_pk_bf16_f32 v105, v82, v83\n"
"ds_read_b128 v[160:163], v18 offset:0\n"
"v_mfma_f32_16x16x32_bf16 v[140:143], v[172:175], v[32:35], v[8:11]\n"
"v_cvt_pk_bf16_f32 v106, v84, v85\n"
"v_mfma_f32_16x16x32_bf16 v[156:159], v[172:175], v[48:51], v[12:15]\n"
"v_cvt_pk_bf16_f32 v107, v86, v87\n"
"ds_read_b128 v[164:167], v18 offset:4096\n"
"s_waitcnt lgkmcnt(4)\n"
"v_mfma_f32_16x16x32_bf16 v[128:131], v[176:179], v[36:39], v[128:131]\n"
"v_exp_f32 v72, v72\n"
"v_mfma_f32_16x16x32_bf16 v[144:147], v[176:179], v[52:55], v[144:147]\n"
"v_exp_f32 v73, v73\n"
"ds_read_b128 v[168:171], v18 offset:8192\n"
"v_mfma_f32_16x16x32_bf16 v[132:135], v[180:183], v[36:39], v[132:135]\n"
"v_exp_f32 v74, v74\n"
"v_mfma_f32_16x16x32_bf16 v[148:151], v[180:183], v[52:55], v[148:151]\n"
"v_exp_f32 v75, v75\n"
"ds_read_b128 v[172:175], v18 offset:12288\n"
"s_waitcnt lgkmcnt(4)\n"
"v_mfma_f32_16x16x32_bf16 v[136:139], v[184:187], v[36:39], v[136:139]\n"
"v_exp_f32 v88, v88\n"
"v_mfma_f32_16x16x32_bf16 v[152:155], v[184:187], v[52:55], v[152:155]\n"
"v_exp_f32 v89, v89\n"
"ds_read_b128 v[176:179], v19 offset:0\n"
"v_mfma_f32_16x16x32_bf16 v[140:143], v[188:191], v[36:39], v[140:143]\n"
"v_exp_f32 v90, v90\n"
"v_mfma_f32_16x16x32_bf16 v[156:159], v[188:191], v[52:55], v[156:159]\n"
"v_exp_f32 v91, v91\n"
"ds_read_b128 v[180:183], v19 offset:4096\n"
"s_waitcnt lgkmcnt(4)\n"
"v_mfma_f32_16x16x32_bf16 v[128:131], v[160:163], v[40:43], v[128:131]\n"
"v_exp_f32 v76, v76\n"
"v_mfma_f32_16x16x32_bf16 v[144:147], v[160:163], v[56:59], v[144:147]\n"
"v_exp_f32 v77, v77\n"
"ds_read_b128 v[184:187], v19 offset:8192\n"
"v_mfma_f32_16x16x32_bf16 v[132:135], v[164:167], v[40:43], v[132:135]\n"
"v_exp_f32 v78, v78\n"
"v_mfma_f32_16x16x32_bf16 v[148:151], v[164:167], v[56:59], v[148:151]\n"
"v_exp_f32 v79, v79\n"
"ds_read_b128 v[188:191], v19 offset:12288\n"
"s_waitcnt lgkmcnt(4)\n"
"v_mfma_f32_16x16x32_bf16 v[136:139], v[168:171], v[40:43], v[136:139]\n"
"v_exp_f32 v92, v92\n"
"v_mfma_f32_16x16x32_bf16 v[152:155], v[168:171], v[56:59], v[152:155]\n"
"v_exp_f32 v93, v93\n"
"v_xor_b32 v20, 0x30000, v20\n"
"v_xor_b32 v21, 0x30000, v21\n"
"v_xor_b32 v22, 0x30000, v22\n"
"v_xor_b32 v23, 0x30000, v23\n"
"ds_read_b64_tr_b16 v[160:161], v20 offset:0\n"
"ds_read_b64_tr_b16 v[162:163], v20 offset:2048\n"
"v_mfma_f32_16x16x32_bf16 v[140:143], v[172:175], v[40:43], v[140:143]\n"
"v_exp_f32 v94, v94\n"
"v_mfma_f32_16x16x32_bf16 v[156:159], v[172:175], v[56:59], v[156:159]\n"
"v_exp_f32 v95, v95\n"
"ds_read_b64_tr_b16 v[164:165], v21 offset:0\n"
"ds_read_b64_tr_b16 v[166:167], v21 offset:2048\n"
"s_waitcnt lgkmcnt(6)\n"
"v_mfma_f32_16x16x32_bf16 v[128:131], v[176:179], v[44:47], v[128:131]\n"
"v_cvt_pk_bf16_f32 v100, v72, v73\n"
"v_mfma_f32_16x16x32_bf16 v[144:147], v[176:179], v[60:63], v[144:147]\n"
"v_cvt_pk_bf16_f32 v101, v74, v75\n"
"ds_read_b64_tr_b16 v[168:169], v22 offset:0\n"
"ds_read_b64_tr_b16 v[170:171], v22 offset:2048\n"
"v_mfma_f32_16x16x32_bf16 v[132:135], v[180:183], v[44:47], v[132:135]\n"
"v_cvt_pk_bf16_f32 v102, v76, v77\n"
"v_mfma_f32_16x16x32_bf16 v[148:151], v[180:183], v[60:63], v[148:151]\n"
"v_cvt_pk_bf16_f32 v103, v78, v79\n"
"ds_read_b64_tr_b16 v[172:173], v23 offset:0\n"
"ds_read_b64_tr_b16 v[174:175], v23 offset:2048\n"
"s_waitcnt lgkmcnt(8)\n"
"v_mfma_f32_16x16x32_bf16 v[136:139], v[184:187], v[44:47], v[136:139]\n"
"v_cvt_pk_bf16_f32 v108, v88, v89\n"
"v_mfma_f32_16x16x32_bf16 v[152:155], v[184:187], v[60:63], v[152:155]\n"
"v_cvt_pk_bf16_f32 v109, v90, v91\n"
"ds_read_b64_tr_b16 v[176:177], v20 offset:8192\n"
"ds_read_b64_tr_b16 v[178:179], v20 offset:10240\n"
"v_mfma_f32_16x16x32_bf16 v[140:143], v[188:191], v[44:47], v[140:143]\n"
"v_cvt_pk_bf16_f32 v110, v92, v93\n"
"v_mfma_f32_16x16x32_bf16 v[156:159], v[188:191], v[60:63], v[156:159]\n"
"v_cvt_pk_bf16_f32 v111, v94, v95\n"
"ds_read_b64_tr_b16 v[180:181], v21 offset:8192\n"
"ds_read_b64_tr_b16 v[182:183], v21 offset:10240\n"
"s_nop 1\n"
"global_load_dwordx4 v[28:31], v24, s[92:93] offset:2048\n"
"s_waitcnt lgkmcnt(8)\n"
"v_mfma_f32_16x16x32_bf16 v[192:195], v[160:163], v[96:99], v[192:195]\n"
"v_mfma_f32_16x16x32_bf16 v[196:199], v[160:163], v[104:107], v[196:199]\n"
"ds_read_b64_tr_b16 v[184:185], v22 offset:8192\n"
"ds_read_b64_tr_b16 v[186:187], v22 offset:10240\n"
"v_mfma_f32_16x16x32_bf16 v[200:203], v[164:167], v[96:99], v[200:203]\n"
"v_mfma_f32_16x16x32_bf16 v[204:207], v[164:167], v[104:107], v[204:207]\n"
"ds_read_b64_tr_b16 v[188:189], v23 offset:8192\n"
"ds_read_b64_tr_b16 v[190:191], v23 offset:10240\n"
"s_waitcnt lgkmcnt(8)\n"
"v_mfma_f32_16x16x32_bf16 v[208:211], v[168:171], v[96:99], v[208:211]\n"
"v_exp_f32 v128, v128\n"
"v_mfma_f32_16x16x32_bf16 v[212:215], v[168:171], v[104:107], v[212:215]\n"
"ds_read_b64_tr_b16 v[160:161], v20 offset:4096\n"
"ds_read_b64_tr_b16 v[162:163], v20 offset:6144\n"
"v_mfma_f32_16x16x32_bf16 v[216:219], v[172:175], v[96:99], v[216:219]\n"
"v_exp_f32 v129, v129\n"
"v_mfma_f32_16x16x32_bf16 v[220:223], v[172:175], v[104:107], v[220:223]\n"
"v_mfma_f32_16x16x32_bf16 v[112:115], v[120:123], v[96:99], v[112:115]\n"
"v_exp_f32 v130, v130\n"
"v_mfma_f32_16x16x32_bf16 v[116:119], v[120:123], v[104:107], v[116:119]\n"
"ds_read_b64_tr_b16 v[164:165], v21 offset:4096\n"
"ds_read_b64_tr_b16 v[166:167], v21 offset:6144\n"
"s_waitcnt lgkmcnt(8)\n"
"v_mfma_f32_16x16x32_bf16 v[224:227], v[176:179], v[96:99], v[224:227]\n"
"v_exp_f32 v131, v131\n"
"v_mfma_f32_16x16x32_bf16 v[228:231], v[176:179], v[104:107], v[228:231]\n"
"ds_read_b64_tr_b16 v[168:169], v22 offset:4096\n"
"ds_read_b64_tr_b16 v[170:171], v22 offset:6144\n"
"v_mfma_f32_16x16x32_bf16 v[232:235], v[180:183], v[96:99], v[232:235]\n"
"v_exp_f32 v144, v144\n"
"v_mfma_f32_16x16x32_bf16 v[236:239], v[180:183], v[104:107], v[236:239]\n"
"ds_read_b64_tr_b16 v[172:173], v23 offset:4096\n"
"ds_read_b64_tr_b16 v[174:175], v23 offset:6144\n"
"s_waitcnt lgkmcnt(8)\n"
"v_mfma_f32_16x16x32_bf16 v[240:243], v[184:187], v[96:99], v[240:243]\n"
"v_exp_f32 v145, v145\n"
"v_mfma_f32_16x16x32_bf16 v[244:247], v[184:187], v[104:107], v[244:247]\n"
"ds_read_b64_tr_b16 v[176:177], v20 offset:12288\n"
"ds_read_b64_tr_b16 v[178:179], v20 offset:14336\n"
"v_mfma_f32_16x16x32_bf16 v[248:251], v[188:191], v[96:99], v[248:251]\n"
"v_exp_f32 v146, v146\n"
"v_mfma_f32_16x16x32_bf16 v[252:255], v[188:191], v[104:107], v[252:255]\n"
"ds_read_b64_tr_b16 v[180:181], v21 offset:12288\n"
"ds_read_b64_tr_b16 v[182:183], v21 offset:14336\n"
"s_waitcnt lgkmcnt(8)\n"
"v_mfma_f32_16x16x32_bf16 v[192:195], v[160:163], v[100:103], v[192:195]\n"
"v_exp_f32 v147, v147\n"
"v_mfma_f32_16x16x32_bf16 v[196:199], v[160:163], v[108:111], v[196:199]\n"
"ds_read_b64_tr_b16 v[184:185], v22 offset:12288\n"
"ds_read_b64_tr_b16 v[186:187], v22 offset:14336\n"
"v_mfma_f32_16x16x32_bf16 v[200:203], v[164:167], v[100:103], v[200:203]\n"
"v_exp_f32 v132, v132\n"
"v_mfma_f32_16x16x32_bf16 v[204:207], v[164:167], v[108:111], v[204:207]\n"
"ds_read_b64_tr_b16 v[188:189], v23 offset:12288\n"
"ds_read_b64_tr_b16 v[190:191], v23 offset:14336\n"
"s_waitcnt lgkmcnt(8)\n"
"v_mfma_f32_16x16x32_bf16 v[208:211], v[168:171], v[100:103], v[208:211]\n"
"v_exp_f32 v133, v133\n"
"v_mfma_f32_16x16x32_bf16 v[212:215], v[168:171], v[108:111], v[212:215]\n"
"s_waitcnt vmcnt(6)\n"
"s_barrier\n"
"ds_read_b128 v[160:163], v16 offset:32768\n"
"v_mfma_f32_16x16x32_bf16 v[216:219], v[172:175], v[100:103], v[216:219]\n"
"v_exp_f32 v134, v134\n"
"v_mfma_f32_16x16x32_bf16 v[220:223], v[172:175], v[108:111], v[220:223]\n"
"v_mfma_f32_16x16x32_bf16 v[112:115], v[120:123], v[100:103], v[112:115]\n"
"v_exp_f32 v135, v135\n"
"v_mfma_f32_16x16x32_bf16 v[116:119], v[120:123], v[108:111], v[116:119]\n"
"ds_read_b128 v[164:167], v16 offset:36864\n"
"s_waitcnt lgkmcnt(6)\n"
"v_mfma_f32_16x16x32_bf16 v[224:227], v[176:179], v[100:103], v[224:227]\n"
"v_exp_f32 v148, v148\n"
"v_mfma_f32_16x16x32_bf16 v[228:231], v[176:179], v[108:111], v[228:231]\n"
"ds_read_b128 v[168:171], v16 offset:40960\n"
"v_mfma_f32_16x16x32_bf16 v[232:235], v[180:183], v[100:103], v[232:235]\n"
"v_exp_f32 v149, v149\n"
"v_mfma_f32_16x16x32_bf16 v[236:239], v[180:183], v[108:111], v[236:239]\n"
"ds_read_b128 v[172:175], v16 offset:45056\n"
"s_waitcnt lgkmcnt(4)\n"
"v_mfma_f32_16x16x32_bf16 v[240:243], v[184:187], v[100:103], v[240:243]\n"
"v_exp_f32 v150, v150\n"
"v_mfma_f32_16x16x32_bf16 v[244:247], v[184:187], v[108:111], v[244:247]\n"
"ds_read_b128 v[176:179], v17 offset:32768\n"
"v_mfma_f32_16x16x32_bf16 v[248:251], v[188:191], v[100:103], v[248:251]\n"
"v_exp_f32 v151, v151\n"
"v_mfma_f32_16x16x32_bf16 v[252:255], v[188:191], v[108:111], v[252:255]\n"
"ds_read_b128 v[180:183], v17 offset:36864\n"
"s_waitcnt lgkmcnt(4)\n"
"v_mfma_f32_16x16x32_bf16 v[64:67], v[160:163], v[32:35], v[8:11]\n"
"v_cvt_pk_bf16_f32 v96, v128, v129\n"
"v_mfma_f32_16x16x32_bf16 v[80:83], v[160:163], v[48:51], v[12:15]\n"
"v_cvt_pk_bf16_f32 v97, v130, v131\n"
"ds_read_b128 v[184:187], v17 offset:40960\n"
"v_mfma_f32_16x16x32_bf16 v[68:71], v[164:167], v[32:35], v[8:11]\n"
"v_cvt_pk_bf16_f32 v98, v132, v133\n"
"v_mfma_f32_16x16x32_bf16 v[84:87], v[164:167], v[48:51], v[12:15]\n"
"v_cvt_pk_bf16_f32 v99, v134, v135\n"
"ds_read_b128 v[188:191], v17 offset:45056\n"
"s_waitcnt lgkmcnt(4)\n"
"v_mfma_f32_16x16x32_bf16 v[72:75], v[168:171], v[32:35], v[8:11]\n"
"v_cvt_pk_bf16_f32 v104, v144, v145\n"
"v_mfma_f32_16x16x32_bf16 v[88:91], v[168:171], v[48:51], v[12:15]\n"
"v_cvt_pk_bf16_f32 v105, v146, v147\n"
"ds_read_b128 v[160:163], v18 offset:32768\n"
"v_mfma_f32_16x16x32_bf16 v[76:79], v[172:175], v[32:35], v[8:11]\n"
"v_cvt_pk_bf16_f32 v106, v148, v149\n"
"v_mfma_f32_16x16x32_bf16 v[92:95], v[172:175], v[48:51], v[12:15]\n"
"v_cvt_pk_bf16_f32 v107, v150, v151\n"
"ds_read_b128 v[164:167], v18 offset:36864\n"
"s_waitcnt lgkmcnt(4)\n"
"v_mfma_f32_16x16x32_bf16 v[64:67], v[176:179], v[36:39], v[64:67]\n"
"v_exp_f32 v136, v136\n"
"v_mfma_f32_16x16x32_bf16 v[80:83], v[176:179], v[52:55], v[80:83]\n"
"v_exp_f32 v137, v137\n"
"ds_read_b128 v[168:171], v18 offset:40960\n"
"v_mfma_f32_16x16x32_bf16 v[68:71], v[180:183], v[36:39], v[68:71]\n"
"v_exp_f32 v138, v138\n"
"v_mfma_f32_16x16x32_bf16 v[84:87], v[180:183], v[52:55], v[84:87]\n"
"v_exp_f32 v139, v139\n"
"ds_read_b128 v[172:175], v18 offset:45056\n"
"s_waitcnt lgkmcnt(4)\n"
"v_mfma_f32_16x16x32_bf16 v[72:75], v[184:187], v[36:39], v[72:75]\n"
"v_exp_f32 v152, v152\n"
"v_mfma_f32_16x16x32_bf16 v[88:91], v[184:187], v[52:55], v[88:91]\n"
"v_exp_f32 v153, v153\n"
"ds_read_b128 v[176:179], v19 offset:32768\n"
"v_mfma_f32_16x16x32_bf16 v[76:79], v[188:191], v[36:39], v[76:79]\n"
"v_exp_f32 v154, v154\n"
"v_mfma_f32_16x16x32_bf16 v[92:95], v[188:191], v[52:55], v[92:95]\n"
"v_exp_f32 v155, v155\n"
"ds_read_b128 v[180:183], v19 offset:36864\n"
"s_waitcnt lgkmcnt(4)\n"
"v_mfma_f32_16x16x32_bf16 v[64:67], v[160:163], v[40:43], v[64:67]\n"
"v_exp_f32 v140, v140\n"
"v_mfma_f32_16x16x32_bf16 v[80:83], v[160:163], v[56:59], v[80:83]\n"
"v_exp_f32 v141, v141\n"
"ds_read_b128 v[184:187], v19 offset:40960\n"
"v_mfma_f32_16x16x32_bf16 v[68:71], v[164:167], v[40:43], v[68:71]\n"
"v_exp_f32 v142, v142\n"
"v_mfma_f32_16x16x32_bf16 v[84:87], v[164:167], v[56:59], v[84:87]\n"
"v_exp_f32 v143, v143\n"
"ds_read_b128 v[188:191], v19 offset:45056\n"
"s_waitcnt lgkmcnt(4)\n"
"v_mfma_f32_16x16x32_bf16 v[72:75], v[168:171], v[40:43], v[72:75]\n"
"v_exp_f32 v156, v156\n"
"v_mfma_f32_16x16x32_bf16 v[88:91], v[168:171], v[56:59], v[88:91]\n"
"v_exp_f32 v157, v157\n"
"v_xor_b32 v20, 0x20000, v20\n"
"v_xor_b32 v21, 0x20000, v21\n"
"v_xor_b32 v22, 0x20000, v22\n"
"v_xor_b32 v23, 0x20000, v23\n"
"ds_read_b64_tr_b16 v[160:161], v20 offset:0\n"
"ds_read_b64_tr_b16 v[162:163], v20 offset:2048\n"
"v_mfma_f32_16x16x32_bf16 v[76:79], v[172:175], v[40:43], v[76:79]\n"
"v_exp_f32 v158, v158\n"
"v_mfma_f32_16x16x32_bf16 v[92:95], v[172:175], v[56:59], v[92:95]\n"
"v_exp_f32 v159, v159\n"
"ds_read_b64_tr_b16 v[164:165], v21 offset:0\n"
"ds_read_b64_tr_b16 v[166:167], v21 offset:2048\n"
"s_waitcnt lgkmcnt(6)\n"
"v_mfma_f32_16x16x32_bf16 v[64:67], v[176:179], v[44:47], v[64:67]\n"
"v_cvt_pk_bf16_f32 v100, v136, v137\n"
"v_mfma_f32_16x16x32_bf16 v[80:83], v[176:179], v[60:63], v[80:83]\n"
"v_cvt_pk_bf16_f32 v101, v138, v139\n"
"ds_read_b64_tr_b16 v[168:169], v22 offset:0\n"
"ds_read_b64_tr_b16 v[170:171], v22 offset:2048\n"
"v_mfma_f32_16x16x32_bf16 v[68:71], v[180:183], v[44:47], v[68:71]\n"
"v_cvt_pk_bf16_f32 v102, v140, v141\n"
"v_mfma_f32_16x16x32_bf16 v[84:87], v[180:183], v[60:63], v[84:87]\n"
"v_cvt_pk_bf16_f32 v103, v142, v143\n"
"ds_read_b64_tr_b16 v[172:173], v23 offset:0\n"
"ds_read_b64_tr_b16 v[174:175], v23 offset:2048\n"
"s_waitcnt lgkmcnt(8)\n"
"v_mfma_f32_16x16x32_bf16 v[72:75], v[184:187], v[44:47], v[72:75]\n"
"v_cvt_pk_bf16_f32 v108, v152, v153\n"
"v_mfma_f32_16x16x32_bf16 v[88:91], v[184:187], v[60:63], v[88:91]\n"
"v_cvt_pk_bf16_f32 v109, v154, v155\n"
"ds_read_b64_tr_b16 v[176:177], v20 offset:8192\n"
"ds_read_b64_tr_b16 v[178:179], v20 offset:10240\n"
"v_mfma_f32_16x16x32_bf16 v[76:79], v[188:191], v[44:47], v[76:79]\n"
"v_cvt_pk_bf16_f32 v110, v156, v157\n"
"v_mfma_f32_16x16x32_bf16 v[92:95], v[188:191], v[60:63], v[92:95]\n"
"v_cvt_pk_bf16_f32 v111, v158, v159\n"
"ds_read_b64_tr_b16 v[180:181], v21 offset:8192\n"
"ds_read_b64_tr_b16 v[182:183], v21 offset:10240\n"
"s_nop 1\n"
"global_load_dwordx4 v[28:31], v25, s[94:95] offset:2048\n"
"s_waitcnt lgkmcnt(8)\n"
"v_mfma_f32_16x16x32_bf16 v[192:195], v[160:163], v[96:99], v[192:195]\n"
"v_mfma_f32_16x16x32_bf16 v[196:199], v[160:163], v[104:107], v[196:199]\n"
"ds_read_b64_tr_b16 v[184:185], v22 offset:8192\n"
"ds_read_b64_tr_b16 v[186:187], v22 offset:10240\n"
"v_mfma_f32_16x16x32_bf16 v[200:203], v[164:167], v[96:99], v[200:203]\n"
"v_mfma_f32_16x16x32_bf16 v[204:207], v[164:167], v[104:107], v[204:207]\n"
"ds_read_b64_tr_b16 v[188:189], v23 offset:8192\n"
"ds_read_b64_tr_b16 v[190:191], v23 offset:10240\n"
"s_waitcnt lgkmcnt(8)\n"
"v_mfma_f32_16x16x32_bf16 v[208:211], v[168:171], v[96:99], v[208:211]\n"
"v_exp_f32 v64, v64\n"
"v_mfma_f32_16x16x32_bf16 v[212:215], v[168:171], v[104:107], v[212:215]\n"
"ds_read_b64_tr_b16 v[160:161], v20 offset:4096\n"
"ds_read_b64_tr_b16 v[162:163], v20 offset:6144\n"
"v_mfma_f32_16x16x32_bf16 v[216:219], v[172:175], v[96:99], v[216:219]\n"
"v_exp_f32 v65, v65\n"
"v_mfma_f32_16x16x32_bf16 v[220:223], v[172:175], v[104:107], v[220:223]\n"
"v_mfma_f32_16x16x32_bf16 v[112:115], v[120:123], v[96:99], v[112:115]\n"
"v_exp_f32 v66, v66\n"
"v_mfma_f32_16x16x32_bf16 v[116:119], v[120:123], v[104:107], v[116:119]\n"
"ds_read_b64_tr_b16 v[164:165], v21 offset:4096\n"
"ds_read_b64_tr_b16 v[166:167], v21 offset:6144\n"
"s_waitcnt lgkmcnt(8)\n"
"v_mfma_f32_16x16x32_bf16 v[224:227], v[176:179], v[96:99], v[224:227]\n"
"v_exp_f32 v67, v67\n"
"v_mfma_f32_16x16x32_bf16 v[228:231], v[176:179], v[104:107], v[228:231]\n"
"ds_read_b64_tr_b16 v[168:169], v22 offset:4096\n"
"ds_read_b64_tr_b16 v[170:171], v22 offset:6144\n"
"v_mfma_f32_16x16x32_bf16 v[232:235], v[180:183], v[96:99], v[232:235]\n"
"v_exp_f32 v80, v80\n"
"v_mfma_f32_16x16x32_bf16 v[236:239], v[180:183], v[104:107], v[236:239]\n"
"ds_read_b64_tr_b16 v[172:173], v23 offset:4096\n"
"ds_read_b64_tr_b16 v[174:175], v23 offset:6144\n"
"s_waitcnt lgkmcnt(8)\n"
"v_mfma_f32_16x16x32_bf16 v[240:243], v[184:187], v[96:99], v[240:243]\n"
"v_exp_f32 v81, v81\n"
"v_mfma_f32_16x16x32_bf16 v[244:247], v[184:187], v[104:107], v[244:247]\n"
"ds_read_b64_tr_b16 v[176:177], v20 offset:12288\n"
"ds_read_b64_tr_b16 v[178:179], v20 offset:14336\n"
"v_mfma_f32_16x16x32_bf16 v[248:251], v[188:191], v[96:99], v[248:251]\n"
"v_exp_f32 v82, v82\n"
"v_mfma_f32_16x16x32_bf16 v[252:255], v[188:191], v[104:107], v[252:255]\n"
"ds_read_b64_tr_b16 v[180:181], v21 offset:12288\n"
"ds_read_b64_tr_b16 v[182:183], v21 offset:14336\n"
"s_waitcnt lgkmcnt(8)\n"
"v_mfma_f32_16x16x32_bf16 v[192:195], v[160:163], v[100:103], v[192:195]\n"
"v_exp_f32 v83, v83\n"
"v_mfma_f32_16x16x32_bf16 v[196:199], v[160:163], v[108:111], v[196:199]\n"
"ds_read_b64_tr_b16 v[184:185], v22 offset:12288\n"
"ds_read_b64_tr_b16 v[186:187], v22 offset:14336\n"
"v_mfma_f32_16x16x32_bf16 v[200:203], v[164:167], v[100:103], v[200:203]\n"
"v_exp_f32 v68, v68\n"
"v_mfma_f32_16x16x32_bf16 v[204:207], v[164:167], v[108:111], v[204:207]\n"
"ds_read_b64_tr_b16 v[188:189], v23 offset:12288\n"
"ds_read_b64_tr_b16 v[190:191], v23 offset:14336\n"
"s_waitcnt lgkmcnt(8)\n"
"v_mfma_f32_16x16x32_bf16 v[208:211], v[168:171], v[100:103], v[208:211]\n"
"v_exp_f32 v69, v69\n"
"v_mfma_f32_16x16x32_bf16 v[212:215], v[168:171], v[108:111], v[212:215]\n"
"s_waitcnt vmcnt(2)\n"
"s_barrier\n"
"s_and_b32 s50, s20, 1\n"
"s_lshl_b32 s50, s50, 2\n"
"s_lshr_b32 s51, s20, 3\n"
"s_and_b32 s51, s51, 3\n"
"s_add_u32 s50, s50, s51\n"
"s_lshl_b32 s50, s50, 7\n"
"v_and_b32 v27, 31, v1\n"
"v_lshlrev_b32 v27, 2, v27\n"
"v_add_u32 v27, s50, v27\n"
"global_load_dword v27, v27, s[6:7] offset:0 sc1\n"
"v_xor_b32 v16, 0x10000, v16\n"
"v_xor_b32 v17, 0x10000, v17\n"
"v_xor_b32 v18, 0x10000, v18\n"
"v_xor_b32 v19, 0x10000, v19\n"
"ds_read_b128 v[160:163], v16 offset:0\n"
"v_mfma_f32_16x16x32_bf16 v[216:219], v[172:175], v[100:103], v[216:219]\n"
"v_exp_f32 v70, v70\n"
"v_mfma_f32_16x16x32_bf16 v[220:223], v[172:175], v[108:111], v[220:223]\n"
"v_mfma_f32_16x16x32_bf16 v[112:115], v[120:123], v[100:103], v[112:115]\n"
"v_exp_f32 v71, v71\n"
"v_mfma_f32_16x16x32_bf16 v[116:119], v[120:123], v[108:111], v[116:119]\n"
"ds_read_b128 v[164:167], v16 offset:4096\n"
"s_waitcnt lgkmcnt(6)\n"
"v_mfma_f32_16x16x32_bf16 v[224:227], v[176:179], v[100:103], v[224:227]\n"
"v_exp_f32 v84, v84\n"
"v_mfma_f32_16x16x32_bf16 v[228:231], v[176:179], v[108:111], v[228:231]\n"
"ds_read_b128 v[168:171], v16 offset:8192\n"
"v_mfma_f32_16x16x32_bf16 v[232:235], v[180:183], v[100:103], v[232:235]\n"
"v_exp_f32 v85, v85\n"
"v_mfma_f32_16x16x32_bf16 v[236:239], v[180:183], v[108:111], v[236:239]\n"
"ds_read_b128 v[172:175], v16 offset:12288\n"
"s_waitcnt lgkmcnt(4)\n"
"v_mfma_f32_16x16x32_bf16 v[240:243], v[184:187], v[100:103], v[240:243]\n"
"v_exp_f32 v86, v86\n"
"v_mfma_f32_16x16x32_bf16 v[244:247], v[184:187], v[108:111], v[244:247]\n"
"ds_read_b128 v[176:179], v17 offset:0\n"
"v_mfma_f32_16x16x32_bf16 v[248:251], v[188:191], v[100:103], v[248:251]\n"
"v_exp_f32 v87, v87\n"
"v_mfma_f32_16x16x32_bf16 v[252:255], v[188:191], v[108:111], v[252:255]\n"
"ds_read_b128 v[180:183], v17 offset:4096\n"
"s_waitcnt lgkmcnt(4)\n"
"v_mfma_f32_16x16x32_bf16 v[128:131], v[160:163], v[32:35], v[8:11]\n"
"v_cvt_pk_bf16_f32 v96, v64, v65\n"
"v_mfma_f32_16x16x32_bf16 v[144:147], v[160:163], v[48:51], v[12:15]\n"
"v_cvt_pk_bf16_f32 v97, v66, v67\n"
"ds_read_b128 v[184:187], v17 offset:8192\n"
"v_mfma_f32_16x16x32_bf16 v[132:135], v[164:167], v[32:35], v[8:11]\n"
"v_cvt_pk_bf16_f32 v98, v68, v69\n"
"v_mfma_f32_16x16x32_bf16 v[148:151], v[164:167], v[48:51], v[12:15]\n"
"v_cvt_pk_bf16_f32 v99, v70, v71\n"
"ds_read_b128 v[188:191], v17 offset:12288\n"
"s_waitcnt lgkmcnt(4)\n"
"v_mfma_f32_16x16x32_bf16 v[136:139], v[168:171], v[32:35], v[8:11]\n"
"v_cvt_pk_bf16_f32 v104, v80, v81\n"
"v_mfma_f32_16x16x32_bf16 v[152:155], v[168:171], v[48:51], v[12:15]\n"
"v_cvt_pk_bf16_f32 v105, v82, v83\n"
"ds_read_b128 v[160:163], v18 offset:0\n"
"v_mfma_f32_16x16x32_bf16 v[140:143], v[172:175], v[32:35], v[8:11]\n"
"v_cvt_pk_bf16_f32 v106, v84, v85\n"
"v_mfma_f32_16x16x32_bf16 v[156:159], v[172:175], v[48:51], v[12:15]\n"
"v_cvt_pk_bf16_f32 v107, v86, v87\n"
"ds_read_b128 v[164:167], v18 offset:4096\n"
"s_waitcnt lgkmcnt(4)\n"
"v_mfma_f32_16x16x32_bf16 v[128:131], v[176:179], v[36:39], v[128:131]\n"
"v_exp_f32 v72, v72\n"
"v_mfma_f32_16x16x32_bf16 v[144:147], v[176:179], v[52:55], v[144:147]\n"
"v_exp_f32 v73, v73\n"
"ds_read_b128 v[168:171], v18 offset:8192\n"
"v_mfma_f32_16x16x32_bf16 v[132:135], v[180:183], v[36:39], v[132:135]\n"
"v_exp_f32 v74, v74\n"
"v_mfma_f32_16x16x32_bf16 v[148:151], v[180:183], v[52:55], v[148:151]\n"
"v_exp_f32 v75, v75\n"
"ds_read_b128 v[172:175], v18 offset:12288\n"
"s_waitcnt lgkmcnt(4)\n"
"v_mfma_f32_16x16x32_bf16 v[136:139], v[184:187], v[36:39], v[136:139]\n"
"v_exp_f32 v88, v88\n"
"v_mfma_f32_16x16x32_bf16 v[152:155], v[184:187], v[52:55], v[152:155]\n"
"v_exp_f32 v89, v89\n"
"ds_read_b128 v[176:179], v19 offset:0\n"
"v_mfma_f32_16x16x32_bf16 v[140:143], v[188:191], v[36:39], v[140:143]\n"
"v_exp_f32 v90, v90\n"
"v_mfma_f32_16x16x32_bf16 v[156:159], v[188:191], v[52:55], v[156:159]\n"
"v_exp_f32 v91, v91\n"
"ds_read_b128 v[180:183], v19 offset:4096\n"
"s_waitcnt lgkmcnt(4)\n"
"v_mfma_f32_16x16x32_bf16 v[128:131], v[160:163], v[40:43], v[128:131]\n"
"v_exp_f32 v76, v76\n"
"v_mfma_f32_16x16x32_bf16 v[144:147], v[160:163], v[56:59], v[144:147]\n"
"v_exp_f32 v77, v77\n"
"ds_read_b128 v[184:187], v19 offset:8192\n"
"v_mfma_f32_16x16x32_bf16 v[132:135], v[164:167], v[40:43], v[132:135]\n"
"v_exp_f32 v78, v78\n"
"v_mfma_f32_16x16x32_bf16 v[148:151], v[164:167], v[56:59], v[148:151]\n"
"v_exp_f32 v79, v79\n"
"ds_read_b128 v[188:191], v19 offset:12288\n"
"s_waitcnt lgkmcnt(4)\n"
"v_mfma_f32_16x16x32_bf16 v[136:139], v[168:171], v[40:43], v[136:139]\n"
"v_exp_f32 v92, v92\n"
"v_mfma_f32_16x16x32_bf16 v[152:155], v[168:171], v[56:59], v[152:155]\n"
"v_exp_f32 v93, v93\n"
"ds_read_b64_tr_b16 v[160:161], v20 offset:32768\n"
"ds_read_b64_tr_b16 v[162:163], v20 offset:34816\n"
"v_mfma_f32_16x16x32_bf16 v[140:143], v[172:175], v[40:43], v[140:143]\n"
"v_exp_f32 v94, v94\n"
"v_mfma_f32_16x16x32_bf16 v[156:159], v[172:175], v[56:59], v[156:159]\n"
"v_exp_f32 v95, v95\n"
"ds_read_b64_tr_b16 v[164:165], v21 offset:32768\n"
"ds_read_b64_tr_b16 v[166:167], v21 offset:34816\n"
"s_waitcnt lgkmcnt(6)\n"
"v_mfma_f32_16x16x32_bf16 v[128:131], v[176:179], v[44:47], v[128:131]\n"
"v_cvt_pk_bf16_f32 v100, v72, v73\n"
"v_mfma_f32_16x16x32_bf16 v[144:147], v[176:179], v[60:63], v[144:147]\n"
"v_cvt_pk_bf16_f32 v101, v74, v75\n"
"ds_read_b64_tr_b16 v[168:169], v22 offset:32768\n"
"ds_read_b64_tr_b16 v[170:171], v22 offset:34816\n"
"v_mfma_f32_16x16x32_bf16 v[132:135], v[180:183], v[44:47], v[132:135]\n"
"v_cvt_pk_bf16_f32 v102, v76, v77\n"
"v_mfma_f32_16x16x32_bf16 v[148:151], v[180:183], v[60:63], v[148:151]\n"
"v_cvt_pk_bf16_f32 v103, v78, v79\n"
"ds_read_b64_tr_b16 v[172:173], v23 offset:32768\n"
"ds_read_b64_tr_b16 v[174:175], v23 offset:34816\n"
"s_waitcnt lgkmcnt(8)\n"
"v_mfma_f32_16x16x32_bf16 v[136:139], v[184:187], v[44:47], v[136:139]\n"
"v_cvt_pk_bf16_f32 v108, v88, v89\n"
"v_mfma_f32_16x16x32_bf16 v[152:155], v[184:187], v[60:63], v[152:155]\n"
"v_cvt_pk_bf16_f32 v109, v90, v91\n"
"ds_read_b64_tr_b16 v[176:177], v20 offset:40960\n"
"ds_read_b64_tr_b16 v[178:179], v20 offset:43008\n"
"v_mfma_f32_16x16x32_bf16 v[140:143], v[188:191], v[44:47], v[140:143]\n"
"v_cvt_pk_bf16_f32 v110, v92, v93\n"
"v_mfma_f32_16x16x32_bf16 v[156:159], v[188:191], v[60:63], v[156:159]\n"
"v_cvt_pk_bf16_f32 v111, v94, v95\n"
"ds_read_b64_tr_b16 v[180:181], v21 offset:40960\n"
"ds_read_b64_tr_b16 v[182:183], v21 offset:43008\n"
"s_nop 1\n"
"global_load_dwordx4 v[28:31], v24, s[92:93] offset:3072\n"
"s_waitcnt lgkmcnt(8)\n"
"v_mfma_f32_16x16x32_bf16 v[192:195], v[160:163], v[96:99], v[192:195]\n"
"v_mfma_f32_16x16x32_bf16 v[196:199], v[160:163], v[104:107], v[196:199]\n"
"ds_read_b64_tr_b16 v[184:185], v22 offset:40960\n"
"ds_read_b64_tr_b16 v[186:187], v22 offset:43008\n"
"v_mfma_f32_16x16x32_bf16 v[200:203], v[164:167], v[96:99], v[200:203]\n"
"v_mfma_f32_16x16x32_bf16 v[204:207], v[164:167], v[104:107], v[204:207]\n"
"ds_read_b64_tr_b16 v[188:189], v23 offset:40960\n"
"ds_read_b64_tr_b16 v[190:191], v23 offset:43008\n"
"s_waitcnt lgkmcnt(8)\n"
"v_mfma_f32_16x16x32_bf16 v[208:211], v[168:171], v[96:99], v[208:211]\n"
"v_exp_f32 v128, v128\n"
"v_mfma_f32_16x16x32_bf16 v[212:215], v[168:171], v[104:107], v[212:215]\n"
"ds_read_b64_tr_b16 v[160:161], v20 offset:36864\n"
"ds_read_b64_tr_b16 v[162:163], v20 offset:38912\n"
"v_mfma_f32_16x16x32_bf16 v[216:219], v[172:175], v[96:99], v[216:219]\n"
"v_exp_f32 v129, v129\n"
"v_mfma_f32_16x16x32_bf16 v[220:223], v[172:175], v[104:107], v[220:223]\n"
"v_mfma_f32_16x16x32_bf16 v[112:115], v[120:123], v[96:99], v[112:115]\n"
"v_exp_f32 v130, v130\n"
"v_mfma_f32_16x16x32_bf16 v[116:119], v[120:123], v[104:107], v[116:119]\n"
"ds_read_b64_tr_b16 v[164:165], v21 offset:36864\n"
"ds_read_b64_tr_b16 v[166:167], v21 offset:38912\n"
"s_waitcnt lgkmcnt(8)\n"
"v_mfma_f32_16x16x32_bf16 v[224:227], v[176:179], v[96:99], v[224:227]\n"
"v_exp_f32 v131, v131\n"
"v_mfma_f32_16x16x32_bf16 v[228:231], v[176:179], v[104:107], v[228:231]\n"
"ds_read_b64_tr_b16 v[168:169], v22 offset:36864\n"
"ds_read_b64_tr_b16 v[170:171], v22 offset:38912\n"
"v_mfma_f32_16x16x32_bf16 v[232:235], v[180:183], v[96:99], v[232:235]\n"
"v_exp_f32 v144, v144\n"
"v_mfma_f32_16x16x32_bf16 v[236:239], v[180:183], v[104:107], v[236:239]\n"
"ds_read_b64_tr_b16 v[172:173], v23 offset:36864\n"
"ds_read_b64_tr_b16 v[174:175], v23 offset:38912\n"
"s_waitcnt lgkmcnt(8)\n"
"v_mfma_f32_16x16x32_bf16 v[240:243], v[184:187], v[96:99], v[240:243]\n"
"v_exp_f32 v145, v145\n"
"v_mfma_f32_16x16x32_bf16 v[244:247], v[184:187], v[104:107], v[244:247]\n"
"ds_read_b64_tr_b16 v[176:177], v20 offset:45056\n"
"ds_read_b64_tr_b16 v[178:179], v20 offset:47104\n"
"v_mfma_f32_16x16x32_bf16 v[248:251], v[188:191], v[96:99], v[248:251]\n"
"v_exp_f32 v146, v146\n"
"v_mfma_f32_16x16x32_bf16 v[252:255], v[188:191], v[104:107], v[252:255]\n"
"ds_read_b64_tr_b16 v[180:181], v21 offset:45056\n"
"ds_read_b64_tr_b16 v[182:183], v21 offset:47104\n"
"s_waitcnt lgkmcnt(8)\n"
"v_mfma_f32_16x16x32_bf16 v[192:195], v[160:163], v[100:103], v[192:195]\n"
"v_exp_f32 v147, v147\n"
"v_mfma_f32_16x16x32_bf16 v[196:199], v[160:163], v[108:111], v[196:199]\n"
"ds_read_b64_tr_b16 v[184:185], v22 offset:45056\n"
"ds_read_b64_tr_b16 v[186:187], v22 offset:47104\n"
"v_mfma_f32_16x16x32_bf16 v[200:203], v[164:167], v[100:103], v[200:203]\n"
"v_exp_f32 v132, v132\n"
"v_mfma_f32_16x16x32_bf16 v[204:207], v[164:167], v[108:111], v[204:207]\n"
"ds_read_b64_tr_b16 v[188:189], v23 offset:45056\n"
"ds_read_b64_tr_b16 v[190:191], v23 offset:47104\n"
"s_waitcnt lgkmcnt(8)\n"
"v_mfma_f32_16x16x32_bf16 v[208:211], v[168:171], v[100:103], v[208:211]\n"
"v_exp_f32 v133, v133\n"
"v_mfma_f32_16x16x32_bf16 v[212:215], v[168:171], v[108:111], v[212:215]\n"
"v_xor_b32 v20, 0x10000, v20\n"
"v_xor_b32 v21, 0x10000, v21\n"
"v_xor_b32 v22, 0x10000, v22\n"
"v_xor_b32 v23, 0x10000, v23\n"
"ds_read_b64_tr_b16 v[160:161], v20 offset:0\n"
"ds_read_b64_tr_b16 v[162:163], v20 offset:2048\n"
"v_mfma_f32_16x16x32_bf16 v[216:219], v[172:175], v[100:103], v[216:219]\n"
"v_exp_f32 v134, v134\n"
"v_mfma_f32_16x16x32_bf16 v[220:223], v[172:175], v[108:111], v[220:223]\n"
"v_mfma_f32_16x16x32_bf16 v[112:115], v[120:123], v[100:103], v[112:115]\n"
"v_exp_f32 v135, v135\n"
"v_mfma_f32_16x16x32_bf16 v[116:119], v[120:123], v[108:111], v[116:119]\n"
"ds_read_b64_tr_b16 v[164:165], v21 offset:0\n"
"ds_read_b64_tr_b16 v[166:167], v21 offset:2048\n"
"s_waitcnt lgkmcnt(8)\n"
"v_mfma_f32_16x16x32_bf16 v[224:227], v[176:179], v[100:103], v[224:227]\n"
"v_exp_f32 v148, v148\n"
"v_mfma_f32_16x16x32_bf16 v[228:231], v[176:179], v[108:111], v[228:231]\n"
"ds_read_b64_tr_b16 v[168:169], v22 offset:0\n"
"ds_read_b64_tr_b16 v[170:171], v22 offset:2048\n"
"v_mfma_f32_16x16x32_bf16 v[232:235], v[180:183], v[100:103], v[232:235]\n"
"v_exp_f32 v149, v149\n"
"v_mfma_f32_16x16x32_bf16 v[236:239], v[180:183], v[108:111], v[236:239]\n"
"ds_read_b64_tr_b16 v[172:173], v23 offset:0\n"
"ds_read_b64_tr_b16 v[174:175], v23 offset:2048\n"
"s_waitcnt lgkmcnt(8)\n"
"v_mfma_f32_16x16x32_bf16 v[240:243], v[184:187], v[100:103], v[240:243]\n"
"v_exp_f32 v150, v150\n"
"v_mfma_f32_16x16x32_bf16 v[244:247], v[184:187], v[108:111], v[244:247]\n"
"ds_read_b64_tr_b16 v[176:177], v20 offset:8192\n"
"ds_read_b64_tr_b16 v[178:179], v20 offset:10240\n"
"v_mfma_f32_16x16x32_bf16 v[248:251], v[188:191], v[100:103], v[248:251]\n"
"v_exp_f32 v151, v151\n"
"v_mfma_f32_16x16x32_bf16 v[252:255], v[188:191], v[108:111], v[252:255]\n"
"ds_read_b64_tr_b16 v[180:181], v21 offset:8192\n"
"ds_read_b64_tr_b16 v[182:183], v21 offset:10240\n"
"v_cvt_pk_bf16_f32 v96, v128, v129\n"
"v_cvt_pk_bf16_f32 v97, v130, v131\n"
"v_cvt_pk_bf16_f32 v98, v132, v133\n"
"v_cvt_pk_bf16_f32 v99, v134, v135\n"
"v_cvt_pk_bf16_f32 v104, v144, v145\n"
"v_cvt_pk_bf16_f32 v105, v146, v147\n"
"v_cvt_pk_bf16_f32 v106, v148, v149\n"
"v_cvt_pk_bf16_f32 v107, v150, v151\n"
"v_exp_f32 v136, v136\n"
"v_exp_f32 v137, v137\n"
"v_exp_f32 v138, v138\n"
"v_exp_f32 v139, v139\n"
"v_exp_f32 v152, v152\n"
"v_exp_f32 v153, v153\n"
"v_exp_f32 v154, v154\n"
"v_exp_f32 v155, v155\n"
"v_exp_f32 v140, v140\n"
"v_exp_f32 v141, v141\n"
"v_exp_f32 v142, v142\n"
"v_exp_f32 v143, v143\n"
"v_exp_f32 v156, v156\n"
"v_exp_f32 v157, v157\n"
"v_exp_f32 v158, v158\n"
"v_exp_f32 v159, v159\n"
"v_cvt_pk_bf16_f32 v100, v136, v137\n"
"v_cvt_pk_bf16_f32 v101, v138, v139\n"
"v_cvt_pk_bf16_f32 v102, v140, v141\n"
"v_cvt_pk_bf16_f32 v103, v142, v143\n"
"v_cvt_pk_bf16_f32 v108, v152, v153\n"
"v_cvt_pk_bf16_f32 v109, v154, v155\n"
"v_cvt_pk_bf16_f32 v110, v156, v157\n"
"v_cvt_pk_bf16_f32 v111, v158, v159\n"
"s_nop 1\n"
"global_load_dwordx4 v[28:31], v26, s[94:95]\n"
"s_waitcnt lgkmcnt(8)\n"
"v_mfma_f32_16x16x32_bf16 v[192:195], v[160:163], v[96:99], v[192:195]\n"
"v_mfma_f32_16x16x32_bf16 v[196:199], v[160:163], v[104:107], v[196:199]\n"
"ds_read_b64_tr_b16 v[184:185], v22 offset:8192\n"
"ds_read_b64_tr_b16 v[186:187], v22 offset:10240\n"
"v_mfma_f32_16x16x32_bf16 v[200:203], v[164:167], v[96:99], v[200:203]\n"
"v_mfma_f32_16x16x32_bf16 v[204:207], v[164:167], v[104:107], v[204:207]\n"
"ds_read_b64_tr_b16 v[188:189], v23 offset:8192\n"
"ds_read_b64_tr_b16 v[190:191], v23 offset:10240\n"
"s_waitcnt lgkmcnt(8)\n"
"v_mfma_f32_16x16x32_bf16 v[208:211], v[168:171], v[96:99], v[208:211]\n"
"v_mfma_f32_16x16x32_bf16 v[212:215], v[168:171], v[104:107], v[212:215]\n"
"ds_read_b64_tr_b16 v[160:161], v20 offset:4096\n"
"ds_read_b64_tr_b16 v[162:163], v20 offset:6144\n"
"v_mfma_f32_16x16x32_bf16 v[216:219], v[172:175], v[96:99], v[216:219]\n"
"v_mfma_f32_16x16x32_bf16 v[220:223], v[172:175], v[104:107], v[220:223]\n"
"v_mfma_f32_16x16x32_bf16 v[112:115], v[120:123], v[96:99], v[112:115]\n"
"v_mfma_f32_16x16x32_bf16 v[116:119], v[120:123], v[104:107], v[116:119]\n"
"ds_read_b64_tr_b16 v[164:165], v21 offset:4096\n"
"ds_read_b64_tr_b16 v[166:167], v21 offset:6144\n"
"s_waitcnt lgkmcnt(8)\n"
"v_mfma_f32_16x16x32_bf16 v[224:227], v[176:179], v[96:99], v[224:227]\n"
"v_mfma_f32_16x16x32_bf16 v[228:231], v[176:179], v[104:107], v[228:231]\n"
"ds_read_b64_tr_b16 v[168:169], v22 offset:4096\n"
"ds_read_b64_tr_b16 v[170:171], v22 offset:6144\n"
"v_mfma_f32_16x16x32_bf16 v[232:235], v[180:183], v[96:99], v[232:235]\n"
"v_mfma_f32_16x16x32_bf16 v[236:239], v[180:183], v[104:107], v[236:239]\n"
"ds_read_b64_tr_b16 v[172:173], v23 offset:4096\n"
"ds_read_b64_tr_b16 v[174:175], v23 offset:6144\n"
"s_waitcnt lgkmcnt(8)\n"
"v_mfma_f32_16x16x32_bf16 v[240:243], v[184:187], v[96:99], v[240:243]\n"
"v_mfma_f32_16x16x32_bf16 v[244:247], v[184:187], v[104:107], v[244:247]\n"
"ds_read_b64_tr_b16 v[176:177], v20 offset:12288\n"
"ds_read_b64_tr_b16 v[178:179], v20 offset:14336\n"
"v_mfma_f32_16x16x32_bf16 v[248:251], v[188:191], v[96:99], v[248:251]\n"
"v_mfma_f32_16x16x32_bf16 v[252:255], v[188:191], v[104:107], v[252:255]\n"
"ds_read_b64_tr_b16 v[180:181], v21 offset:12288\n"
"ds_read_b64_tr_b16 v[182:183], v21 offset:14336\n"
"s_waitcnt lgkmcnt(8)\n"
"v_mfma_f32_16x16x32_bf16 v[192:195], v[160:163], v[100:103], v[192:195]\n"
"v_mfma_f32_16x16x32_bf16 v[196:199], v[160:163], v[108:111], v[196:199]\n"
"ds_read_b64_tr_b16 v[184:185], v22 offset:12288\n"
"ds_read_b64_tr_b16 v[186:187], v22 offset:14336\n"
"v_mfma_f32_16x16x32_bf16 v[200:203], v[164:167], v[100:103], v[200:203]\n"
"v_mfma_f32_16x16x32_bf16 v[204:207], v[164:167], v[108:111], v[204:207]\n"
"ds_read_b64_tr_b16 v[188:189], v23 offset:12288\n"
"ds_read_b64_tr_b16 v[190:191], v23 offset:14336\n"
"s_waitcnt lgkmcnt(8)\n"
"v_mfma_f32_16x16x32_bf16 v[208:211], v[168:171], v[100:103], v[208:211]\n"
"v_mfma_f32_16x16x32_bf16 v[212:215], v[168:171], v[108:111], v[212:215]\n"
"v_mfma_f32_16x16x32_bf16 v[216:219], v[172:175], v[100:103], v[216:219]\n"
"v_mfma_f32_16x16x32_bf16 v[220:223], v[172:175], v[108:111], v[220:223]\n"
"v_mfma_f32_16x16x32_bf16 v[112:115], v[120:123], v[100:103], v[112:115]\n"
"v_mfma_f32_16x16x32_bf16 v[116:119], v[120:123], v[108:111], v[116:119]\n"
"s_waitcnt lgkmcnt(4)\n"
"v_mfma_f32_16x16x32_bf16 v[224:227], v[176:179], v[100:103], v[224:227]\n"
"v_mfma_f32_16x16x32_bf16 v[228:231], v[176:179], v[108:111], v[228:231]\n"
"v_mfma_f32_16x16x32_bf16 v[232:235], v[180:183], v[100:103], v[232:235]\n"
"v_mfma_f32_16x16x32_bf16 v[236:239], v[180:183], v[108:111], v[236:239]\n"
"s_waitcnt lgkmcnt(0)\n"
"v_mfma_f32_16x16x32_bf16 v[240:243], v[184:187], v[100:103], v[240:243]\n"
"v_mfma_f32_16x16x32_bf16 v[244:247], v[184:187], v[108:111], v[244:247]\n"
"v_mfma_f32_16x16x32_bf16 v[248:251], v[188:191], v[100:103], v[248:251]\n"
"v_mfma_f32_16x16x32_bf16 v[252:255], v[188:191], v[108:111], v[252:255]\n"
"s_waitcnt vmcnt(0)\n"
"s_nop 7\n"
"v_mov_b32 v2, 0x4e800000\n"
"v_cmp_nlt_f32 vcc, v112, v2\n"
"s_nop 1\n"
"s_mov_b64 s[50:51], vcc\n"
"v_cmp_nlt_f32 vcc, v116, v2\n"
"s_nop 1\n"
"s_or_b64 s[50:51], s[50:51], vcc\n"
"s_cmp_lg_u64 s[50:51], 0\n"
"s_cselect_b32 s50, 1, 0\n"
"v_mov_b32 v3, s50\n"
"s_lshl_b32 s51, s21, 2\n"
"s_add_u32 s51, s51, 98304\n"
"v_mov_b32 v2, s51\n"
"ds_write_b32 v2, v3\n"
"s_waitcnt lgkmcnt(0)\n"
"s_barrier\n"
"v_and_b32 v2, 7, v1\n"
"v_lshlrev_b32 v2, 2, v2\n"
"v_add_u32 v2, 98304, v2\n"
"ds_read_b32 v3, v2\n"
"s_waitcnt lgkmcnt(0)\n"
"v_cmp_ne_u32 vcc, 0, v3\n"
"s_nop 1\n"
"s_mov_b64 s[50:51], vcc\n"
"s_barrier\n"
"s_cmp_lg_u64 s[50:51], 0\n"
"s_cbranch_scc1 AT_slow\n"
"AT_join:\n"
"s_load_dwordx4 s[8:11], s[4:5], 48\n"
"s_load_dwordx4 s[12:15], s[4:5], 80\n"
"s_load_dwordx2 s[16:17], s[4:5], 96\n"
"s_waitcnt lgkmcnt(0)\n"
"s_mov_b32 s58, s22\n"
"s_mov_b32 s59, s23\n"
"s_and_b32 s50, s20, 7\n"
"s_lshr_b32 s51, s20, 3\n"
"s_lshr_b32 s62, s50, 1\n"
"s_and_b32 s50, s50, 1\n"
"s_lshl_b32 s50, s50, 2\n"
"s_and_b32 s22, s51, 3\n"
"s_add_u32 s22, s22, s50\n"
"s_lshr_b32 s23, s51, 2\n"
"s_lshl_b32 s63, s22, 3\n"
"s_add_u32 s63, s63, s23\n"
"s_mul_i32 s50, s22, 1835008\n"
"s_add_u32 s24, s8, s50\n"
"s_addc_u32 s25, s9, 0\n"
"s_and_b32 s25, s25, 0xffff\n"
"s_mov_b32 s26, 1835008\n"
"s_mov_b32 s27, 0x20000\n"
"s_mul_i32 s44, s62, 1792\n"
"s_lshl_b32 s50, s23, 8\n"
"s_add_u32 s28, s94, s50\n"
"s_addc_u32 s29, s95, 0\n"
"s_and_b32 s29, s29, 0xffff\n"
"s_sub_u32 s30, 7340032, s50\n"
"s_mov_b32 s31, 0x20000\n"
"s_mul_i32 s45, s62, 1835008\n"
"v_lshrrev_b32 v2, 3, v1\n"
"v_lshrrev_b32 v3, 4, v1\n"
"v_and_b32 v4, 7, v1\n"
"v_xor_b32 v5, v4, v3\n"
"s_lshl_b32 s50, s21, 5\n"
"v_add_u32 v6, s50, v2\n"
"v_add_u32 v7, 0, v6\n"
"v_xor_b32 v8, 0, v5\n"
"v_lshlrev_b32 v8, 4, v8\n"
"v_mul_u32_u24 v16, 7168, v7\n"
"v_add_u32 v16, v16, v8\n"
"v_lshlrev_b32 v7, 13, v7\n"
"v_add_u32 v8, v8, v7\n"
"v_add_u32 v7, 8, v6\n"
"v_xor_b32 v9, 4, v5\n"
"v_lshlrev_b32 v9, 4, v9\n"
"v_mul_u32_u24 v17, 7168, v7\n"
"v_add_u32 v17, v17, v9\n"
"v_lshlrev_b32 v7, 13, v7\n"
"v_add_u32 v9, v9, v7\n"
"v_add_u32 v7, 16, v6\n"
"v_xor_b32 v10, 0, v5\n"
"v_lshlrev_b32 v10, 4, v10\n"
"v_mul_u32_u24 v18, 7168, v7\n"
"v_add_u32 v18, v18, v10\n"
"v_lshlrev_b32 v7, 13, v7\n"
"v_add_u32 v10, v10, v7\n"
"v_add_u32 v7, 24, v6\n"
"v_xor_b32 v11, 4, v5\n"
"v_lshlrev_b32 v11, 4, v11\n"
"v_mul_u32_u24 v19, 7168, v7\n"
"v_add_u32 v19, v19, v11\n"
"v_lshlrev_b32 v7, 13, v7\n"
"v_add_u32 v11, v11, v7\n"
"s_lshl_b32 s50, s34, 4\n"
"v_add_u32 v6, s50, v2\n"
"s_lshl_b32 s51, s35, 7\n"
"v_and_b32 v5, 1, v3\n"
"v_lshlrev_b32 v5, 1, v5\n"
"v_xor_b32 v5, v5, v4\n"
"v_add_u32 v7, 0, v6\n"
"v_lshlrev_b32 v7, 11, v7\n"
"v_xor_b32 v20, 0, v5\n"
"v_lshlrev_b32 v20, 4, v20\n"
"v_add_u32 v20, v20, v7\n"
"v_add_u32 v20, s51, v20\n"
"v_add_u32 v7, 8, v6\n"
"v_lshlrev_b32 v7, 11, v7\n"
"v_xor_b32 v21, 4, v5\n"
"v_lshlrev_b32 v21, 4, v21\n"
"v_add_u32 v21, v21, v7\n"
"v_add_u32 v21, s51, v21\n"
"s_lshr_b32 s50, s21, 1\n"
"s_lshl_b32 s46, s50, 13\n"
"s_and_b32 s50, s21, 1\n"
"s_lshl_b32 s50, s50, 1\n"
"s_xor_b32 s50, s50, s62\n"
"s_lshl_b32 s50, s50, 11\n"
"s_add_u32 s46, s46, s50\n"
"s_lshl_b32 s49, s35, 13\n"
"s_lshl_b32 s50, s34, 11\n"
"s_add_u32 s49, s49, s50\n"
"s_add_u32 s49, s49, 98304\n"
"s_mov_b32 s68, 0\n"
"s_mov_b32 s69, 0\n"
"s_mov_b32 s86, 32768\n"
"s_mov_b32 s87, 16384\n"
"v_cmp_ne_u32 vcc, s61, v27\n"
"s_nop 1\n"
"s_cmp_eq_u64 vcc, 0\n"
"s_cbranch_scc1 P2_ch_ok\n"
"s_lshl_b32 s50, s22, 7\n"
"s_add_u32 s50, s50, 0\n"
"v_and_b32 v2, 31, v1\n"
"v_lshlrev_b32 v2, 2, v2\n"
"v_add_u32 v2, s50, v2\n"
"s_mov_b32 s52, 0\n"
"P2_ch_spin:\n"
"global_load_dword v3, v2, s[6:7] sc1\n"
"s_waitcnt vmcnt(0)\n"
"v_cmp_ne_u32 vcc, s61, v3\n"
"s_nop 1\n"
"s_cmp_eq_u64 vcc, 0\n"
"s_cbranch_scc1 P2_ch_ok\n"
"s_add_u32 s52, s52, 1\n"
"s_cmp_lt_u32 s52, 0x8000\n"
"s_cbranch_scc0 P2_ch_ok\n"
"s_sleep 1\n"
"s_branch P2_ch_spin\n"
"P2_ch_ok:\n"
"s_xor_b32 m0, s46, 0\n"
"s_nop 0\n"
"buffer_load_dwordx4 v16, s[24:27], s44 offen sc1 lds\n"
"s_xor_b32 m0, s46, 1024\n"
"s_nop 0\n"
"buffer_load_dwordx4 v17, s[24:27], s44 offen sc1 lds\n"
"s_xor_b32 m0, s46, 2048\n"
"s_nop 0\n"
"buffer_load_dwordx4 v18, s[24:27], s44 offen sc1 lds\n"
"s_xor_b32 m0, s46, 3072\n"
"s_nop 0\n"
"buffer_load_dwordx4 v19, s[24:27], s44 offen sc1 lds\n"
"s_add_u32 s44, s44, 128\n"
"s_add_u32 s46, s46, 32768\n"
"s_cmp_ge_u32 s46, 98304\n"
"s_cselect_b32 s50, 98304, 0\n"
"s_sub_u32 s46, s46, s50\n"
"s_add_u32 m0, s49, 0\n"
"s_nop 0\n"
"buffer_load_dwordx4 v20, s[28:31], s45 offen lds\n"
"s_add_u32 m0, s49, 1024\n"
"s_nop 0\n"
"buffer_load_dwordx4 v21, s[28:31], s45 offen lds\n"
"s_add_u32 s45, s45, 131072\n"
"s_add_u32 s49, s49, 16384\n"
"s_cmp_ge_u32 s49, 163840\n"
"s_cselect_b32 s50, 65536, 0\n"
"s_sub_u32 s49, s49, s50\n"
"s_add_u32 m0, s49, 0\n"
"s_nop 0\n"
"buffer_load_dwordx4 v20, s[28:31], s45 offen lds\n"
"s_add_u32 m0, s49, 1024\n"
"s_nop 0\n"
"buffer_load_dwordx4 v21, s[28:31], s45 offen lds\n"
"s_add_u32 s45, s45, 131072\n"
"s_add_u32 s49, s49, 16384\n"
"s_cmp_ge_u32 s49, 163840\n"
"s_cselect_b32 s50, 65536, 0\n"
"s_sub_u32 s49, s49, s50\n"
"s_xor_b32 m0, s46, 0\n"
"s_nop 0\n"
"buffer_load_dwordx4 v16, s[24:27], s44 offen sc1 lds\n"
"s_xor_b32 m0, s46, 1024\n"
"s_nop 0\n"
"buffer_load_dwordx4 v17, s[24:27], s44 offen sc1 lds\n"
"s_xor_b32 m0, s46, 2048\n"
"s_nop 0\n"
"buffer_load_dwordx4 v18, s[24:27], s44 offen sc1 lds\n"
"s_xor_b32 m0, s46, 3072\n"
"s_nop 0\n"
"buffer_load_dwordx4 v19, s[24:27], s44 offen sc1 lds\n"
"s_add_u32 s44, s44, 128\n"
"s_add_u32 s46, s46, 32768\n"
"s_cmp_ge_u32 s46, 98304\n"
"s_cselect_b32 s50, 98304, 0\n"
"s_sub_u32 s46, s46, s50\n"
"s_add_u32 m0, s49, 0\n"
"s_nop 0\n"
"buffer_load_dwordx4 v20, s[28:31], s45 offen lds\n"
"s_add_u32 m0, s49, 1024\n"
"s_nop 0\n"
"buffer_load_dwordx4 v21, s[28:31], s45 offen lds\n"
"s_add_u32 s45, s45, 131072\n"
"s_add_u32 s49, s49, 16384\n"
"s_cmp_ge_u32 s49, 163840\n"
"s_cselect_b32 s50, 65536, 0\n"
"s_sub_u32 s49, s49, s50\n"
"v_and_b32 v2, 15, v1\n"
"v_lshrrev_b32 v3, 4, v1\n"
"v_lshrrev_b32 v4, 1, v1\n"
"v_and_b32 v4, 7, v4\n"
"v_xor_b32 v4, v4, v3\n"
"s_lshl_b32 s50, s34, 6\n"
"v_add_u32 v5, s50, v2\n"
"v_lshlrev_b32 v5, 7, v5\n"
"v_lshlrev_b32 v4, 4, v4\n"
"v_add_u32 v24, v5, v4\n"
"v_xor_b32 v25, 64, v24\n"
"v_lshrrev_b32 v4, 2, v2\n"
"v_and_b32 v5, 3, v1\n"
"v_lshlrev_b32 v6, 3, v3\n"
"v_add_u32 v6, v6, v4\n"
"v_lshlrev_b32 v6, 7, v6\n"
"v_lshrrev_b32 v7, 1, v4\n"
"v_lshlrev_b32 v7, 1, v7\n"
"v_and_b32 v3, 1, v3\n"
"v_lshlrev_b32 v3, 2, v3\n"
"v_or_b32 v7, v7, v3\n"
"v_lshrrev_b32 v3, 1, v5\n"
"v_xor_b32 v7, v7, v3\n"
"v_lshlrev_b32 v7, 4, v7\n"
"v_and_b32 v5, 1, v5\n"
"v_lshlrev_b32 v5, 3, v5\n"
"v_add_u32 v6, v6, v5\n"
"v_add_u32 v6, v6, v7\n"
"s_lshl_b32 s50, s35, 13\n"
"s_add_u32 s50, s50, 98304\n"
"v_add_u32 v26, s50, v6\n"
"v_xor_b32 v27, 32, v26\n"
"v_xor_b32 v28, 64, v26\n"
"v_xor_b32 v29, 96, v26\n"
"s_lshl_b32 s50, s58, 21\n"
"s_lshl_b32 s51, s59, 8\n"
"s_add_u32 s50, s50, s51\n"
"s_add_u32 s40, s10, s50\n"
"s_addc_u32 s41, s11, 0\n"
"s_and_b32 s41, s41, 0xffff\n"
"s_sub_u32 s42, 2097152, s51\n"
"s_mov_b32 s43, 0x20000\n"
"v_rcp_f32 v2, v112\n"
"v_rcp_f32 v4, v116\n"
"v_and_b32 v6, 15, v1\n"
"v_lshrrev_b32 v7, 4, v1\n"
"v_mov_b32 v3, v2\n"
"v_mov_b32 v5, v4\n"
"s_lshl_b32 s50, s34, 6\n"
"s_lshl_b32 s51, s35, 5\n"
"s_add_u32 s50, s50, s51\n"
"v_add_u32 v30, s50, v7\n"
"v_lshlrev_b32 v30, 13, v30\n"
"v_lshlrev_b32 v31, 4, v6\n"
"v_add_u32 v30, v30, v31\n"
"s_lshl_b32 s50, s21, 12\n"
"s_add_u32 s50, s50, 65536\n"
"v_and_b32 v14, 1, v7\n"
"v_lshlrev_b32 v14, 1, v14\n"
"v_lshrrev_b32 v15, 1, v7\n"
"v_or_b32 v14, v14, v15\n"
"v_xor_b32 v14, v14, v6\n"
"v_lshlrev_b32 v14, 4, v14\n"
"v_lshlrev_b32 v15, 8, v6\n"
"v_add_u32 v14, v14, v15\n"
"v_add_u32 v14, s50, v14\n"
"v_xor_b32 v15, v6, v7\n"
"v_lshlrev_b32 v15, 4, v15\n"
"v_lshlrev_b32 v31, 8, v7\n"
"v_add_u32 v15, v15, v31\n"
"v_add_u32 v15, s50, v15\n"
"v_pk_mul_f32 v[192:193], v[192:193], v[2:3]\n"
"v_pk_mul_f32 v[194:195], v[194:195], v[2:3]\n"
"v_pk_mul_f32 v[200:201], v[200:201], v[2:3]\n"
"v_pk_mul_f32 v[202:203], v[202:203], v[2:3]\n"
"v_pk_mul_f32 v[208:209], v[208:209], v[2:3]\n"
"v_pk_mul_f32 v[210:211], v[210:211], v[2:3]\n"
"v_pk_mul_f32 v[216:217], v[216:217], v[2:3]\n"
"v_pk_mul_f32 v[218:219], v[218:219], v[2:3]\n"
"v_pk_mul_f32 v[224:225], v[224:225], v[2:3]\n"
"v_pk_mul_f32 v[226:227], v[226:227], v[2:3]\n"
"v_pk_mul_f32 v[232:233], v[232:233], v[2:3]\n"
"v_pk_mul_f32 v[234:235], v[234:235], v[2:3]\n"
"v_pk_mul_f32 v[240:241], v[240:241], v[2:3]\n"
"v_pk_mul_f32 v[242:243], v[242:243], v[2:3]\n"
"v_pk_mul_f32 v[248:249], v[248:249], v[2:3]\n"
"v_pk_mul_f32 v[250:251], v[250:251], v[2:3]\n"
"v_pk_mul_f32 v[196:197], v[196:197], v[4:5]\n"
"v_pk_mul_f32 v[198:199], v[198:199], v[4:5]\n"
"v_pk_mul_f32 v[204:205], v[204:205], v[4:5]\n"
"v_pk_mul_f32 v[206:207], v[206:207], v[4:5]\n"
"v_pk_mul_f32 v[212:213], v[212:213], v[4:5]\n"
"v_pk_mul_f32 v[214:215], v[214:215], v[4:5]\n"
"v_pk_mul_f32 v[220:221], v[220:221], v[4:5]\n"
"v_pk_mul_f32 v[222:223], v[222:223], v[4:5]\n"
"v_pk_mul_f32 v[228:229], v[228:229], v[4:5]\n"
"v_pk_mul_f32 v[230:231], v[230:231], v[4:5]\n"
"v_pk_mul_f32 v[236:237], v[236:237], v[4:5]\n"
"v_pk_mul_f32 v[238:239], v[238:239], v[4:5]\n"
"v_pk_mul_f32 v[244:245], v[244:245], v[4:5]\n"
"v_pk_mul_f32 v[246:247], v[246:247], v[4:5]\n"
"v_pk_mul_f32 v[252:253], v[252:253], v[4:5]\n"
"v_pk_mul_f32 v[254:255], v[254:255], v[4:5]\n"
"v_cvt_pk_bf16_f32 v64, v192, v193\n"
"v_cvt_pk_bf16_f32 v65, v194, v195\n"
"v_cvt_pk_bf16_f32 v66, v200, v201\n"
"v_cvt_pk_bf16_f32 v67, v202, v203\n"
"v_cvt_pk_bf16_f32 v68, v208, v209\n"
"v_cvt_pk_bf16_f32 v69, v210, v211\n"
"v_cvt_pk_bf16_f32 v70, v216, v217\n"
"v_cvt_pk_bf16_f32 v71, v218, v219\n"
"v_cvt_pk_bf16_f32 v72, v224, v225\n"
"v_cvt_pk_bf16_f32 v73, v226, v227\n"
"v_cvt_pk_bf16_f32 v74, v232, v233\n"
"v_cvt_pk_bf16_f32 v75, v234, v235\n"
"v_cvt_pk_bf16_f32 v76, v240, v241\n"
"v_cvt_pk_bf16_f32 v77, v242, v243\n"
"v_cvt_pk_bf16_f32 v78, v248, v249\n"
"v_cvt_pk_bf16_f32 v79, v250, v251\n"
"v_cvt_pk_bf16_f32 v80, v196, v197\n"
"v_cvt_pk_bf16_f32 v81, v198, v199\n"
"v_cvt_pk_bf16_f32 v82, v204, v205\n"
"v_cvt_pk_bf16_f32 v83, v206, v207\n"
"v_cvt_pk_bf16_f32 v84, v212, v213\n"
"v_cvt_pk_bf16_f32 v85, v214, v215\n"
"v_cvt_pk_bf16_f32 v86, v220, v221\n"
"v_cvt_pk_bf16_f32 v87, v222, v223\n"
"v_cvt_pk_bf16_f32 v88, v228, v229\n"
"v_cvt_pk_bf16_f32 v89, v230, v231\n"
"v_cvt_pk_bf16_f32 v90, v236, v237\n"
"v_cvt_pk_bf16_f32 v91, v238, v239\n"
"v_cvt_pk_bf16_f32 v92, v244, v245\n"
"v_cvt_pk_bf16_f32 v93, v246, v247\n"
"v_cvt_pk_bf16_f32 v94, v252, v253\n"
"v_cvt_pk_bf16_f32 v95, v254, v255\n"
"s_nop 1\n"
"v_permlane16_swap_b32 v64, v66\n"
"v_permlane16_swap_b32 v65, v67\n"
"v_permlane16_swap_b32 v68, v70\n"
"v_permlane16_swap_b32 v69, v71\n"
"v_permlane16_swap_b32 v72, v74\n"
"v_permlane16_swap_b32 v73, v75\n"
"v_permlane16_swap_b32 v76, v78\n"
"v_permlane16_swap_b32 v77, v79\n"
"v_permlane16_swap_b32 v80, v82\n"
"v_permlane16_swap_b32 v81, v83\n"
"v_permlane16_swap_b32 v84, v86\n"
"v_permlane16_swap_b32 v85, v87\n"
"v_permlane16_swap_b32 v88, v90\n"
"v_permlane16_swap_b32 v89, v91\n"
"v_permlane16_swap_b32 v92, v94\n"
"v_permlane16_swap_b32 v93, v95\n"
"ds_write_b128 v14, v[64:67]\n"
"v_xor_b32 v31, 64, v14\n"
"ds_write_b128 v31, v[68:71]\n"
"v_xor_b32 v31, 128, v14\n"
"ds_write_b128 v31, v[72:75]\n"
"v_xor_b32 v31, 192, v14\n"
"ds_write_b128 v31, v[76:79]\n"
"s_waitcnt lgkmcnt(0)\n"
"ds_read_b128 v[96:99], v15\n"
"v_xor_b32 v31, 64, v15\n"
"ds_read_b128 v[100:103], v31 offset:1024\n"
"v_xor_b32 v31, 128, v15\n"
"ds_read_b128 v[104:107], v31 offset:2048\n"
"v_xor_b32 v31, 192, v15\n"
"ds_read_b128 v[108:111], v31 offset:3072\n"
"s_waitcnt lgkmcnt(0)\n"
"ds_write_b128 v14, v[80:83]\n"
"v_xor_b32 v31, 64, v14\n"
"ds_write_b128 v31, v[84:87]\n"
"v_xor_b32 v31, 128, v14\n"
"ds_write_b128 v31, v[88:91]\n"
"v_xor_b32 v31, 192, v14\n"
"ds_write_b128 v31, v[92:95]\n"
"s_waitcnt lgkmcnt(0)\n"
"ds_read_b128 v[112:115], v15\n"
"v_xor_b32 v31, 64, v15\n"
"ds_read_b128 v[116:119], v31 offset:1024\n"
"v_xor_b32 v31, 128, v15\n"
"ds_read_b128 v[120:123], v31 offset:2048\n"
"v_xor_b32 v31, 192, v15\n"
"ds_read_b128 v[124:127], v31 offset:3072\n"
"s_waitcnt lgkmcnt(0)\n"
"s_waitcnt vmcnt(8)\n"
"s_barrier\n"
"s_cmp_eq_u32 s35, 0\n"
"s_cbranch_scc1 P2_lead\n"
"s_barrier\n"
"P2_lead:\n"
"s_xor_b32 m0, s46, 0\n"
"ds_read_b64_tr_b16 v[64:65], v26 offset:0\n"
"buffer_load_dwordx4 v16, s[24:27], s44 offen sc1 lds\n"
"ds_read_b64_tr_b16 v[66:67], v26 offset:512\n"
"ds_read_b64_tr_b16 v[68:69], v27 offset:0\n"
"ds_read_b64_tr_b16 v[70:71], v27 offset:512\n"
"s_xor_b32 m0, s46, 1024\n"
"ds_read_b64_tr_b16 v[72:73], v28 offset:0\n"
"buffer_load_dwordx4 v17, s[24:27], s44 offen sc1 lds\n"
"ds_read_b64_tr_b16 v[74:75], v28 offset:512\n"
"ds_read_b64_tr_b16 v[76:77], v29 offset:0\n"
"ds_read_b64_tr_b16 v[78:79], v29 offset:512\n"
"s_xor_b32 m0, s46, 2048\n"
"ds_read_b128 v[32:35], v24 offset:0\n"
"buffer_load_dwordx4 v18, s[24:27], s44 offen sc1 lds\n"
"ds_read_b128 v[36:39], v24 offset:2048\n"
"ds_read_b128 v[40:43], v24 offset:4096\n"
"ds_read_b128 v[44:47], v24 offset:6144\n"
"s_xor_b32 m0, s46, 3072\n"
"ds_read_b64_tr_b16 v[80:81], v26 offset:4096\n"
"buffer_load_dwordx4 v19, s[24:27], s44 offen sc1 lds\n"
"ds_read_b64_tr_b16 v[82:83], v26 offset:4608\n"
"ds_read_b64_tr_b16 v[84:85], v27 offset:4096\n"
"ds_read_b64_tr_b16 v[86:87], v27 offset:4608\n"
"s_add_u32 m0, s49, 0\n"
"ds_read_b64_tr_b16 v[88:89], v28 offset:4096\n"
"buffer_load_dwordx4 v20, s[28:31], s45 offen lds\n"
"ds_read_b64_tr_b16 v[90:91], v28 offset:4608\n"
"ds_read_b64_tr_b16 v[92:93], v29 offset:4096\n"
"ds_read_b64_tr_b16 v[94:95], v29 offset:4608\n"
"s_add_u32 m0, s49, 1024\n"
"ds_read_b128 v[48:51], v25 offset:0\n"
"buffer_load_dwordx4 v21, s[28:31], s45 offen lds\n"
"ds_read_b128 v[52:55], v25 offset:2048\n"
"ds_read_b128 v[56:59], v25 offset:4096\n"
"ds_read_b128 v[60:63], v25 offset:6144\n"
"s_mov_b32 s88, 0\n"
"buffer_store_dwordx4 v[96:99], v30, s[40:43], s88 offen sc1\n"
"s_mov_b32 s88, 32768\n"
"buffer_store_dwordx4 v[100:103], v30, s[40:43], s88 offen sc1\n"
"s_waitcnt vmcnt(10) lgkmcnt(0)\n"
"s_barrier\n"
"v_mfma_f32_16x16x32_bf16 v[128:131], v[64:67], v[32:35], 0\n"
"v_mfma_f32_16x16x32_bf16 v[128:131], v[80:83], v[48:51], v[128:131]\n"
"s_add_u32 s68, s68, 1\n"
"s_cmp_eq_u32 s68, 3\n"
"s_cselect_b32 s48, 4294901760, s86\n"
"s_cselect_b32 s68, 0, s68\n"
"v_mfma_f32_16x16x32_bf16 v[144:147], v[80:83], v[52:55], 0\n"
"s_add_u32 s69, s69, 1\n"
"s_cmp_eq_u32 s69, 4\n"
"s_cselect_b32 s60, 4294918144, s87\n"
"s_cselect_b32 s69, 0, s69\n"
"v_mfma_f32_16x16x32_bf16 v[144:147], v[64:67], v[36:39], v[144:147]\n"
"v_mfma_f32_16x16x32_bf16 v[160:163], v[64:67], v[40:43], 0\n"
"v_add_u32 v24, s48, v24\n"
"v_mfma_f32_16x16x32_bf16 v[160:163], v[80:83], v[56:59], v[160:163]\n"
"v_add_u32 v25, s48, v25\n"
"v_mfma_f32_16x16x32_bf16 v[176:179], v[80:83], v[60:63], 0\n"
"v_add_u32 v26, s60, v26\n"
"v_mfma_f32_16x16x32_bf16 v[176:179], v[64:67], v[44:47], v[176:179]\n"
"v_add_u32 v27, s60, v27\n"
"v_mfma_f32_16x16x32_bf16 v[180:183], v[68:71], v[44:47], 0\n"
"v_add_u32 v28, s60, v28\n"
"v_mfma_f32_16x16x32_bf16 v[180:183], v[84:87], v[60:63], v[180:183]\n"
"v_add_u32 v29, s60, v29\n"
"v_mfma_f32_16x16x32_bf16 v[164:167], v[84:87], v[56:59], 0\n"
"v_mfma_f32_16x16x32_bf16 v[164:167], v[68:71], v[40:43], v[164:167]\n"
"v_mfma_f32_16x16x32_bf16 v[148:151], v[68:71], v[36:39], 0\n"
"s_add_u32 s44, s44, 128\n"
"s_add_u32 s46, s46, 32768\n"
"s_cmp_ge_u32 s46, 98304\n"
"s_cselect_b32 s50, 98304, 0\n"
"s_sub_u32 s46, s46, s50\n"
"s_add_u32 s45, s45, 131072\n"
"s_add_u32 s49, s49, 16384\n"
"s_cmp_ge_u32 s49, 163840\n"
"s_cselect_b32 s50, 65536, 0\n"
"s_sub_u32 s49, s49, s50\n"
"v_mfma_f32_16x16x32_bf16 v[148:151], v[84:87], v[52:55], v[148:151]\n"
"v_mfma_f32_16x16x32_bf16 v[132:135], v[84:87], v[48:51], 0\n"
"v_mfma_f32_16x16x32_bf16 v[132:135], v[68:71], v[32:35], v[132:135]\n"
"v_mfma_f32_16x16x32_bf16 v[136:139], v[72:75], v[32:35], 0\n"
"v_mfma_f32_16x16x32_bf16 v[136:139], v[88:91], v[48:51], v[136:139]\n"
"v_mfma_f32_16x16x32_bf16 v[152:155], v[88:91], v[52:55], 0\n"
"v_mfma_f32_16x16x32_bf16 v[152:155], v[72:75], v[36:39], v[152:155]\n"
"v_mfma_f32_16x16x32_bf16 v[168:171], v[72:75], v[40:43], 0\n"
"v_mfma_f32_16x16x32_bf16 v[168:171], v[88:91], v[56:59], v[168:171]\n"
"v_mfma_f32_16x16x32_bf16 v[184:187], v[88:91], v[60:63], 0\n"
"v_mfma_f32_16x16x32_bf16 v[184:187], v[72:75], v[44:47], v[184:187]\n"
"v_mfma_f32_16x16x32_bf16 v[188:191], v[76:79], v[44:47], 0\n"
"v_mfma_f32_16x16x32_bf16 v[188:191], v[92:95], v[60:63], v[188:191]\n"
"v_mfma_f32_16x16x32_bf16 v[172:175], v[92:95], v[56:59], 0\n"
"v_mfma_f32_16x16x32_bf16 v[172:175], v[76:79], v[40:43], v[172:175]\n"
"v_mfma_f32_16x16x32_bf16 v[156:159], v[76:79], v[36:39], 0\n"
"v_mfma_f32_16x16x32_bf16 v[156:159], v[92:95], v[52:55], v[156:159]\n"
"v_mfma_f32_16x16x32_bf16 v[140:143], v[92:95], v[48:51], 0\n"
"v_mfma_f32_16x16x32_bf16 v[140:143], v[76:79], v[32:35], v[140:143]\n"
"s_barrier\n"
"s_xor_b32 m0, s46, 0\n"
"ds_read_b64_tr_b16 v[64:65], v26 offset:0\n"
"buffer_load_dwordx4 v16, s[24:27], s44 offen sc1 lds\n"
"ds_read_b64_tr_b16 v[66:67], v26 offset:512\n"
"ds_read_b64_tr_b16 v[68:69], v27 offset:0\n"
"ds_read_b64_tr_b16 v[70:71], v27 offset:512\n"
"s_xor_b32 m0, s46, 1024\n"
"ds_read_b64_tr_b16 v[72:73], v28 offset:0\n"
"buffer_load_dwordx4 v17, s[24:27], s44 offen sc1 lds\n"
"ds_read_b64_tr_b16 v[74:75], v28 offset:512\n"
"ds_read_b64_tr_b16 v[76:77], v29 offset:0\n"
"ds_read_b64_tr_b16 v[78:79], v29 offset:512\n"
"s_xor_b32 m0, s46, 2048\n"
"ds_read_b128 v[32:35], v24 offset:0\n"
"buffer_load_dwordx4 v18, s[24:27], s44 offen sc1 lds\n"
"ds_read_b128 v[36:39], v24 offset:2048\n"
"ds_read_b128 v[40:43], v24 offset:4096\n"
"ds_read_b128 v[44:47], v24 offset:6144\n"
"s_xor_b32 m0, s46, 3072\n"
"ds_read_b64_tr_b16 v[80:81], v26 offset:4096\n"
"buffer_load_dwordx4 v19, s[24:27], s44 offen sc1 lds\n"
"ds_read_b64_tr_b16 v[82:83], v26 offset:4608\n"
"ds_read_b64_tr_b16 v[84:85], v27 offset:4096\n"
"ds_read_b64_tr_b16 v[86:87], v27 offset:4608\n"
"s_add_u32 m0, s49, 0\n"
"ds_read_b64_tr_b16 v[88:89], v28 offset:4096\n"
"buffer_load_dwordx4 v20, s[28:31], s45 offen lds\n"
"ds_read_b64_tr_b16 v[90:91], v28 offset:4608\n"
"ds_read_b64_tr_b16 v[92:93], v29 offset:4096\n"
"ds_read_b64_tr_b16 v[94:95], v29 offset:4608\n"
"s_add_u32 m0, s49, 1024\n"
"ds_read_b128 v[48:51], v25 offset:0\n"
"buffer_load_dwordx4 v21, s[28:31], s45 offen lds\n"
"ds_read_b128 v[52:55], v25 offset:2048\n"
"ds_read_b128 v[56:59], v25 offset:4096\n"
"ds_read_b128 v[60:63], v25 offset:6144\n"
"s_mov_b32 s88, 65536\n"
"buffer_store_dwordx4 v[104:107], v30, s[40:43], s88 offen sc1\n"
"s_mov_b32 s88, 98304\n"
"buffer_store_dwordx4 v[108:111], v30, s[40:43], s88 offen sc1\n"
"s_waitcnt vmcnt(12) lgkmcnt(0)\n"
"s_barrier\n"
"v_mfma_f32_16x16x32_bf16 v[128:131], v[64:67], v[32:35], v[128:131]\n"
"v_mfma_f32_16x16x32_bf16 v[128:131], v[80:83], v[48:51], v[128:131]\n"
"s_add_u32 s68, s68, 1\n"
"s_cmp_eq_u32 s68, 3\n"
"s_cselect_b32 s48, 4294901760, s86\n"
"s_cselect_b32 s68, 0, s68\n"
"v_mfma_f32_16x16x32_bf16 v[144:147], v[80:83], v[52:55], v[144:147]\n"
"s_add_u32 s69, s69, 1\n"
"s_cmp_eq_u32 s69, 4\n"
"s_cselect_b32 s60, 4294918144, s87\n"
"s_cselect_b32 s69, 0, s69\n"
"v_mfma_f32_16x16x32_bf16 v[144:147], v[64:67], v[36:39], v[144:147]\n"
"v_mfma_f32_16x16x32_bf16 v[160:163], v[64:67], v[40:43], v[160:163]\n"
"v_add_u32 v24, s48, v24\n"
"v_mfma_f32_16x16x32_bf16 v[160:163], v[80:83], v[56:59], v[160:163]\n"
"v_add_u32 v25, s48, v25\n"
"v_mfma_f32_16x16x32_bf16 v[176:179], v[80:83], v[60:63], v[176:179]\n"
"v_add_u32 v26, s60, v26\n"
"v_mfma_f32_16x16x32_bf16 v[176:179], v[64:67], v[44:47], v[176:179]\n"
"v_add_u32 v27, s60, v27\n"
"v_mfma_f32_16x16x32_bf16 v[180:183], v[68:71], v[44:47], v[180:183]\n"
"v_add_u32 v28, s60, v28\n"
"v_mfma_f32_16x16x32_bf16 v[180:183], v[84:87], v[60:63], v[180:183]\n"
"v_add_u32 v29, s60, v29\n"
"v_mfma_f32_16x16x32_bf16 v[164:167], v[84:87], v[56:59], v[164:167]\n"
"v_mfma_f32_16x16x32_bf16 v[164:167], v[68:71], v[40:43], v[164:167]\n"
"v_mfma_f32_16x16x32_bf16 v[148:151], v[68:71], v[36:39], v[148:151]\n"
"s_add_u32 s44, s44, 128\n"
"s_add_u32 s46, s46, 32768\n"
"s_cmp_ge_u32 s46, 98304\n"
"s_cselect_b32 s50, 98304, 0\n"
"s_sub_u32 s46, s46, s50\n"
"s_add_u32 s45, s45, 131072\n"
"s_add_u32 s49, s49, 16384\n"
"s_cmp_ge_u32 s49, 163840\n"
"s_cselect_b32 s50, 65536, 0\n"
"s_sub_u32 s49, s49, s50\n"
"v_mfma_f32_16x16x32_bf16 v[148:151], v[84:87], v[52:55], v[148:151]\n"
"v_mfma_f32_16x16x32_bf16 v[132:135], v[84:87], v[48:51], v[132:135]\n"
"v_mfma_f32_16x16x32_bf16 v[132:135], v[68:71], v[32:35], v[132:135]\n"
"v_mfma_f32_16x16x32_bf16 v[136:139], v[72:75], v[32:35], v[136:139]\n"
"v_mfma_f32_16x16x32_bf16 v[136:139], v[88:91], v[48:51], v[136:139]\n"
"v_mfma_f32_16x16x32_bf16 v[152:155], v[88:91], v[52:55], v[152:155]\n"
"v_mfma_f32_16x16x32_bf16 v[152:155], v[72:75], v[36:39], v[152:155]\n"
"v_mfma_f32_16x16x32_bf16 v[168:171], v[72:75], v[40:43], v[168:171]\n"
"v_mfma_f32_16x16x32_bf16 v[168:171], v[88:91], v[56:59], v[168:171]\n"
"v_mfma_f32_16x16x32_bf16 v[184:187], v[88:91], v[60:63], v[184:187]\n"
"v_mfma_f32_16x16x32_bf16 v[184:187], v[72:75], v[44:47], v[184:187]\n"
"v_mfma_f32_16x16x32_bf16 v[188:191], v[76:79], v[44:47], v[188:191]\n"
"v_mfma_f32_16x16x32_bf16 v[188:191], v[92:95], v[60:63], v[188:191]\n"
"v_mfma_f32_16x16x32_bf16 v[172:175], v[92:95], v[56:59], v[172:175]\n"
"v_mfma_f32_16x16x32_bf16 v[172:175], v[76:79], v[40:43], v[172:175]\n"
"v_mfma_f32_16x16x32_bf16 v[156:159], v[76:79], v[36:39], v[156:159]\n"
"v_mfma_f32_16x16x32_bf16 v[156:159], v[92:95], v[52:55], v[156:159]\n"
"v_mfma_f32_16x16x32_bf16 v[140:143], v[92:95], v[48:51], v[140:143]\n"
"v_mfma_f32_16x16x32_bf16 v[140:143], v[76:79], v[32:35], v[140:143]\n"
"s_barrier\n"
"s_xor_b32 m0, s46, 0\n"
"ds_read_b64_tr_b16 v[64:65], v26 offset:0\n"
"buffer_load_dwordx4 v16, s[24:27], s44 offen sc1 lds\n"
"ds_read_b64_tr_b16 v[66:67], v26 offset:512\n"
"ds_read_b64_tr_b16 v[68:69], v27 offset:0\n"
"ds_read_b64_tr_b16 v[70:71], v27 offset:512\n"
"s_xor_b32 m0, s46, 1024\n"
"ds_read_b64_tr_b16 v[72:73], v28 offset:0\n"
"buffer_load_dwordx4 v17, s[24:27], s44 offen sc1 lds\n"
"ds_read_b64_tr_b16 v[74:75], v28 offset:512\n"
"ds_read_b64_tr_b16 v[76:77], v29 offset:0\n"
"ds_read_b64_tr_b16 v[78:79], v29 offset:512\n"
"s_xor_b32 m0, s46, 2048\n"
"ds_read_b128 v[32:35], v24 offset:0\n"
"buffer_load_dwordx4 v18, s[24:27], s44 offen sc1 lds\n"
"ds_read_b128 v[36:39], v24 offset:2048\n"
"ds_read_b128 v[40:43], v24 offset:4096\n"
"ds_read_b128 v[44:47], v24 offset:6144\n"
"s_xor_b32 m0, s46, 3072\n"
"ds_read_b64_tr_b16 v[80:81], v26 offset:4096\n"
"buffer_load_dwordx4 v19, s[24:27], s44 offen sc1 lds\n"
"ds_read_b64_tr_b16 v[82:83], v26 offset:4608\n"
"ds_read_b64_tr_b16 v[84:85], v27 offset:4096\n"
"ds_read_b64_tr_b16 v[86:87], v27 offset:4608\n"
"s_add_u32 m0, s49, 0\n"
"ds_read_b64_tr_b16 v[88:89], v28 offset:4096\n"
"buffer_load_dwordx4 v20, s[28:31], s45 offen lds\n"
"ds_read_b64_tr_b16 v[90:91], v28 offset:4608\n"
"ds_read_b64_tr_b16 v[92:93], v29 offset:4096\n"
"ds_read_b64_tr_b16 v[94:95], v29 offset:4608\n"
"s_add_u32 m0, s49, 1024\n"
"ds_read_b128 v[48:51], v25 offset:0\n"
"buffer_load_dwordx4 v21, s[28:31], s45 offen lds\n"
"ds_read_b128 v[52:55], v25 offset:2048\n"
"ds_read_b128 v[56:59], v25 offset:4096\n"
"ds_read_b128 v[60:63], v25 offset:6144\n"
"s_mov_b32 s88, 131072\n"
"buffer_store_dwordx4 v[112:115], v30, s[40:43], s88 offen sc1\n"
"s_waitcnt vmcnt(11) lgkmcnt(0)\n"
"s_barrier\n"
"v_mfma_f32_16x16x32_bf16 v[128:131], v[64:67], v[32:35], v[128:131]\n"
"v_mfma_f32_16x16x32_bf16 v[128:131], v[80:83], v[48:51], v[128:131]\n"
"s_add_u32 s68, s68, 1\n"
"s_cmp_eq_u32 s68, 3\n"
"s_cselect_b32 s48, 4294901760, s86\n"
"s_cselect_b32 s68, 0, s68\n"
"v_mfma_f32_16x16x32_bf16 v[144:147], v[80:83], v[52:55], v[144:147]\n"
"s_add_u32 s69, s69, 1\n"
"s_cmp_eq_u32 s69, 4\n"
"s_cselect_b32 s60, 4294918144, s87\n"
"s_cselect_b32 s69, 0, s69\n"
"v_mfma_f32_16x16x32_bf16 v[144:147], v[64:67], v[36:39], v[144:147]\n"
"v_mfma_f32_16x16x32_bf16 v[160:163], v[64:67], v[40:43], v[160:163]\n"
"v_add_u32 v24, s48, v24\n"
"v_mfma_f32_16x16x32_bf16 v[160:163], v[80:83], v[56:59], v[160:163]\n"
"v_add_u32 v25, s48, v25\n"
"v_mfma_f32_16x16x32_bf16 v[176:179], v[80:83], v[60:63], v[176:179]\n"
"v_add_u32 v26, s60, v26\n"
"v_mfma_f32_16x16x32_bf16 v[176:179], v[64:67], v[44:47], v[176:179]\n"
"v_add_u32 v27, s60, v27\n"
"v_mfma_f32_16x16x32_bf16 v[180:183], v[68:71], v[44:47], v[180:183]\n"
"v_add_u32 v28, s60, v28\n"
"v_mfma_f32_16x16x32_bf16 v[180:183], v[84:87], v[60:63], v[180:183]\n"
"v_add_u32 v29, s60, v29\n"
"v_mfma_f32_16x16x32_bf16 v[164:167], v[84:87], v[56:59], v[164:167]\n"
"v_mfma_f32_16x16x32_bf16 v[164:167], v[68:71], v[40:43], v[164:167]\n"
"v_mfma_f32_16x16x32_bf16 v[148:151], v[68:71], v[36:39], v[148:151]\n"
"s_add_u32 s44, s44, 128\n"
"s_add_u32 s46, s46, 32768\n"
"s_cmp_ge_u32 s46, 98304\n"
"s_cselect_b32 s50, 98304, 0\n"
"s_sub_u32 s46, s46, s50\n"
"s_add_u32 s45, s45, 131072\n"
"s_add_u32 s49, s49, 16384\n"
"s_cmp_ge_u32 s49, 163840\n"
"s_cselect_b32 s50, 65536, 0\n"
"s_sub_u32 s49, s49, s50\n"
"v_mfma_f32_16x16x32_bf16 v[148:151], v[84:87], v[52:55], v[148:151]\n"
"v_mfma_f32_16x16x32_bf16 v[132:135], v[84:87], v[48:51], v[132:135]\n"
"v_mfma_f32_16x16x32_bf16 v[132:135], v[68:71], v[32:35], v[132:135]\n"
"v_mfma_f32_16x16x32_bf16 v[136:139], v[72:75], v[32:35], v[136:139]\n"
"v_mfma_f32_16x16x32_bf16 v[136:139], v[88:91], v[48:51], v[136:139]\n"
"v_mfma_f32_16x16x32_bf16 v[152:155], v[88:91], v[52:55], v[152:155]\n"
"v_mfma_f32_16x16x32_bf16 v[152:155], v[72:75], v[36:39], v[152:155]\n"
"v_mfma_f32_16x16x32_bf16 v[168:171], v[72:75], v[40:43], v[168:171]\n"
"v_mfma_f32_16x16x32_bf16 v[168:171], v[88:91], v[56:59], v[168:171]\n"
"v_mfma_f32_16x16x32_bf16 v[184:187], v[88:91], v[60:63], v[184:187]\n"
"v_mfma_f32_16x16x32_bf16 v[184:187], v[72:75], v[44:47], v[184:187]\n"
"v_mfma_f32_16x16x32_bf16 v[188:191], v[76:79], v[44:47], v[188:191]\n"
"v_mfma_f32_16x16x32_bf16 v[188:191], v[92:95], v[60:63], v[188:191]\n"
"v_mfma_f32_16x16x32_bf16 v[172:175], v[92:95], v[56:59], v[172:175]\n"
"v_mfma_f32_16x16x32_bf16 v[172:175], v[76:79], v[40:43], v[172:175]\n"
"v_mfma_f32_16x16x32_bf16 v[156:159], v[76:79], v[36:39], v[156:159]\n"
"v_mfma_f32_16x16x32_bf16 v[156:159], v[92:95], v[52:55], v[156:159]\n"
"v_mfma_f32_16x16x32_bf16 v[140:143], v[92:95], v[48:51], v[140:143]\n"
"v_mfma_f32_16x16x32_bf16 v[140:143], v[76:79], v[32:35], v[140:143]\n"
"s_barrier\n"
"s_xor_b32 m0, s46, 0\n"
"ds_read_b64_tr_b16 v[64:65], v26 offset:0\n"
"buffer_load_dwordx4 v16, s[24:27], s44 offen sc1 lds\n"
"ds_read_b64_tr_b16 v[66:67], v26 offset:512\n"
"ds_read_b64_tr_b16 v[68:69], v27 offset:0\n"
"ds_read_b64_tr_b16 v[70:71], v27 offset:512\n"
"s_xor_b32 m0, s46, 1024\n"
"ds_read_b64_tr_b16 v[72:73], v28 offset:0\n"
"buffer_load_dwordx4 v17, s[24:27], s44 offen sc1 lds\n"
"ds_read_b64_tr_b16 v[74:75], v28 offset:512\n"
"ds_read_b64_tr_b16 v[76:77], v29 offset:0\n"
"ds_read_b64_tr_b16 v[78:79], v29 offset:512\n"
"s_xor_b32 m0, s46, 2048\n"
"ds_read_b128 v[32:35], v24 offset:0\n"
"buffer_load_dwordx4 v18, s[24:27], s44 offen sc1 lds\n"
"ds_read_b128 v[36:39], v24 offset:2048\n"
"ds_read_b128 v[40:43], v24 offset:4096\n"
"ds_read_b128 v[44:47], v24 offset:6144\n"
"s_xor_b32 m0, s46, 3072\n"
"ds_read_b64_tr_b16 v[80:81], v26 offset:4096\n"
"buffer_load_dwordx4 v19, s[24:27], s44 offen sc1 lds\n"
"ds_read_b64_tr_b16 v[82:83], v26 offset:4608\n"
"ds_read_b64_tr_b16 v[84:85], v27 offset:4096\n"
"ds_read_b64_tr_b16 v[86:87], v27 offset:4608\n"
"s_add_u32 m0, s49, 0\n"
"ds_read_b64_tr_b16 v[88:89], v28 offset:4096\n"
"buffer_load_dwordx4 v20, s[28:31], s45 offen lds\n"
"ds_read_b64_tr_b16 v[90:91], v28 offset:4608\n"
"ds_read_b64_tr_b16 v[92:93], v29 offset:4096\n"
"ds_read_b64_tr_b16 v[94:95], v29 offset:4608\n"
"s_add_u32 m0, s49, 1024\n"
"ds_read_b128 v[48:51], v25 offset:0\n"
"buffer_load_dwordx4 v21, s[28:31], s45 offen lds\n"
"ds_read_b128 v[52:55], v25 offset:2048\n"
"ds_read_b128 v[56:59], v25 offset:4096\n"
"ds_read_b128 v[60:63], v25 offset:6144\n"
"s_mov_b32 s88, 163840\n"
"buffer_store_dwordx4 v[116:119], v30, s[40:43], s88 offen sc1\n"
"s_waitcnt vmcnt(10) lgkmcnt(0)\n"
"s_barrier\n"
"v_mfma_f32_16x16x32_bf16 v[128:131], v[64:67], v[32:35], v[128:131]\n"
"v_mfma_f32_16x16x32_bf16 v[128:131], v[80:83], v[48:51], v[128:131]\n"
"s_add_u32 s68, s68, 1\n"
"s_cmp_eq_u32 s68, 3\n"
"s_cselect_b32 s48, 4294901760, s86\n"
"s_cselect_b32 s68, 0, s68\n"
"v_mfma_f32_16x16x32_bf16 v[144:147], v[80:83], v[52:55], v[144:147]\n"
"s_add_u32 s69, s69, 1\n"
"s_cmp_eq_u32 s69, 4\n"
"s_cselect_b32 s60, 4294918144, s87\n"
"s_cselect_b32 s69, 0, s69\n"
"v_mfma_f32_16x16x32_bf16 v[144:147], v[64:67], v[36:39], v[144:147]\n"
"v_mfma_f32_16x16x32_bf16 v[160:163], v[64:67], v[40:43], v[160:163]\n"
"v_add_u32 v24, s48, v24\n"
"v_mfma_f32_16x16x32_bf16 v[160:163], v[80:83], v[56:59], v[160:163]\n"
"v_add_u32 v25, s48, v25\n"
"v_mfma_f32_16x16x32_bf16 v[176:179], v[80:83], v[60:63], v[176:179]\n"
"v_add_u32 v26, s60, v26\n"
"v_mfma_f32_16x16x32_bf16 v[176:179], v[64:67], v[44:47], v[176:179]\n"
"v_add_u32 v27, s60, v27\n"
"v_mfma_f32_16x16x32_bf16 v[180:183], v[68:71], v[44:47], v[180:183]\n"
"v_add_u32 v28, s60, v28\n"
"v_mfma_f32_16x16x32_bf16 v[180:183], v[84:87], v[60:63], v[180:183]\n"
"v_add_u32 v29, s60, v29\n"
"v_mfma_f32_16x16x32_bf16 v[164:167], v[84:87], v[56:59], v[164:167]\n"
"v_mfma_f32_16x16x32_bf16 v[164:167], v[68:71], v[40:43], v[164:167]\n"
"v_mfma_f32_16x16x32_bf16 v[148:151], v[68:71], v[36:39], v[148:151]\n"
"s_add_u32 s44, s44, 128\n"
"s_add_u32 s46, s46, 32768\n"
"s_cmp_ge_u32 s46, 98304\n"
"s_cselect_b32 s50, 98304, 0\n"
"s_sub_u32 s46, s46, s50\n"
"s_add_u32 s45, s45, 131072\n"
"s_add_u32 s49, s49, 16384\n"
"s_cmp_ge_u32 s49, 163840\n"
"s_cselect_b32 s50, 65536, 0\n"
"s_sub_u32 s49, s49, s50\n"
"v_mfma_f32_16x16x32_bf16 v[148:151], v[84:87], v[52:55], v[148:151]\n"
"v_mfma_f32_16x16x32_bf16 v[132:135], v[84:87], v[48:51], v[132:135]\n"
"v_mfma_f32_16x16x32_bf16 v[132:135], v[68:71], v[32:35], v[132:135]\n"
"v_mfma_f32_16x16x32_bf16 v[136:139], v[72:75], v[32:35], v[136:139]\n"
"v_mfma_f32_16x16x32_bf16 v[136:139], v[88:91], v[48:51], v[136:139]\n"
"v_mfma_f32_16x16x32_bf16 v[152:155], v[88:91], v[52:55], v[152:155]\n"
"v_mfma_f32_16x16x32_bf16 v[152:155], v[72:75], v[36:39], v[152:155]\n"
"v_mfma_f32_16x16x32_bf16 v[168:171], v[72:75], v[40:43], v[168:171]\n"
"v_mfma_f32_16x16x32_bf16 v[168:171], v[88:91], v[56:59], v[168:171]\n"
"v_mfma_f32_16x16x32_bf16 v[184:187], v[88:91], v[60:63], v[184:187]\n"
"v_mfma_f32_16x16x32_bf16 v[184:187], v[72:75], v[44:47], v[184:187]\n"
"v_mfma_f32_16x16x32_bf16 v[188:191], v[76:79], v[44:47], v[188:191]\n"
"v_mfma_f32_16x16x32_bf16 v[188:191], v[92:95], v[60:63], v[188:191]\n"
"v_mfma_f32_16x16x32_bf16 v[172:175], v[92:95], v[56:59], v[172:175]\n"
"v_mfma_f32_16x16x32_bf16 v[172:175], v[76:79], v[40:43], v[172:175]\n"
"v_mfma_f32_16x16x32_bf16 v[156:159], v[76:79], v[36:39], v[156:159]\n"
"v_mfma_f32_16x16x32_bf16 v[156:159], v[92:95], v[52:55], v[156:159]\n"
"v_mfma_f32_16x16x32_bf16 v[140:143], v[92:95], v[48:51], v[140:143]\n"
"v_mfma_f32_16x16x32_bf16 v[140:143], v[76:79], v[32:35], v[140:143]\n"
"s_barrier\n"
"s_xor_b32 m0, s46, 0\n"
"ds_read_b64_tr_b16 v[64:65], v26 offset:0\n"
"buffer_load_dwordx4 v16, s[24:27], s44 offen sc1 lds\n"
"ds_read_b64_tr_b16 v[66:67], v26 offset:512\n"
"ds_read_b64_tr_b16 v[68:69], v27 offset:0\n"
"ds_read_b64_tr_b16 v[70:71], v27 offset:512\n"
"s_xor_b32 m0, s46, 1024\n"
"ds_read_b64_tr_b16 v[72:73], v28 offset:0\n"
"buffer_load_dwordx4 v17, s[24:27], s44 offen sc1 lds\n"
"ds_read_b64_tr_b16 v[74:75], v28 offset:512\n"
"ds_read_b64_tr_b16 v[76:77], v29 offset:0\n"
"ds_read_b64_tr_b16 v[78:79], v29 offset:512\n"
"s_xor_b32 m0, s46, 2048\n"
"ds_read_b128 v[32:35], v24 offset:0\n"
"buffer_load_dwordx4 v18, s[24:27], s44 offen sc1 lds\n"
"ds_read_b128 v[36:39], v24 offset:2048\n"
"ds_read_b128 v[40:43], v24 offset:4096\n"
"ds_read_b128 v[44:47], v24 offset:6144\n"
"s_xor_b32 m0, s46, 3072\n"
"ds_read_b64_tr_b16 v[80:81], v26 offset:4096\n"
"buffer_load_dwordx4 v19, s[24:27], s44 offen sc1 lds\n"
"ds_read_b64_tr_b16 v[82:83], v26 offset:4608\n"
"ds_read_b64_tr_b16 v[84:85], v27 offset:4096\n"
"ds_read_b64_tr_b16 v[86:87], v27 offset:4608\n"
"s_add_u32 m0, s49, 0\n"
"ds_read_b64_tr_b16 v[88:89], v28 offset:4096\n"
"buffer_load_dwordx4 v20, s[28:31], s45 offen lds\n"
"ds_read_b64_tr_b16 v[90:91], v28 offset:4608\n"
"ds_read_b64_tr_b16 v[92:93], v29 offset:4096\n"
"ds_read_b64_tr_b16 v[94:95], v29 offset:4608\n"
"s_add_u32 m0, s49, 1024\n"
"ds_read_b128 v[48:51], v25 offset:0\n"
"buffer_load_dwordx4 v21, s[28:31], s45 offen lds\n"
"ds_read_b128 v[52:55], v25 offset:2048\n"
"ds_read_b128 v[56:59], v25 offset:4096\n"
"ds_read_b128 v[60:63], v25 offset:6144\n"
"s_mov_b32 s88, 196608\n"
"buffer_store_dwordx4 v[120:123], v30, s[40:43], s88 offen sc1\n"
"s_waitcnt vmcnt(10) lgkmcnt(0)\n"
"s_barrier\n"
"v_mfma_f32_16x16x32_bf16 v[128:131], v[64:67], v[32:35], v[128:131]\n"
"v_mfma_f32_16x16x32_bf16 v[128:131], v[80:83], v[48:51], v[128:131]\n"
"s_add_u32 s68, s68, 1\n"
"s_cmp_eq_u32 s68, 3\n"
"s_cselect_b32 s48, 4294901760, s86\n"
"s_cselect_b32 s68, 0, s68\n"
"v_mfma_f32_16x16x32_bf16 v[144:147], v[80:83], v[52:55], v[144:147]\n"
"s_add_u32 s69, s69, 1\n"
"s_cmp_eq_u32 s69, 4\n"
"s_cselect_b32 s60, 4294918144, s87\n"
"s_cselect_b32 s69, 0, s69\n"
"v_mfma_f32_16x16x32_bf16 v[144:147], v[64:67], v[36:39], v[144:147]\n"
"v_mfma_f32_16x16x32_bf16 v[160:163], v[64:67], v[40:43], v[160:163]\n"
"v_add_u32 v24, s48, v24\n"
"v_mfma_f32_16x16x32_bf16 v[160:163], v[80:83], v[56:59], v[160:163]\n"
"v_add_u32 v25, s48, v25\n"
"v_mfma_f32_16x16x32_bf16 v[176:179], v[80:83], v[60:63], v[176:179]\n"
"v_add_u32 v26, s60, v26\n"
"v_mfma_f32_16x16x32_bf16 v[176:179], v[64:67], v[44:47], v[176:179]\n"
"v_add_u32 v27, s60, v27\n"
"v_mfma_f32_16x16x32_bf16 v[180:183], v[68:71], v[44:47], v[180:183]\n"
"v_add_u32 v28, s60, v28\n"
"v_mfma_f32_16x16x32_bf16 v[180:183], v[84:87], v[60:63], v[180:183]\n"
"v_add_u32 v29, s60, v29\n"
"v_mfma_f32_16x16x32_bf16 v[164:167], v[84:87], v[56:59], v[164:167]\n"
"v_mfma_f32_16x16x32_bf16 v[164:167], v[68:71], v[40:43], v[164:167]\n"
"v_mfma_f32_16x16x32_bf16 v[148:151], v[68:71], v[36:39], v[148:151]\n"
"s_add_u32 s44, s44, 128\n"
"s_add_u32 s46, s46, 32768\n"
"s_cmp_ge_u32 s46, 98304\n"
"s_cselect_b32 s50, 98304, 0\n"
"s_sub_u32 s46, s46, s50\n"
"s_add_u32 s45, s45, 131072\n"
"s_add_u32 s49, s49, 16384\n"
"s_cmp_ge_u32 s49, 163840\n"
"s_cselect_b32 s50, 65536, 0\n"
"s_sub_u32 s49, s49, s50\n"
"v_mfma_f32_16x16x32_bf16 v[148:151], v[84:87], v[52:55], v[148:151]\n"
"v_mfma_f32_16x16x32_bf16 v[132:135], v[84:87], v[48:51], v[132:135]\n"
"v_mfma_f32_16x16x32_bf16 v[132:135], v[68:71], v[32:35], v[132:135]\n"
"v_mfma_f32_16x16x32_bf16 v[136:139], v[72:75], v[32:35], v[136:139]\n"
"v_mfma_f32_16x16x32_bf16 v[136:139], v[88:91], v[48:51], v[136:139]\n"
"v_mfma_f32_16x16x32_bf16 v[152:155], v[88:91], v[52:55], v[152:155]\n"
"v_mfma_f32_16x16x32_bf16 v[152:155], v[72:75], v[36:39], v[152:155]\n"
"v_mfma_f32_16x16x32_bf16 v[168:171], v[72:75], v[40:43], v[168:171]\n"
"v_mfma_f32_16x16x32_bf16 v[168:171], v[88:91], v[56:59], v[168:171]\n"
"v_mfma_f32_16x16x32_bf16 v[184:187], v[88:91], v[60:63], v[184:187]\n"
"v_mfma_f32_16x16x32_bf16 v[184:187], v[72:75], v[44:47], v[184:187]\n"
"v_mfma_f32_16x16x32_bf16 v[188:191], v[76:79], v[44:47], v[188:191]\n"
"v_mfma_f32_16x16x32_bf16 v[188:191], v[92:95], v[60:63], v[188:191]\n"
"v_mfma_f32_16x16x32_bf16 v[172:175], v[92:95], v[56:59], v[172:175]\n"
"v_mfma_f32_16x16x32_bf16 v[172:175], v[76:79], v[40:43], v[172:175]\n"
"v_mfma_f32_16x16x32_bf16 v[156:159], v[76:79], v[36:39], v[156:159]\n"
"v_mfma_f32_16x16x32_bf16 v[156:159], v[92:95], v[52:55], v[156:159]\n"
"v_mfma_f32_16x16x32_bf16 v[140:143], v[92:95], v[48:51], v[140:143]\n"
"v_mfma_f32_16x16x32_bf16 v[140:143], v[76:79], v[32:35], v[140:143]\n"
"s_barrier\n"
"s_xor_b32 m0, s46, 0\n"
"ds_read_b64_tr_b16 v[64:65], v26 offset:0\n"
"buffer_load_dwordx4 v16, s[24:27], s44 offen sc1 lds\n"
"ds_read_b64_tr_b16 v[66:67], v26 offset:512\n"
"ds_read_b64_tr_b16 v[68:69], v27 offset:0\n"
"ds_read_b64_tr_b16 v[70:71], v27 offset:512\n"
"s_xor_b32 m0, s46, 1024\n"
"ds_read_b64_tr_b16 v[72:73], v28 offset:0\n"
"buffer_load_dwordx4 v17, s[24:27], s44 offen sc1 lds\n"
"ds_read_b64_tr_b16 v[74:75], v28 offset:512\n"
"ds_read_b64_tr_b16 v[76:77], v29 offset:0\n"
"ds_read_b64_tr_b16 v[78:79], v29 offset:512\n"
"s_xor_b32 m0, s46, 2048\n"
"ds_read_b128 v[32:35], v24 offset:0\n"
"buffer_load_dwordx4 v18, s[24:27], s44 offen sc1 lds\n"
"ds_read_b128 v[36:39], v24 offset:2048\n"
"ds_read_b128 v[40:43], v24 offset:4096\n"
"ds_read_b128 v[44:47], v24 offset:6144\n"
"s_xor_b32 m0, s46, 3072\n"
"ds_read_b64_tr_b16 v[80:81], v26 offset:4096\n"
"buffer_load_dwordx4 v19, s[24:27], s44 offen sc1 lds\n"
"ds_read_b64_tr_b16 v[82:83], v26 offset:4608\n"
"ds_read_b64_tr_b16 v[84:85], v27 offset:4096\n"
"ds_read_b64_tr_b16 v[86:87], v27 offset:4608\n"
"s_add_u32 m0, s49, 0\n"
"ds_read_b64_tr_b16 v[88:89], v28 offset:4096\n"
"buffer_load_dwordx4 v20, s[28:31], s45 offen lds\n"
"ds_read_b64_tr_b16 v[90:91], v28 offset:4608\n"
"ds_read_b64_tr_b16 v[92:93], v29 offset:4096\n"
"ds_read_b64_tr_b16 v[94:95], v29 offset:4608\n"
"s_add_u32 m0, s49, 1024\n"
"ds_read_b128 v[48:51], v25 offset:0\n"
"buffer_load_dwordx4 v21, s[28:31], s45 offen lds\n"
"ds_read_b128 v[52:55], v25 offset:2048\n"
"ds_read_b128 v[56:59], v25 offset:4096\n"
"ds_read_b128 v[60:63], v25 offset:6144\n"
"s_mov_b32 s88, 229376\n"
"buffer_store_dwordx4 v[124:127], v30, s[40:43], s88 offen sc1\n"
"s_waitcnt vmcnt(10) lgkmcnt(0)\n"
"s_barrier\n"
"v_mfma_f32_16x16x32_bf16 v[128:131], v[64:67], v[32:35], v[128:131]\n"
"v_mfma_f32_16x16x32_bf16 v[128:131], v[80:83], v[48:51], v[128:131]\n"
"s_add_u32 s68, s68, 1\n"
"s_cmp_eq_u32 s68, 3\n"
"s_cselect_b32 s48, 4294901760, s86\n"
"s_cselect_b32 s68, 0, s68\n"
"v_mfma_f32_16x16x32_bf16 v[144:147], v[80:83], v[52:55], v[144:147]\n"
"s_add_u32 s69, s69, 1\n"
"s_cmp_eq_u32 s69, 4\n"
"s_cselect_b32 s60, 4294918144, s87\n"
"s_cselect_b32 s69, 0, s69\n"
"v_mfma_f32_16x16x32_bf16 v[144:147], v[64:67], v[36:39], v[144:147]\n"
"v_mfma_f32_16x16x32_bf16 v[160:163], v[64:67], v[40:43], v[160:163]\n"
"v_add_u32 v24, s48, v24\n"
"v_mfma_f32_16x16x32_bf16 v[160:163], v[80:83], v[56:59], v[160:163]\n"
"v_add_u32 v25, s48, v25\n"
"v_mfma_f32_16x16x32_bf16 v[176:179], v[80:83], v[60:63], v[176:179]\n"
"v_add_u32 v26, s60, v26\n"
"v_mfma_f32_16x16x32_bf16 v[176:179], v[64:67], v[44:47], v[176:179]\n"
"v_add_u32 v27, s60, v27\n"
"v_mfma_f32_16x16x32_bf16 v[180:183], v[68:71], v[44:47], v[180:183]\n"
"v_add_u32 v28, s60, v28\n"
"v_mfma_f32_16x16x32_bf16 v[180:183], v[84:87], v[60:63], v[180:183]\n"
"v_add_u32 v29, s60, v29\n"
"v_mfma_f32_16x16x32_bf16 v[164:167], v[84:87], v[56:59], v[164:167]\n"
"v_mfma_f32_16x16x32_bf16 v[164:167], v[68:71], v[40:43], v[164:167]\n"
"v_mfma_f32_16x16x32_bf16 v[148:151], v[68:71], v[36:39], v[148:151]\n"
"s_add_u32 s44, s44, 128\n"
"s_add_u32 s46, s46, 32768\n"
"s_cmp_ge_u32 s46, 98304\n"
"s_cselect_b32 s50, 98304, 0\n"
"s_sub_u32 s46, s46, s50\n"
"s_add_u32 s45, s45, 131072\n"
"s_add_u32 s49, s49, 16384\n"
"s_cmp_ge_u32 s49, 163840\n"
"s_cselect_b32 s50, 65536, 0\n"
"s_sub_u32 s49, s49, s50\n"
"v_mfma_f32_16x16x32_bf16 v[148:151], v[84:87], v[52:55], v[148:151]\n"
"v_mfma_f32_16x16x32_bf16 v[132:135], v[84:87], v[48:51], v[132:135]\n"
"v_mfma_f32_16x16x32_bf16 v[132:135], v[68:71], v[32:35], v[132:135]\n"
"v_mfma_f32_16x16x32_bf16 v[136:139], v[72:75], v[32:35], v[136:139]\n"
"v_mfma_f32_16x16x32_bf16 v[136:139], v[88:91], v[48:51], v[136:139]\n"
"v_mfma_f32_16x16x32_bf16 v[152:155], v[88:91], v[52:55], v[152:155]\n"
"v_mfma_f32_16x16x32_bf16 v[152:155], v[72:75], v[36:39], v[152:155]\n"
"v_mfma_f32_16x16x32_bf16 v[168:171], v[72:75], v[40:43], v[168:171]\n"
"v_mfma_f32_16x16x32_bf16 v[168:171], v[88:91], v[56:59], v[168:171]\n"
"v_mfma_f32_16x16x32_bf16 v[184:187], v[88:91], v[60:63], v[184:187]\n"
"v_mfma_f32_16x16x32_bf16 v[184:187], v[72:75], v[44:47], v[184:187]\n"
"v_mfma_f32_16x16x32_bf16 v[188:191], v[76:79], v[44:47], v[188:191]\n"
"v_mfma_f32_16x16x32_bf16 v[188:191], v[92:95], v[60:63], v[188:191]\n"
"v_mfma_f32_16x16x32_bf16 v[172:175], v[92:95], v[56:59], v[172:175]\n"
"v_mfma_f32_16x16x32_bf16 v[172:175], v[76:79], v[40:43], v[172:175]\n"
"v_mfma_f32_16x16x32_bf16 v[156:159], v[76:79], v[36:39], v[156:159]\n"
"v_mfma_f32_16x16x32_bf16 v[156:159], v[92:95], v[52:55], v[156:159]\n"
"v_mfma_f32_16x16x32_bf16 v[140:143], v[92:95], v[48:51], v[140:143]\n"
"v_mfma_f32_16x16x32_bf16 v[140:143], v[76:79], v[32:35], v[140:143]\n"
"s_barrier\n"
"s_xor_b32 m0, s46, 0\n"
"ds_read_b64_tr_b16 v[64:65], v26 offset:0\n"
"buffer_load_dwordx4 v16, s[24:27], s44 offen sc1 lds\n"
"ds_read_b64_tr_b16 v[66:67], v26 offset:512\n"
"ds_read_b64_tr_b16 v[68:69], v27 offset:0\n"
"ds_read_b64_tr_b16 v[70:71], v27 offset:512\n"
"s_xor_b32 m0, s46, 1024\n"
"ds_read_b64_tr_b16 v[72:73], v28 offset:0\n"
"buffer_load_dwordx4 v17, s[24:27], s44 offen sc1 lds\n"
"ds_read_b64_tr_b16 v[74:75], v28 offset:512\n"
"ds_read_b64_tr_b16 v[76:77], v29 offset:0\n"
"ds_read_b64_tr_b16 v[78:79], v29 offset:512\n"
"s_xor_b32 m0, s46, 2048\n"
"ds_read_b128 v[32:35], v24 offset:0\n"
"buffer_load_dwordx4 v18, s[24:27], s44 offen sc1 lds\n"
"ds_read_b128 v[36:39], v24 offset:2048\n"
"ds_read_b128 v[40:43], v24 offset:4096\n"
"ds_read_b128 v[44:47], v24 offset:6144\n"
"s_xor_b32 m0, s46, 3072\n"
"ds_read_b64_tr_b16 v[80:81], v26 offset:4096\n"
"buffer_load_dwordx4 v19, s[24:27], s44 offen sc1 lds\n"
"ds_read_b64_tr_b16 v[82:83], v26 offset:4608\n"
"ds_read_b64_tr_b16 v[84:85], v27 offset:4096\n"
"ds_read_b64_tr_b16 v[86:87], v27 offset:4608\n"
"s_add_u32 m0, s49, 0\n"
"ds_read_b64_tr_b16 v[88:89], v28 offset:4096\n"
"buffer_load_dwordx4 v20, s[28:31], s45 offen lds\n"
"ds_read_b64_tr_b16 v[90:91], v28 offset:4608\n"
"ds_read_b64_tr_b16 v[92:93], v29 offset:4096\n"
"ds_read_b64_tr_b16 v[94:95], v29 offset:4608\n"
"s_add_u32 m0, s49, 1024\n"
"ds_read_b128 v[48:51], v25 offset:0\n"
"buffer_load_dwordx4 v21, s[28:31], s45 offen lds\n"
"ds_read_b128 v[52:55], v25 offset:2048\n"
"ds_read_b128 v[56:59], v25 offset:4096\n"
"ds_read_b128 v[60:63], v25 offset:6144\n"
"s_waitcnt vmcnt(9) lgkmcnt(0)\n"
"s_barrier\n"
"v_mfma_f32_16x16x32_bf16 v[128:131], v[64:67], v[32:35], v[128:131]\n"
"v_mfma_f32_16x16x32_bf16 v[128:131], v[80:83], v[48:51], v[128:131]\n"
"s_add_u32 s68, s68, 1\n"
"s_cmp_eq_u32 s68, 3\n"
"s_cselect_b32 s48, 4294901760, s86\n"
"s_cselect_b32 s68, 0, s68\n"
"v_mfma_f32_16x16x32_bf16 v[144:147], v[80:83], v[52:55], v[144:147]\n"
"s_add_u32 s69, s69, 1\n"
"s_cmp_eq_u32 s69, 4\n"
"s_cselect_b32 s60, 4294918144, s87\n"
"s_cselect_b32 s69, 0, s69\n"
"v_mfma_f32_16x16x32_bf16 v[144:147], v[64:67], v[36:39], v[144:147]\n"
"v_mfma_f32_16x16x32_bf16 v[160:163], v[64:67], v[40:43], v[160:163]\n"
"v_add_u32 v24, s48, v24\n"
"v_mfma_f32_16x16x32_bf16 v[160:163], v[80:83], v[56:59], v[160:163]\n"
"v_add_u32 v25, s48, v25\n"
"v_mfma_f32_16x16x32_bf16 v[176:179], v[80:83], v[60:63], v[176:179]\n"
"v_add_u32 v26, s60, v26\n"
"v_mfma_f32_16x16x32_bf16 v[176:179], v[64:67], v[44:47], v[176:179]\n"
"v_add_u32 v27, s60, v27\n"
"v_mfma_f32_16x16x32_bf16 v[180:183], v[68:71], v[44:47], v[180:183]\n"
"v_add_u32 v28, s60, v28\n"
"v_mfma_f32_16x16x32_bf16 v[180:183], v[84:87], v[60:63], v[180:183]\n"
"v_add_u32 v29, s60, v29\n"
"v_mfma_f32_16x16x32_bf16 v[164:167], v[84:87], v[56:59], v[164:167]\n"
"v_mfma_f32_16x16x32_bf16 v[164:167], v[68:71], v[40:43], v[164:167]\n"
"v_mfma_f32_16x16x32_bf16 v[148:151], v[68:71], v[36:39], v[148:151]\n"
"s_add_u32 s44, s44, 128\n"
"s_add_u32 s46, s46, 32768\n"
"s_cmp_ge_u32 s46, 98304\n"
"s_cselect_b32 s50, 98304, 0\n"
"s_sub_u32 s46, s46, s50\n"
"s_add_u32 s45, s45, 131072\n"
"s_add_u32 s49, s49, 16384\n"
"s_cmp_ge_u32 s49, 163840\n"
"s_cselect_b32 s50, 65536, 0\n"
"s_sub_u32 s49, s49, s50\n"
"v_mfma_f32_16x16x32_bf16 v[148:151], v[84:87], v[52:55], v[148:151]\n"
"v_mfma_f32_16x16x32_bf16 v[132:135], v[84:87], v[48:51], v[132:135]\n"
"v_mfma_f32_16x16x32_bf16 v[132:135], v[68:71], v[32:35], v[132:135]\n"
"v_mfma_f32_16x16x32_bf16 v[136:139], v[72:75], v[32:35], v[136:139]\n"
"v_mfma_f32_16x16x32_bf16 v[136:139], v[88:91], v[48:51], v[136:139]\n"
"v_mfma_f32_16x16x32_bf16 v[152:155], v[88:91], v[52:55], v[152:155]\n"
"v_mfma_f32_16x16x32_bf16 v[152:155], v[72:75], v[36:39], v[152:155]\n"
"v_mfma_f32_16x16x32_bf16 v[168:171], v[72:75], v[40:43], v[168:171]\n"
"v_mfma_f32_16x16x32_bf16 v[168:171], v[88:91], v[56:59], v[168:171]\n"
"v_mfma_f32_16x16x32_bf16 v[184:187], v[88:91], v[60:63], v[184:187]\n"
"v_mfma_f32_16x16x32_bf16 v[184:187], v[72:75], v[44:47], v[184:187]\n"
"v_mfma_f32_16x16x32_bf16 v[188:191], v[76:79], v[44:47], v[188:191]\n"
"v_mfma_f32_16x16x32_bf16 v[188:191], v[92:95], v[60:63], v[188:191]\n"
"v_mfma_f32_16x16x32_bf16 v[172:175], v[92:95], v[56:59], v[172:175]\n"
"v_mfma_f32_16x16x32_bf16 v[172:175], v[76:79], v[40:43], v[172:175]\n"
"v_mfma_f32_16x16x32_bf16 v[156:159], v[76:79], v[36:39], v[156:159]\n"
"v_mfma_f32_16x16x32_bf16 v[156:159], v[92:95], v[52:55], v[156:159]\n"
"v_mfma_f32_16x16x32_bf16 v[140:143], v[92:95], v[48:51], v[140:143]\n"
"v_mfma_f32_16x16x32_bf16 v[140:143], v[76:79], v[32:35], v[140:143]\n"
"s_barrier\n"
"s_xor_b32 m0, s46, 0\n"
"ds_read_b64_tr_b16 v[64:65], v26 offset:0\n"
"buffer_load_dwordx4 v16, s[24:27], s44 offen sc1 lds\n"
"ds_read_b64_tr_b16 v[66:67], v26 offset:512\n"
"ds_read_b64_tr_b16 v[68:69], v27 offset:0\n"
"ds_read_b64_tr_b16 v[70:71], v27 offset:512\n"
"s_xor_b32 m0, s46, 1024\n"
"ds_read_b64_tr_b16 v[72:73], v28 offset:0\n"
"buffer_load_dwordx4 v17, s[24:27], s44 offen sc1 lds\n"
"ds_read_b64_tr_b16 v[74:75], v28 offset:512\n"
"ds_read_b64_tr_b16 v[76:77], v29 offset:0\n"
"ds_read_b64_tr_b16 v[78:79], v29 offset:512\n"
"s_xor_b32 m0, s46, 2048\n"
"ds_read_b128 v[32:35], v24 offset:0\n"
"buffer_load_dwordx4 v18, s[24:27], s44 offen sc1 lds\n"
"ds_read_b128 v[36:39], v24 offset:2048\n"
"ds_read_b128 v[40:43], v24 offset:4096\n"
"ds_read_b128 v[44:47], v24 offset:6144\n"
"s_xor_b32 m0, s46, 3072\n"
"ds_read_b64_tr_b16 v[80:81], v26 offset:4096\n"
"buffer_load_dwordx4 v19, s[24:27], s44 offen sc1 lds\n"
"ds_read_b64_tr_b16 v[82:83], v26 offset:4608\n"
"ds_read_b64_tr_b16 v[84:85], v27 offset:4096\n"
"ds_read_b64_tr_b16 v[86:87], v27 offset:4608\n"
"s_add_u32 m0, s49, 0\n"
"ds_read_b64_tr_b16 v[88:89], v28 offset:4096\n"
"buffer_load_dwordx4 v20, s[28:31], s45 offen lds\n"
"ds_read_b64_tr_b16 v[90:91], v28 offset:4608\n"
"ds_read_b64_tr_b16 v[92:93], v29 offset:4096\n"
"ds_read_b64_tr_b16 v[94:95], v29 offset:4608\n"
"s_add_u32 m0, s49, 1024\n"
"ds_read_b128 v[48:51], v25 offset:0\n"
"buffer_load_dwordx4 v21, s[28:31], s45 offen lds\n"
"ds_read_b128 v[52:55], v25 offset:2048\n"
"ds_read_b128 v[56:59], v25 offset:4096\n"
"ds_read_b128 v[60:63], v25 offset:6144\n"
"s_waitcnt vmcnt(8) lgkmcnt(0)\n"
"s_barrier\n"
"v_mfma_f32_16x16x32_bf16 v[128:131], v[64:67], v[32:35], v[128:131]\n"
"v_mfma_f32_16x16x32_bf16 v[128:131], v[80:83], v[48:51], v[128:131]\n"
"s_add_u32 s68, s68, 1\n"
"s_cmp_eq_u32 s68, 3\n"
"s_cselect_b32 s48, 4294901760, s86\n"
"s_cselect_b32 s68, 0, s68\n"
"v_mfma_f32_16x16x32_bf16 v[144:147], v[80:83], v[52:55], v[144:147]\n"
"s_add_u32 s69, s69, 1\n"
"s_cmp_eq_u32 s69, 4\n"
"s_cselect_b32 s60, 4294918144, s87\n"
"s_cselect_b32 s69, 0, s69\n"
"v_mfma_f32_16x16x32_bf16 v[144:147], v[64:67], v[36:39], v[144:147]\n"
"v_mfma_f32_16x16x32_bf16 v[160:163], v[64:67], v[40:43], v[160:163]\n"
"v_add_u32 v24, s48, v24\n"
"v_mfma_f32_16x16x32_bf16 v[160:163], v[80:83], v[56:59], v[160:163]\n"
"v_add_u32 v25, s48, v25\n"
"v_mfma_f32_16x16x32_bf16 v[176:179], v[80:83], v[60:63], v[176:179]\n"
"v_add_u32 v26, s60, v26\n"
"v_mfma_f32_16x16x32_bf16 v[176:179], v[64:67], v[44:47], v[176:179]\n"
"v_add_u32 v27, s60, v27\n"
"v_mfma_f32_16x16x32_bf16 v[180:183], v[68:71], v[44:47], v[180:183]\n"
"v_add_u32 v28, s60, v28\n"
"v_mfma_f32_16x16x32_bf16 v[180:183], v[84:87], v[60:63], v[180:183]\n"
"v_add_u32 v29, s60, v29\n"
"v_mfma_f32_16x16x32_bf16 v[164:167], v[84:87], v[56:59], v[164:167]\n"
"v_mfma_f32_16x16x32_bf16 v[164:167], v[68:71], v[40:43], v[164:167]\n"
"v_mfma_f32_16x16x32_bf16 v[148:151], v[68:71], v[36:39], v[148:151]\n"
"s_add_u32 s44, s44, 128\n"
"s_add_u32 s46, s46, 32768\n"
"s_cmp_ge_u32 s46, 98304\n"
"s_cselect_b32 s50, 98304, 0\n"
"s_sub_u32 s46, s46, s50\n"
"s_add_u32 s45, s45, 131072\n"
"s_add_u32 s49, s49, 16384\n"
"s_cmp_ge_u32 s49, 163840\n"
"s_cselect_b32 s50, 65536, 0\n"
"s_sub_u32 s49, s49, s50\n"
"v_mfma_f32_16x16x32_bf16 v[148:151], v[84:87], v[52:55], v[148:151]\n"
"v_mfma_f32_16x16x32_bf16 v[132:135], v[84:87], v[48:51], v[132:135]\n"
"v_mfma_f32_16x16x32_bf16 v[132:135], v[68:71], v[32:35], v[132:135]\n"
"v_mfma_f32_16x16x32_bf16 v[136:139], v[72:75], v[32:35], v[136:139]\n"
"v_mfma_f32_16x16x32_bf16 v[136:139], v[88:91], v[48:51], v[136:139]\n"
"v_mfma_f32_16x16x32_bf16 v[152:155], v[88:91], v[52:55], v[152:155]\n"
"v_mfma_f32_16x16x32_bf16 v[152:155], v[72:75], v[36:39], v[152:155]\n"
"v_mfma_f32_16x16x32_bf16 v[168:171], v[72:75], v[40:43], v[168:171]\n"
"v_mfma_f32_16x16x32_bf16 v[168:171], v[88:91], v[56:59], v[168:171]\n"
"v_mfma_f32_16x16x32_bf16 v[184:187], v[88:91], v[60:63], v[184:187]\n"
"v_mfma_f32_16x16x32_bf16 v[184:187], v[72:75], v[44:47], v[184:187]\n"
"v_mfma_f32_16x16x32_bf16 v[188:191], v[76:79], v[44:47], v[188:191]\n"
"v_mfma_f32_16x16x32_bf16 v[188:191], v[92:95], v[60:63], v[188:191]\n"
"v_mfma_f32_16x16x32_bf16 v[172:175], v[92:95], v[56:59], v[172:175]\n"
"v_mfma_f32_16x16x32_bf16 v[172:175], v[76:79], v[40:43], v[172:175]\n"
"v_mfma_f32_16x16x32_bf16 v[156:159], v[76:79], v[36:39], v[156:159]\n"
"v_mfma_f32_16x16x32_bf16 v[156:159], v[92:95], v[52:55], v[156:159]\n"
"v_mfma_f32_16x16x32_bf16 v[140:143], v[92:95], v[48:51], v[140:143]\n"
"v_mfma_f32_16x16x32_bf16 v[140:143], v[76:79], v[32:35], v[140:143]\n"
"s_barrier\n"
"s_cmp_eq_u32 s21, 0\n"
"s_cbranch_scc0 P2_nof\n"
"s_lshl_b32 s50, s58, 7\n"
"s_lshl_b32 s51, s59, 2\n"
"s_add_u32 s50, s50, s51\n"
"s_add_u32 s50, s50, 1024\n"
"v_mov_b32 v2, s50\n"
"v_mov_b32 v3, s61\n"
"s_mov_b64 exec, 1\n"
"global_store_dword v2, v3, s[6:7] sc1\n"
"s_mov_b64 exec, -1\n"
"P2_nof:\n"
"s_xor_b32 m0, s46, 0\n"
"ds_read_b64_tr_b16 v[64:65], v26 offset:0\n"
"buffer_load_dwordx4 v16, s[24:27], s44 offen sc1 lds\n"
"ds_read_b64_tr_b16 v[66:67], v26 offset:512\n"
"ds_read_b64_tr_b16 v[68:69], v27 offset:0\n"
"ds_read_b64_tr_b16 v[70:71], v27 offset:512\n"
"s_xor_b32 m0, s46, 1024\n"
"ds_read_b64_tr_b16 v[72:73], v28 offset:0\n"
"buffer_load_dwordx4 v17, s[24:27], s44 offen sc1 lds\n"
"ds_read_b64_tr_b16 v[74:75], v28 offset:512\n"
"ds_read_b64_tr_b16 v[76:77], v29 offset:0\n"
"ds_read_b64_tr_b16 v[78:79], v29 offset:512\n"
"s_xor_b32 m0, s46, 2048\n"
"ds_read_b128 v[32:35], v24 offset:0\n"
"buffer_load_dwordx4 v18, s[24:27], s44 offen sc1 lds\n"
"ds_read_b128 v[36:39], v24 offset:2048\n"
"ds_read_b128 v[40:43], v24 offset:4096\n"
"ds_read_b128 v[44:47], v24 offset:6144\n"
"s_xor_b32 m0, s46, 3072\n"
"ds_read_b64_tr_b16 v[80:81], v26 offset:4096\n"
"buffer_load_dwordx4 v19, s[24:27], s44 offen sc1 lds\n"
"ds_read_b64_tr_b16 v[82:83], v26 offset:4608\n"
"ds_read_b64_tr_b16 v[84:85], v27 offset:4096\n"
"ds_read_b64_tr_b16 v[86:87], v27 offset:4608\n"
"s_add_u32 m0, s49, 0\n"
"ds_read_b64_tr_b16 v[88:89], v28 offset:4096\n"
"buffer_load_dwordx4 v20, s[28:31], s45 offen lds\n"
"ds_read_b64_tr_b16 v[90:91], v28 offset:4608\n"
"ds_read_b64_tr_b16 v[92:93], v29 offset:4096\n"
"ds_read_b64_tr_b16 v[94:95], v29 offset:4608\n"
"s_add_u32 m0, s49, 1024\n"
"ds_read_b128 v[48:51], v25 offset:0\n"
"buffer_load_dwordx4 v21, s[28:31], s45 offen lds\n"
"ds_read_b128 v[52:55], v25 offset:2048\n"
"ds_read_b128 v[56:59], v25 offset:4096\n"
"ds_read_b128 v[60:63], v25 offset:6144\n"
"s_waitcnt vmcnt(8) lgkmcnt(0)\n"
"s_barrier\n"
"v_mfma_f32_16x16x32_bf16 v[128:131], v[64:67], v[32:35], v[128:131]\n"
"v_mfma_f32_16x16x32_bf16 v[128:131], v[80:83], v[48:51], v[128:131]\n"
"s_add_u32 s68, s68, 1\n"
"s_cmp_eq_u32 s68, 3\n"
"s_cselect_b32 s48, 4294901760, s86\n"
"s_cselect_b32 s68, 0, s68\n"
"v_mfma_f32_16x16x32_bf16 v[144:147], v[80:83], v[52:55], v[144:147]\n"
"s_add_u32 s69, s69, 1\n"
"s_cmp_eq_u32 s69, 4\n"
"s_cselect_b32 s60, 4294918144, s87\n"
"s_cselect_b32 s69, 0, s69\n"
"v_mfma_f32_16x16x32_bf16 v[144:147], v[64:67], v[36:39], v[144:147]\n"
"v_mfma_f32_16x16x32_bf16 v[160:163], v[64:67], v[40:43], v[160:163]\n"
"v_add_u32 v24, s48, v24\n"
"v_mfma_f32_16x16x32_bf16 v[160:163], v[80:83], v[56:59], v[160:163]\n"
"v_add_u32 v25, s48, v25\n"
"v_mfma_f32_16x16x32_bf16 v[176:179], v[80:83], v[60:63], v[176:179]\n"
"v_add_u32 v26, s60, v26\n"
"v_mfma_f32_16x16x32_bf16 v[176:179], v[64:67], v[44:47], v[176:179]\n"
"v_add_u32 v27, s60, v27\n"
"v_mfma_f32_16x16x32_bf16 v[180:183], v[68:71], v[44:47], v[180:183]\n"
"v_add_u32 v28, s60, v28\n"
"v_mfma_f32_16x16x32_bf16 v[180:183], v[84:87], v[60:63], v[180:183]\n"
"v_add_u32 v29, s60, v29\n"
"v_mfma_f32_16x16x32_bf16 v[164:167], v[84:87], v[56:59], v[164:167]\n"
"v_mfma_f32_16x16x32_bf16 v[164:167], v[68:71], v[40:43], v[164:167]\n"
"v_mfma_f32_16x16x32_bf16 v[148:151], v[68:71], v[36:39], v[148:151]\n"
"s_add_u32 s44, s44, 128\n"
"s_add_u32 s46, s46, 32768\n"
"s_cmp_ge_u32 s46, 98304\n"
"s_cselect_b32 s50, 98304, 0\n"
"s_sub_u32 s46, s46, s50\n"
"s_add_u32 s45, s45, 131072\n"
"s_add_u32 s49, s49, 16384\n"
"s_cmp_ge_u32 s49, 163840\n"
"s_cselect_b32 s50, 65536, 0\n"
"s_sub_u32 s49, s49, s50\n"
"v_mfma_f32_16x16x32_bf16 v[148:151], v[84:87], v[52:55], v[148:151]\n"
"v_mfma_f32_16x16x32_bf16 v[132:135], v[84:87], v[48:51], v[132:135]\n"
"v_mfma_f32_16x16x32_bf16 v[132:135], v[68:71], v[32:35], v[132:135]\n"
"v_mfma_f32_16x16x32_bf16 v[136:139], v[72:75], v[32:35], v[136:139]\n"
"v_mfma_f32_16x16x32_bf16 v[136:139], v[88:91], v[48:51], v[136:139]\n"
"v_mfma_f32_16x16x32_bf16 v[152:155], v[88:91], v[52:55], v[152:155]\n"
"v_mfma_f32_16x16x32_bf16 v[152:155], v[72:75], v[36:39], v[152:155]\n"
"v_mfma_f32_16x16x32_bf16 v[168:171], v[72:75], v[40:43], v[168:171]\n"
"v_mfma_f32_16x16x32_bf16 v[168:171], v[88:91], v[56:59], v[168:171]\n"
"v_mfma_f32_16x16x32_bf16 v[184:187], v[88:91], v[60:63], v[184:187]\n"
"v_mfma_f32_16x16x32_bf16 v[184:187], v[72:75], v[44:47], v[184:187]\n"
"v_mfma_f32_16x16x32_bf16 v[188:191], v[76:79], v[44:47], v[188:191]\n"
"v_mfma_f32_16x16x32_bf16 v[188:191], v[92:95], v[60:63], v[188:191]\n"
"v_mfma_f32_16x16x32_bf16 v[172:175], v[92:95], v[56:59], v[172:175]\n"
"v_mfma_f32_16x16x32_bf16 v[172:175], v[76:79], v[40:43], v[172:175]\n"
"v_mfma_f32_16x16x32_bf16 v[156:159], v[76:79], v[36:39], v[156:159]\n"
"v_mfma_f32_16x16x32_bf16 v[156:159], v[92:95], v[52:55], v[156:159]\n"
"v_mfma_f32_16x16x32_bf16 v[140:143], v[92:95], v[48:51], v[140:143]\n"
"v_mfma_f32_16x16x32_bf16 v[140:143], v[76:79], v[32:35], v[140:143]\n"
"s_barrier\n"
"s_xor_b32 m0, s46, 0\n"
"ds_read_b64_tr_b16 v[64:65], v26 offset:0\n"
"buffer_load_dwordx4 v16, s[24:27], s44 offen sc1 lds\n"
"ds_read_b64_tr_b16 v[66:67], v26 offset:512\n"
"ds_read_b64_tr_b16 v[68:69], v27 offset:0\n"
"ds_read_b64_tr_b16 v[70:71], v27 offset:512\n"
"s_xor_b32 m0, s46, 1024\n"
"ds_read_b64_tr_b16 v[72:73], v28 offset:0\n"
"buffer_load_dwordx4 v17, s[24:27], s44 offen sc1 lds\n"
"ds_read_b64_tr_b16 v[74:75], v28 offset:512\n"
"ds_read_b64_tr_b16 v[76:77], v29 offset:0\n"
"ds_read_b64_tr_b16 v[78:79], v29 offset:512\n"
"s_xor_b32 m0, s46, 2048\n"
"ds_read_b128 v[32:35], v24 offset:0\n"
"buffer_load_dwordx4 v18, s[24:27], s44 offen sc1 lds\n"
"ds_read_b128 v[36:39], v24 offset:2048\n"
"ds_read_b128 v[40:43], v24 offset:4096\n"
"ds_read_b128 v[44:47], v24 offset:6144\n"
"s_xor_b32 m0, s46, 3072\n"
"ds_read_b64_tr_b16 v[80:81], v26 offset:4096\n"
"buffer_load_dwordx4 v19, s[24:27], s44 offen sc1 lds\n"
"ds_read_b64_tr_b16 v[82:83], v26 offset:4608\n"
"ds_read_b64_tr_b16 v[84:85], v27 offset:4096\n"
"ds_read_b64_tr_b16 v[86:87], v27 offset:4608\n"
"s_add_u32 m0, s49, 0\n"
"ds_read_b64_tr_b16 v[88:89], v28 offset:4096\n"
"buffer_load_dwordx4 v20, s[28:31], s45 offen lds\n"
"ds_read_b64_tr_b16 v[90:91], v28 offset:4608\n"
"ds_read_b64_tr_b16 v[92:93], v29 offset:4096\n"
"ds_read_b64_tr_b16 v[94:95], v29 offset:4608\n"
"s_add_u32 m0, s49, 1024\n"
"ds_read_b128 v[48:51], v25 offset:0\n"
"buffer_load_dwordx4 v21, s[28:31], s45 offen lds\n"
"ds_read_b128 v[52:55], v25 offset:2048\n"
"ds_read_b128 v[56:59], v25 offset:4096\n"
"ds_read_b128 v[60:63], v25 offset:6144\n"
"s_waitcnt vmcnt(8) lgkmcnt(0)\n"
"s_barrier\n"
"v_mfma_f32_16x16x32_bf16 v[128:131], v[64:67], v[32:35], v[128:131]\n"
"v_mfma_f32_16x16x32_bf16 v[128:131], v[80:83], v[48:51], v[128:131]\n"
"s_add_u32 s68, s68, 1\n"
"s_cmp_eq_u32 s68, 3\n"
"s_cselect_b32 s48, 4294901760, s86\n"
"s_cselect_b32 s68, 0, s68\n"
"v_mfma_f32_16x16x32_bf16 v[144:147], v[80:83], v[52:55], v[144:147]\n"
"s_add_u32 s69, s69, 1\n"
"s_cmp_eq_u32 s69, 4\n"
"s_cselect_b32 s60, 4294918144, s87\n"
"s_cselect_b32 s69, 0, s69\n"
"v_mfma_f32_16x16x32_bf16 v[144:147], v[64:67], v[36:39], v[144:147]\n"
"v_mfma_f32_16x16x32_bf16 v[160:163], v[64:67], v[40:43], v[160:163]\n"
"v_add_u32 v24, s48, v24\n"
"v_mfma_f32_16x16x32_bf16 v[160:163], v[80:83], v[56:59], v[160:163]\n"
"v_add_u32 v25, s48, v25\n"
"v_mfma_f32_16x16x32_bf16 v[176:179], v[80:83], v[60:63], v[176:179]\n"
"v_add_u32 v26, s60, v26\n"
"v_mfma_f32_16x16x32_bf16 v[176:179], v[64:67], v[44:47], v[176:179]\n"
"v_add_u32 v27, s60, v27\n"
"v_mfma_f32_16x16x32_bf16 v[180:183], v[68:71], v[44:47], v[180:183]\n"
"v_add_u32 v28, s60, v28\n"
"v_mfma_f32_16x16x32_bf16 v[180:183], v[84:87], v[60:63], v[180:183]\n"
"v_add_u32 v29, s60, v29\n"
"v_mfma_f32_16x16x32_bf16 v[164:167], v[84:87], v[56:59], v[164:167]\n"
"v_mfma_f32_16x16x32_bf16 v[164:167], v[68:71], v[40:43], v[164:167]\n"
"v_mfma_f32_16x16x32_bf16 v[148:151], v[68:71], v[36:39], v[148:151]\n"
"s_add_u32 s44, s44, 128\n"
"s_add_u32 s46, s46, 32768\n"
"s_cmp_ge_u32 s46, 98304\n"
"s_cselect_b32 s50, 98304, 0\n"
"s_sub_u32 s46, s46, s50\n"
"s_add_u32 s45, s45, 131072\n"
"s_add_u32 s49, s49, 16384\n"
"s_cmp_ge_u32 s49, 163840\n"
"s_cselect_b32 s50, 65536, 0\n"
"s_sub_u32 s49, s49, s50\n"
"v_mfma_f32_16x16x32_bf16 v[148:151], v[84:87], v[52:55], v[148:151]\n"
"v_mfma_f32_16x16x32_bf16 v[132:135], v[84:87], v[48:51], v[132:135]\n"
"v_mfma_f32_16x16x32_bf16 v[132:135], v[68:71], v[32:35], v[132:135]\n"
"v_mfma_f32_16x16x32_bf16 v[136:139], v[72:75], v[32:35], v[136:139]\n"
"v_mfma_f32_16x16x32_bf16 v[136:139], v[88:91], v[48:51], v[136:139]\n"
"v_mfma_f32_16x16x32_bf16 v[152:155], v[88:91], v[52:55], v[152:155]\n"
"v_mfma_f32_16x16x32_bf16 v[152:155], v[72:75], v[36:39], v[152:155]\n"
"v_mfma_f32_16x16x32_bf16 v[168:171], v[72:75], v[40:43], v[168:171]\n"
"v_mfma_f32_16x16x32_bf16 v[168:171], v[88:91], v[56:59], v[168:171]\n"
"v_mfma_f32_16x16x32_bf16 v[184:187], v[88:91], v[60:63], v[184:187]\n"
"v_mfma_f32_16x16x32_bf16 v[184:187], v[72:75], v[44:47], v[184:187]\n"
"v_mfma_f32_16x16x32_bf16 v[188:191], v[76:79], v[44:47], v[188:191]\n"
"v_mfma_f32_16x16x32_bf16 v[188:191], v[92:95], v[60:63], v[188:191]\n"
"v_mfma_f32_16x16x32_bf16 v[172:175], v[92:95], v[56:59], v[172:175]\n"
"v_mfma_f32_16x16x32_bf16 v[172:175], v[76:79], v[40:43], v[172:175]\n"
"v_mfma_f32_16x16x32_bf16 v[156:159], v[76:79], v[36:39], v[156:159]\n"
"v_mfma_f32_16x16x32_bf16 v[156:159], v[92:95], v[52:55], v[156:159]\n"
"v_mfma_f32_16x16x32_bf16 v[140:143], v[92:95], v[48:51], v[140:143]\n"
"v_mfma_f32_16x16x32_bf16 v[140:143], v[76:79], v[32:35], v[140:143]\n"
"s_barrier\n"
"s_lshl_b32 s50, s22, 7\n"
"s_add_u32 s50, s50, 1024\n"
"v_and_b32 v14, 31, v1\n"
"v_lshlrev_b32 v14, 2, v14\n"
"v_add_u32 v14, s50, v14\n"
"global_load_dword v14, v14, s[6:7] sc1\n"
"s_xor_b32 m0, s46, 0\n"
"ds_read_b64_tr_b16 v[64:65], v26 offset:0\n"
"buffer_load_dwordx4 v16, s[24:27], s44 offen sc1 lds\n"
"ds_read_b64_tr_b16 v[66:67], v26 offset:512\n"
"ds_read_b64_tr_b16 v[68:69], v27 offset:0\n"
"ds_read_b64_tr_b16 v[70:71], v27 offset:512\n"
"s_xor_b32 m0, s46, 1024\n"
"ds_read_b64_tr_b16 v[72:73], v28 offset:0\n"
"buffer_load_dwordx4 v17, s[24:27], s44 offen sc1 lds\n"
"ds_read_b64_tr_b16 v[74:75], v28 offset:512\n"
"ds_read_b64_tr_b16 v[76:77], v29 offset:0\n"
"ds_read_b64_tr_b16 v[78:79], v29 offset:512\n"
"s_xor_b32 m0, s46, 2048\n"
"ds_read_b128 v[32:35], v24 offset:0\n"
"buffer_load_dwordx4 v18, s[24:27], s44 offen sc1 lds\n"
"ds_read_b128 v[36:39], v24 offset:2048\n"
"ds_read_b128 v[40:43], v24 offset:4096\n"
"ds_read_b128 v[44:47], v24 offset:6144\n"
"s_xor_b32 m0, s46, 3072\n"
"ds_read_b64_tr_b16 v[80:81], v26 offset:4096\n"
"buffer_load_dwordx4 v19, s[24:27], s44 offen sc1 lds\n"
"ds_read_b64_tr_b16 v[82:83], v26 offset:4608\n"
"ds_read_b64_tr_b16 v[84:85], v27 offset:4096\n"
"ds_read_b64_tr_b16 v[86:87], v27 offset:4608\n"
"s_add_u32 m0, s49, 0\n"
"ds_read_b64_tr_b16 v[88:89], v28 offset:4096\n"
"buffer_load_dwordx4 v20, s[28:31], s45 offen lds\n"
"ds_read_b64_tr_b16 v[90:91], v28 offset:4608\n"
"ds_read_b64_tr_b16 v[92:93], v29 offset:4096\n"
"ds_read_b64_tr_b16 v[94:95], v29 offset:4608\n"
"s_add_u32 m0, s49, 1024\n"
"ds_read_b128 v[48:51], v25 offset:0\n"
"buffer_load_dwordx4 v21, s[28:31], s45 offen lds\n"
"ds_read_b128 v[52:55], v25 offset:2048\n"
"ds_read_b128 v[56:59], v25 offset:4096\n"
"ds_read_b128 v[60:63], v25 offset:6144\n"
"s_waitcnt vmcnt(9) lgkmcnt(0)\n"
"s_barrier\n"
"v_mfma_f32_16x16x32_bf16 v[128:131], v[64:67], v[32:35], v[128:131]\n"
"v_mfma_f32_16x16x32_bf16 v[128:131], v[80:83], v[48:51], v[128:131]\n"
"s_add_u32 s68, s68, 1\n"
"s_cmp_eq_u32 s68, 3\n"
"s_cselect_b32 s48, 4294901760, s86\n"
"s_cselect_b32 s68, 0, s68\n"
"v_mfma_f32_16x16x32_bf16 v[144:147], v[80:83], v[52:55], v[144:147]\n"
"s_add_u32 s69, s69, 1\n"
"s_cmp_eq_u32 s69, 4\n"
"s_cselect_b32 s60, 4294918144, s87\n"
"s_cselect_b32 s69, 0, s69\n"
"v_mfma_f32_16x16x32_bf16 v[144:147], v[64:67], v[36:39], v[144:147]\n"
"v_mfma_f32_16x16x32_bf16 v[160:163], v[64:67], v[40:43], v[160:163]\n"
"v_add_u32 v24, s48, v24\n"
"v_mfma_f32_16x16x32_bf16 v[160:163], v[80:83], v[56:59], v[160:163]\n"
"v_add_u32 v25, s48, v25\n"
"v_mfma_f32_16x16x32_bf16 v[176:179], v[80:83], v[60:63], v[176:179]\n"
"v_add_u32 v26, s60, v26\n"
"v_mfma_f32_16x16x32_bf16 v[176:179], v[64:67], v[44:47], v[176:179]\n"
"v_add_u32 v27, s60, v27\n"
"v_mfma_f32_16x16x32_bf16 v[180:183], v[68:71], v[44:47], v[180:183]\n"
"v_add_u32 v28, s60, v28\n"
"v_mfma_f32_16x16x32_bf16 v[180:183], v[84:87], v[60:63], v[180:183]\n"
"v_add_u32 v29, s60, v29\n"
"v_mfma_f32_16x16x32_bf16 v[164:167], v[84:87], v[56:59], v[164:167]\n"
"v_mfma_f32_16x16x32_bf16 v[164:167], v[68:71], v[40:43], v[164:167]\n"
"v_mfma_f32_16x16x32_bf16 v[148:151], v[68:71], v[36:39], v[148:151]\n"
"s_add_u32 s44, s44, 128\n"
"s_add_u32 s46, s46, 32768\n"
"s_cmp_ge_u32 s46, 98304\n"
"s_cselect_b32 s50, 98304, 0\n"
"s_sub_u32 s46, s46, s50\n"
"s_add_u32 s45, s45, 131072\n"
"s_add_u32 s49, s49, 16384\n"
"s_cmp_ge_u32 s49, 163840\n"
"s_cselect_b32 s50, 65536, 0\n"
"s_sub_u32 s49, s49, s50\n"
"v_mfma_f32_16x16x32_bf16 v[148:151], v[84:87], v[52:55], v[148:151]\n"
"v_mfma_f32_16x16x32_bf16 v[132:135], v[84:87], v[48:51], v[132:135]\n"
"v_mfma_f32_16x16x32_bf16 v[132:135], v[68:71], v[32:35], v[132:135]\n"
"v_mfma_f32_16x16x32_bf16 v[136:139], v[72:75], v[32:35], v[136:139]\n"
"v_mfma_f32_16x16x32_bf16 v[136:139], v[88:91], v[48:51], v[136:139]\n"
"v_mfma_f32_16x16x32_bf16 v[152:155], v[88:91], v[52:55], v[152:155]\n"
"v_mfma_f32_16x16x32_bf16 v[152:155], v[72:75], v[36:39], v[152:155]\n"
"v_mfma_f32_16x16x32_bf16 v[168:171], v[72:75], v[40:43], v[168:171]\n"
"v_mfma_f32_16x16x32_bf16 v[168:171], v[88:91], v[56:59], v[168:171]\n"
"v_mfma_f32_16x16x32_bf16 v[184:187], v[88:91], v[60:63], v[184:187]\n"
"v_mfma_f32_16x16x32_bf16 v[184:187], v[72:75], v[44:47], v[184:187]\n"
"v_mfma_f32_16x16x32_bf16 v[188:191], v[76:79], v[44:47], v[188:191]\n"
"v_mfma_f32_16x16x32_bf16 v[188:191], v[92:95], v[60:63], v[188:191]\n"
"v_mfma_f32_16x16x32_bf16 v[172:175], v[92:95], v[56:59], v[172:175]\n"
"v_mfma_f32_16x16x32_bf16 v[172:175], v[76:79], v[40:43], v[172:175]\n"
"v_mfma_f32_16x16x32_bf16 v[156:159], v[76:79], v[36:39], v[156:159]\n"
"v_mfma_f32_16x16x32_bf16 v[156:159], v[92:95], v[52:55], v[156:159]\n"
"v_mfma_f32_16x16x32_bf16 v[140:143], v[92:95], v[48:51], v[140:143]\n"
"v_mfma_f32_16x16x32_bf16 v[140:143], v[76:79], v[32:35], v[140:143]\n"
"s_barrier\n"
"s_lshl_b32 s50, s23, 8\n"
"s_add_u32 s28, s92, s50\n"
"s_addc_u32 s29, s93, 0\n"
"s_and_b32 s29, s29, 0xffff\n"
"s_sub_u32 s30, 8388608, s50\n"
"s_mov_b32 s31, 0x20000\n"
"s_mul_i32 s45, s62, 2097152\n"
"s_xor_b32 m0, s46, 0\n"
"ds_read_b64_tr_b16 v[64:65], v26 offset:0\n"
"buffer_load_dwordx4 v16, s[24:27], s44 offen sc1 lds\n"
"ds_read_b64_tr_b16 v[66:67], v26 offset:512\n"
"ds_read_b64_tr_b16 v[68:69], v27 offset:0\n"
"ds_read_b64_tr_b16 v[70:71], v27 offset:512\n"
"s_xor_b32 m0, s46, 1024\n"
"ds_read_b64_tr_b16 v[72:73], v28 offset:0\n"
"buffer_load_dwordx4 v17, s[24:27], s44 offen sc1 lds\n"
"ds_read_b64_tr_b16 v[74:75], v28 offset:512\n"
"ds_read_b64_tr_b16 v[76:77], v29 offset:0\n"
"ds_read_b64_tr_b16 v[78:79], v29 offset:512\n"
"s_xor_b32 m0, s46, 2048\n"
"ds_read_b128 v[32:35], v24 offset:0\n"
"buffer_load_dwordx4 v18, s[24:27], s44 offen sc1 lds\n"
"ds_read_b128 v[36:39], v24 offset:2048\n"
"ds_read_b128 v[40:43], v24 offset:4096\n"
"ds_read_b128 v[44:47], v24 offset:6144\n"
"s_xor_b32 m0, s46, 3072\n"
"ds_read_b64_tr_b16 v[80:81], v26 offset:4096\n"
"buffer_load_dwordx4 v19, s[24:27], s44 offen sc1 lds\n"
"ds_read_b64_tr_b16 v[82:83], v26 offset:4608\n"
"ds_read_b64_tr_b16 v[84:85], v27 offset:4096\n"
"ds_read_b64_tr_b16 v[86:87], v27 offset:4608\n"
"s_add_u32 m0, s49, 0\n"
"ds_read_b64_tr_b16 v[88:89], v28 offset:4096\n"
"buffer_load_dwordx4 v20, s[28:31], s45 offen lds\n"
"ds_read_b64_tr_b16 v[90:91], v28 offset:4608\n"
"ds_read_b64_tr_b16 v[92:93], v29 offset:4096\n"
"ds_read_b64_tr_b16 v[94:95], v29 offset:4608\n"
"s_add_u32 m0, s49, 1024\n"
"ds_read_b128 v[48:51], v25 offset:0\n"
"buffer_load_dwordx4 v21, s[28:31], s45 offen lds\n"
"ds_read_b128 v[52:55], v25 offset:2048\n"
"ds_read_b128 v[56:59], v25 offset:4096\n"
"ds_read_b128 v[60:63], v25 offset:6144\n"
"s_waitcnt vmcnt(8) lgkmcnt(0)\n"
"s_barrier\n"
"v_mfma_f32_16x16x32_bf16 v[128:131], v[64:67], v[32:35], v[128:131]\n"
"v_mfma_f32_16x16x32_bf16 v[128:131], v[80:83], v[48:51], v[128:131]\n"
"s_add_u32 s68, s68, 1\n"
"s_cmp_eq_u32 s68, 3\n"
"s_cselect_b32 s48, 4294901760, s86\n"
"s_cselect_b32 s68, 0, s68\n"
"v_mfma_f32_16x16x32_bf16 v[144:147], v[80:83], v[52:55], v[144:147]\n"
"s_add_u32 s69, s69, 1\n"
"s_cmp_eq_u32 s69, 4\n"
"s_cselect_b32 s60, 4294918144, s87\n"
"s_cselect_b32 s69, 0, s69\n"
"v_mfma_f32_16x16x32_bf16 v[144:147], v[64:67], v[36:39], v[144:147]\n"
"v_mfma_f32_16x16x32_bf16 v[160:163], v[64:67], v[40:43], v[160:163]\n"
"v_add_u32 v24, s48, v24\n"
"v_mfma_f32_16x16x32_bf16 v[160:163], v[80:83], v[56:59], v[160:163]\n"
"v_add_u32 v25, s48, v25\n"
"v_mfma_f32_16x16x32_bf16 v[176:179], v[80:83], v[60:63], v[176:179]\n"
"v_add_u32 v26, s60, v26\n"
"v_mfma_f32_16x16x32_bf16 v[176:179], v[64:67], v[44:47], v[176:179]\n"
"v_add_u32 v27, s60, v27\n"
"v_mfma_f32_16x16x32_bf16 v[180:183], v[68:71], v[44:47], v[180:183]\n"
"v_add_u32 v28, s60, v28\n"
"v_mfma_f32_16x16x32_bf16 v[180:183], v[84:87], v[60:63], v[180:183]\n"
"v_add_u32 v29, s60, v29\n"
"v_mfma_f32_16x16x32_bf16 v[164:167], v[84:87], v[56:59], v[164:167]\n"
"v_mfma_f32_16x16x32_bf16 v[164:167], v[68:71], v[40:43], v[164:167]\n"
"v_mfma_f32_16x16x32_bf16 v[148:151], v[68:71], v[36:39], v[148:151]\n"
"s_add_u32 s44, s44, 128\n"
"s_add_u32 s46, s46, 32768\n"
"s_cmp_ge_u32 s46, 98304\n"
"s_cselect_b32 s50, 98304, 0\n"
"s_sub_u32 s46, s46, s50\n"
"s_add_u32 s45, s45, 131072\n"
"s_add_u32 s49, s49, 16384\n"
"s_cmp_ge_u32 s49, 163840\n"
"s_cselect_b32 s50, 65536, 0\n"
"s_sub_u32 s49, s49, s50\n"
"v_mfma_f32_16x16x32_bf16 v[148:151], v[84:87], v[52:55], v[148:151]\n"
"v_mfma_f32_16x16x32_bf16 v[132:135], v[84:87], v[48:51], v[132:135]\n"
"v_mfma_f32_16x16x32_bf16 v[132:135], v[68:71], v[32:35], v[132:135]\n"
"v_mfma_f32_16x16x32_bf16 v[136:139], v[72:75], v[32:35], v[136:139]\n"
"v_mfma_f32_16x16x32_bf16 v[136:139], v[88:91], v[48:51], v[136:139]\n"
"v_mfma_f32_16x16x32_bf16 v[152:155], v[88:91], v[52:55], v[152:155]\n"
"v_mfma_f32_16x16x32_bf16 v[152:155], v[72:75], v[36:39], v[152:155]\n"
"v_mfma_f32_16x16x32_bf16 v[168:171], v[72:75], v[40:43], v[168:171]\n"
"v_mfma_f32_16x16x32_bf16 v[168:171], v[88:91], v[56:59], v[168:171]\n"
"v_mfma_f32_16x16x32_bf16 v[184:187], v[88:91], v[60:63], v[184:187]\n"
"v_mfma_f32_16x16x32_bf16 v[184:187], v[72:75], v[44:47], v[184:187]\n"
"v_mfma_f32_16x16x32_bf16 v[188:191], v[76:79], v[44:47], v[188:191]\n"
"v_mfma_f32_16x16x32_bf16 v[188:191], v[92:95], v[60:63], v[188:191]\n"
"v_mfma_f32_16x16x32_bf16 v[172:175], v[92:95], v[56:59], v[172:175]\n"
"v_mfma_f32_16x16x32_bf16 v[172:175], v[76:79], v[40:43], v[172:175]\n"
"v_mfma_f32_16x16x32_bf16 v[156:159], v[76:79], v[36:39], v[156:159]\n"
"v_mfma_f32_16x16x32_bf16 v[156:159], v[92:95], v[52:55], v[156:159]\n"
"v_mfma_f32_16x16x32_bf16 v[140:143], v[92:95], v[48:51], v[140:143]\n"
"v_mfma_f32_16x16x32_bf16 v[140:143], v[76:79], v[32:35], v[140:143]\n"
"s_barrier\n"
"v_cmp_ne_u32 vcc, s61, v14\n"
"s_nop 1\n"
"s_cmp_eq_u64 vcc, 0\n"
"s_cbranch_scc1 P2_co_ok\n"
"s_lshl_b32 s50, s22, 7\n"
"s_add_u32 s50, s50, 1024\n"
"v_and_b32 v2, 31, v1\n"
"v_lshlrev_b32 v2, 2, v2\n"
"v_add_u32 v2, s50, v2\n"
"s_mov_b32 s52, 0\n"
"P2_co_spin:\n"
"global_load_dword v3, v2, s[6:7] sc1\n"
"s_waitcnt vmcnt(0)\n"
"v_cmp_ne_u32 vcc, s61, v3\n"
"s_nop 1\n"
"s_cmp_eq_u64 vcc, 0\n"
"s_cbranch_scc1 P2_co_ok\n"
"s_add_u32 s52, s52, 1\n"
"s_cmp_lt_u32 s52, 0x8000\n"
"s_cbranch_scc0 P2_co_ok\n"
"s_sleep 1\n"
"s_branch P2_co_spin\n"
"P2_co_ok:\n"
"s_mul_i32 s50, s22, 2097152\n"
"s_add_u32 s24, s10, s50\n"
"s_addc_u32 s25, s11, 0\n"
"s_and_b32 s25, s25, 0xffff\n"
"s_mov_b32 s26, 2097152\n"
"s_mov_b32 s27, 0x20000\n"
"s_mul_i32 s44, s62, 2048\n"
"s_xor_b32 m0, s46, 0\n"
"ds_read_b64_tr_b16 v[64:65], v26 offset:0\n"
"buffer_load_dwordx4 v8, s[24:27], s44 offen sc1 lds\n"
"ds_read_b64_tr_b16 v[66:67], v26 offset:512\n"
"ds_read_b64_tr_b16 v[68:69], v27 offset:0\n"
"ds_read_b64_tr_b16 v[70:71], v27 offset:512\n"
"s_xor_b32 m0, s46, 1024\n"
"ds_read_b64_tr_b16 v[72:73], v28 offset:0\n"
"buffer_load_dwordx4 v9, s[24:27], s44 offen sc1 lds\n"
"ds_read_b64_tr_b16 v[74:75], v28 offset:512\n"
"ds_read_b64_tr_b16 v[76:77], v29 offset:0\n"
"ds_read_b64_tr_b16 v[78:79], v29 offset:512\n"
"s_xor_b32 m0, s46, 2048\n"
"ds_read_b128 v[32:35], v24 offset:0\n"
"buffer_load_dwordx4 v10, s[24:27], s44 offen sc1 lds\n"
"ds_read_b128 v[36:39], v24 offset:2048\n"
"ds_read_b128 v[40:43], v24 offset:4096\n"
"ds_read_b128 v[44:47], v24 offset:6144\n"
"s_xor_b32 m0, s46, 3072\n"
"ds_read_b64_tr_b16 v[80:81], v26 offset:4096\n"
"buffer_load_dwordx4 v11, s[24:27], s44 offen sc1 lds\n"
"ds_read_b64_tr_b16 v[82:83], v26 offset:4608\n"
"ds_read_b64_tr_b16 v[84:85], v27 offset:4096\n"
"ds_read_b64_tr_b16 v[86:87], v27 offset:4608\n"
"s_add_u32 m0, s49, 0\n"
"ds_read_b64_tr_b16 v[88:89], v28 offset:4096\n"
"buffer_load_dwordx4 v20, s[28:31], s45 offen lds\n"
"ds_read_b64_tr_b16 v[90:91], v28 offset:4608\n"
"ds_read_b64_tr_b16 v[92:93], v29 offset:4096\n"
"ds_read_b64_tr_b16 v[94:95], v29 offset:4608\n"
"s_add_u32 m0, s49, 1024\n"
"ds_read_b128 v[48:51], v25 offset:0\n"
"buffer_load_dwordx4 v21, s[28:31], s45 offen lds\n"
"ds_read_b128 v[52:55], v25 offset:2048\n"
"ds_read_b128 v[56:59], v25 offset:4096\n"
"ds_read_b128 v[60:63], v25 offset:6144\n"
"s_waitcnt vmcnt(8) lgkmcnt(0)\n"
"s_barrier\n"
"v_mfma_f32_16x16x32_bf16 v[128:131], v[64:67], v[32:35], v[128:131]\n"
"v_mfma_f32_16x16x32_bf16 v[128:131], v[80:83], v[48:51], v[128:131]\n"
"s_add_u32 s68, s68, 1\n"
"s_cmp_eq_u32 s68, 3\n"
"s_cselect_b32 s48, 4294901760, s86\n"
"s_cselect_b32 s68, 0, s68\n"
"v_mfma_f32_16x16x32_bf16 v[144:147], v[80:83], v[52:55], v[144:147]\n"
"s_add_u32 s69, s69, 1\n"
"s_cmp_eq_u32 s69, 4\n"
"s_cselect_b32 s60, 4294918144, s87\n"
"s_cselect_b32 s69, 0, s69\n"
"v_mfma_f32_16x16x32_bf16 v[144:147], v[64:67], v[36:39], v[144:147]\n"
"v_mfma_f32_16x16x32_bf16 v[160:163], v[64:67], v[40:43], v[160:163]\n"
"v_add_u32 v24, s48, v24\n"
"v_mfma_f32_16x16x32_bf16 v[160:163], v[80:83], v[56:59], v[160:163]\n"
"v_add_u32 v25, s48, v25\n"
"v_mfma_f32_16x16x32_bf16 v[176:179], v[80:83], v[60:63], v[176:179]\n"
"v_add_u32 v26, s60, v26\n"
"v_mfma_f32_16x16x32_bf16 v[176:179], v[64:67], v[44:47], v[176:179]\n"
"v_add_u32 v27, s60, v27\n"
"v_mfma_f32_16x16x32_bf16 v[180:183], v[68:71], v[44:47], v[180:183]\n"
"v_add_u32 v28, s60, v28\n"
"v_mfma_f32_16x16x32_bf16 v[180:183], v[84:87], v[60:63], v[180:183]\n"
"v_add_u32 v29, s60, v29\n"
"v_mfma_f32_16x16x32_bf16 v[164:167], v[84:87], v[56:59], v[164:167]\n"
"v_mfma_f32_16x16x32_bf16 v[164:167], v[68:71], v[40:43], v[164:167]\n"
"v_mfma_f32_16x16x32_bf16 v[148:151], v[68:71], v[36:39], v[148:151]\n"
"s_add_u32 s44, s44, 128\n"
"s_add_u32 s46, s46, 32768\n"
"s_cmp_ge_u32 s46, 98304\n"
"s_cselect_b32 s50, 98304, 0\n"
"s_sub_u32 s46, s46, s50\n"
"s_add_u32 s45, s45, 131072\n"
"s_add_u32 s49, s49, 16384\n"
"s_cmp_ge_u32 s49, 163840\n"
"s_cselect_b32 s50, 65536, 0\n"
"s_sub_u32 s49, s49, s50\n"
"v_mfma_f32_16x16x32_bf16 v[148:151], v[84:87], v[52:55], v[148:151]\n"
"v_mfma_f32_16x16x32_bf16 v[132:135], v[84:87], v[48:51], v[132:135]\n"
"v_mfma_f32_16x16x32_bf16 v[132:135], v[68:71], v[32:35], v[132:135]\n"
"v_mfma_f32_16x16x32_bf16 v[136:139], v[72:75], v[32:35], v[136:139]\n"
"v_mfma_f32_16x16x32_bf16 v[136:139], v[88:91], v[48:51], v[136:139]\n"
"v_mfma_f32_16x16x32_bf16 v[152:155], v[88:91], v[52:55], v[152:155]\n"
"v_mfma_f32_16x16x32_bf16 v[152:155], v[72:75], v[36:39], v[152:155]\n"
"v_mfma_f32_16x16x32_bf16 v[168:171], v[72:75], v[40:43], v[168:171]\n"
"v_mfma_f32_16x16x32_bf16 v[168:171], v[88:91], v[56:59], v[168:171]\n"
"v_mfma_f32_16x16x32_bf16 v[184:187], v[88:91], v[60:63], v[184:187]\n"
"v_mfma_f32_16x16x32_bf16 v[184:187], v[72:75], v[44:47], v[184:187]\n"
"v_mfma_f32_16x16x32_bf16 v[188:191], v[76:79], v[44:47], v[188:191]\n"
"v_mfma_f32_16x16x32_bf16 v[188:191], v[92:95], v[60:63], v[188:191]\n"
"v_mfma_f32_16x16x32_bf16 v[172:175], v[92:95], v[56:59], v[172:175]\n"
"v_mfma_f32_16x16x32_bf16 v[172:175], v[76:79], v[40:43], v[172:175]\n"
"v_mfma_f32_16x16x32_bf16 v[156:159], v[76:79], v[36:39], v[156:159]\n"
"v_mfma_f32_16x16x32_bf16 v[156:159], v[92:95], v[52:55], v[156:159]\n"
"v_mfma_f32_16x16x32_bf16 v[140:143], v[92:95], v[48:51], v[140:143]\n"
"v_mfma_f32_16x16x32_bf16 v[140:143], v[76:79], v[32:35], v[140:143]\n"
"s_barrier\n"
"s_xor_b32 m0, s46, 0\n"
"ds_read_b64_tr_b16 v[64:65], v26 offset:0\n"
"buffer_load_dwordx4 v8, s[24:27], s44 offen sc1 lds\n"
"ds_read_b64_tr_b16 v[66:67], v26 offset:512\n"
"ds_read_b64_tr_b16 v[68:69], v27 offset:0\n"
"ds_read_b64_tr_b16 v[70:71], v27 offset:512\n"
"s_xor_b32 m0, s46, 1024\n"
"ds_read_b64_tr_b16 v[72:73], v28 offset:0\n"
"buffer_load_dwordx4 v9, s[24:27], s44 offen sc1 lds\n"
"ds_read_b64_tr_b16 v[74:75], v28 offset:512\n"
"ds_read_b64_tr_b16 v[76:77], v29 offset:0\n"
"ds_read_b64_tr_b16 v[78:79], v29 offset:512\n"
"s_xor_b32 m0, s46, 2048\n"
"ds_read_b128 v[32:35], v24 offset:0\n"
"buffer_load_dwordx4 v10, s[24:27], s44 offen sc1 lds\n"
"ds_read_b128 v[36:39], v24 offset:2048\n"
"ds_read_b128 v[40:43], v24 offset:4096\n"
"ds_read_b128 v[44:47], v24 offset:6144\n"
"s_xor_b32 m0, s46, 3072\n"
"ds_read_b64_tr_b16 v[80:81], v26 offset:4096\n"
"buffer_load_dwordx4 v11, s[24:27], s44 offen sc1 lds\n"
"ds_read_b64_tr_b16 v[82:83], v26 offset:4608\n"
"ds_read_b64_tr_b16 v[84:85], v27 offset:4096\n"
"ds_read_b64_tr_b16 v[86:87], v27 offset:4608\n"
"s_add_u32 m0, s49, 0\n"
"ds_read_b64_tr_b16 v[88:89], v28 offset:4096\n"
"buffer_load_dwordx4 v20, s[28:31], s45 offen lds\n"
"ds_read_b64_tr_b16 v[90:91], v28 offset:4608\n"
"ds_read_b64_tr_b16 v[92:93], v29 offset:4096\n"
"ds_read_b64_tr_b16 v[94:95], v29 offset:4608\n"
"s_add_u32 m0, s49, 1024\n"
"ds_read_b128 v[48:51], v25 offset:0\n"
"buffer_load_dwordx4 v21, s[28:31], s45 offen lds\n"
"ds_read_b128 v[52:55], v25 offset:2048\n"
"ds_read_b128 v[56:59], v25 offset:4096\n"
"ds_read_b128 v[60:63], v25 offset:6144\n"
"s_waitcnt vmcnt(8) lgkmcnt(0)\n"
"s_barrier\n"
"v_mfma_f32_16x16x32_bf16 v[128:131], v[64:67], v[32:35], v[128:131]\n"
"v_mfma_f32_16x16x32_bf16 v[128:131], v[80:83], v[48:51], v[128:131]\n"
"s_add_u32 s68, s68, 1\n"
"s_cmp_eq_u32 s68, 3\n"
"s_cselect_b32 s48, 4294901760, s86\n"
"s_cselect_b32 s68, 0, s68\n"
"v_mfma_f32_16x16x32_bf16 v[144:147], v[80:83], v[52:55], v[144:147]\n"
"s_add_u32 s69, s69, 1\n"
"s_cmp_eq_u32 s69, 4\n"
"s_cselect_b32 s60, 4294918144, s87\n"
"s_cselect_b32 s69, 0, s69\n"
"v_mfma_f32_16x16x32_bf16 v[144:147], v[64:67], v[36:39], v[144:147]\n"
"v_mfma_f32_16x16x32_bf16 v[160:163], v[64:67], v[40:43], v[160:163]\n"
"v_add_u32 v24, s48, v24\n"
"v_mfma_f32_16x16x32_bf16 v[160:163], v[80:83], v[56:59], v[160:163]\n"
"v_add_u32 v25, s48, v25\n"
"v_mfma_f32_16x16x32_bf16 v[176:179], v[80:83], v[60:63], v[176:179]\n"
"v_add_u32 v26, s60, v26\n"
"v_mfma_f32_16x16x32_bf16 v[176:179], v[64:67], v[44:47], v[176:179]\n"
"v_add_u32 v27, s60, v27\n"
"v_mfma_f32_16x16x32_bf16 v[180:183], v[68:71], v[44:47], v[180:183]\n"
"v_add_u32 v28, s60, v28\n"
"v_mfma_f32_16x16x32_bf16 v[180:183], v[84:87], v[60:63], v[180:183]\n"
"v_add_u32 v29, s60, v29\n"
"v_mfma_f32_16x16x32_bf16 v[164:167], v[84:87], v[56:59], v[164:167]\n"
"v_mfma_f32_16x16x32_bf16 v[164:167], v[68:71], v[40:43], v[164:167]\n"
"v_mfma_f32_16x16x32_bf16 v[148:151], v[68:71], v[36:39], v[148:151]\n"
"s_add_u32 s44, s44, 128\n"
"s_add_u32 s46, s46, 32768\n"
"s_cmp_ge_u32 s46, 98304\n"
"s_cselect_b32 s50, 98304, 0\n"
"s_sub_u32 s46, s46, s50\n"
"s_add_u32 s45, s45, 131072\n"
"s_add_u32 s49, s49, 16384\n"
"s_cmp_ge_u32 s49, 163840\n"
"s_cselect_b32 s50, 65536, 0\n"
"s_sub_u32 s49, s49, s50\n"
"v_mfma_f32_16x16x32_bf16 v[148:151], v[84:87], v[52:55], v[148:151]\n"
"v_mfma_f32_16x16x32_bf16 v[132:135], v[84:87], v[48:51], v[132:135]\n"
"v_mfma_f32_16x16x32_bf16 v[132:135], v[68:71], v[32:35], v[132:135]\n"
"v_mfma_f32_16x16x32_bf16 v[136:139], v[72:75], v[32:35], v[136:139]\n"
"v_mfma_f32_16x16x32_bf16 v[136:139], v[88:91], v[48:51], v[136:139]\n"
"v_mfma_f32_16x16x32_bf16 v[152:155], v[88:91], v[52:55], v[152:155]\n"
"v_mfma_f32_16x16x32_bf16 v[152:155], v[72:75], v[36:39], v[152:155]\n"
"v_mfma_f32_16x16x32_bf16 v[168:171], v[72:75], v[40:43], v[168:171]\n"
"v_mfma_f32_16x16x32_bf16 v[168:171], v[88:91], v[56:59], v[168:171]\n"
"v_mfma_f32_16x16x32_bf16 v[184:187], v[88:91], v[60:63], v[184:187]\n"
"v_mfma_f32_16x16x32_bf16 v[184:187], v[72:75], v[44:47], v[184:187]\n"
"v_mfma_f32_16x16x32_bf16 v[188:191], v[76:79], v[44:47], v[188:191]\n"
"v_mfma_f32_16x16x32_bf16 v[188:191], v[92:95], v[60:63], v[188:191]\n"
"v_mfma_f32_16x16x32_bf16 v[172:175], v[92:95], v[56:59], v[172:175]\n"
"v_mfma_f32_16x16x32_bf16 v[172:175], v[76:79], v[40:43], v[172:175]\n"
"v_mfma_f32_16x16x32_bf16 v[156:159], v[76:79], v[36:39], v[156:159]\n"
"v_mfma_f32_16x16x32_bf16 v[156:159], v[92:95], v[52:55], v[156:159]\n"
"v_mfma_f32_16x16x32_bf16 v[140:143], v[92:95], v[48:51], v[140:143]\n"
"v_mfma_f32_16x16x32_bf16 v[140:143], v[76:79], v[32:35], v[140:143]\n"
"s_barrier\n"
"s_xor_b32 m0, s46, 0\n"
"ds_read_b64_tr_b16 v[64:65], v26 offset:0\n"
"buffer_load_dwordx4 v8, s[24:27], s44 offen sc1 lds\n"
"ds_read_b64_tr_b16 v[66:67], v26 offset:512\n"
"ds_read_b64_tr_b16 v[68:69], v27 offset:0\n"
"ds_read_b64_tr_b16 v[70:71], v27 offset:512\n"
"s_xor_b32 m0, s46, 1024\n"
"ds_read_b64_tr_b16 v[72:73], v28 offset:0\n"
"buffer_load_dwordx4 v9, s[24:27], s44 offen sc1 lds\n"
"ds_read_b64_tr_b16 v[74:75], v28 offset:512\n"
"ds_read_b64_tr_b16 v[76:77], v29 offset:0\n"
"ds_read_b64_tr_b16 v[78:79], v29 offset:512\n"
"s_xor_b32 m0, s46, 2048\n"
"ds_read_b128 v[32:35], v24 offset:0\n"
"buffer_load_dwordx4 v10, s[24:27], s44 offen sc1 lds\n"
"ds_read_b128 v[36:39], v24 offset:2048\n"
"ds_read_b128 v[40:43], v24 offset:4096\n"
"ds_read_b128 v[44:47], v24 offset:6144\n"
"s_xor_b32 m0, s46, 3072\n"
"ds_read_b64_tr_b16 v[80:81], v26 offset:4096\n"
"buffer_load_dwordx4 v11, s[24:27], s44 offen sc1 lds\n"
"ds_read_b64_tr_b16 v[82:83], v26 offset:4608\n"
"ds_read_b64_tr_b16 v[84:85], v27 offset:4096\n"
"ds_read_b64_tr_b16 v[86:87], v27 offset:4608\n"
"s_add_u32 m0, s49, 0\n"
"ds_read_b64_tr_b16 v[88:89], v28 offset:4096\n"
"buffer_load_dwordx4 v20, s[28:31], s45 offen lds\n"
"ds_read_b64_tr_b16 v[90:91], v28 offset:4608\n"
"ds_read_b64_tr_b16 v[92:93], v29 offset:4096\n"
"ds_read_b64_tr_b16 v[94:95], v29 offset:4608\n"
"s_add_u32 m0, s49, 1024\n"
"ds_read_b128 v[48:51], v25 offset:0\n"
"buffer_load_dwordx4 v21, s[28:31], s45 offen lds\n"
"ds_read_b128 v[52:55], v25 offset:2048\n"
"ds_read_b128 v[56:59], v25 offset:4096\n"
"ds_read_b128 v[60:63], v25 offset:6144\n"
"s_waitcnt vmcnt(8) lgkmcnt(0)\n"
"s_barrier\n"
"v_mfma_f32_16x16x32_bf16 v[192:195], v[64:67], v[32:35], 0\n"
"v_mfma_f32_16x16x32_bf16 v[192:195], v[80:83], v[48:51], v[192:195]\n"
"s_add_u32 s68, s68, 1\n"
"s_cmp_eq_u32 s68, 3\n"
"s_cselect_b32 s48, 4294901760, s86\n"
"s_cselect_b32 s68, 0, s68\n"
"v_mfma_f32_16x16x32_bf16 v[208:211], v[80:83], v[52:55], 0\n"
"s_add_u32 s69, s69, 1\n"
"s_cmp_eq_u32 s69, 4\n"
"s_cselect_b32 s60, 4294918144, s87\n"
"s_cselect_b32 s69, 0, s69\n"
"v_mfma_f32_16x16x32_bf16 v[208:211], v[64:67], v[36:39], v[208:211]\n"
"v_mfma_f32_16x16x32_bf16 v[224:227], v[64:67], v[40:43], 0\n"
"v_add_u32 v24, s48, v24\n"
"v_mfma_f32_16x16x32_bf16 v[224:227], v[80:83], v[56:59], v[224:227]\n"
"v_add_u32 v25, s48, v25\n"
"v_mfma_f32_16x16x32_bf16 v[240:243], v[80:83], v[60:63], 0\n"
"v_add_u32 v26, s60, v26\n"
"v_cvt_pk_bf16_f32 v144, v144, v145\n"
"v_mfma_f32_16x16x32_bf16 v[240:243], v[64:67], v[44:47], v[240:243]\n"
"v_add_u32 v27, s60, v27\n"
"v_cvt_pk_bf16_f32 v145, v146, v147\n"
"v_mfma_f32_16x16x32_bf16 v[244:247], v[68:71], v[44:47], 0\n"
"v_add_u32 v28, s60, v28\n"
"v_cvt_pk_bf16_f32 v146, v148, v149\n"
"v_mfma_f32_16x16x32_bf16 v[244:247], v[84:87], v[60:63], v[244:247]\n"
"v_add_u32 v29, s60, v29\n"
"v_cvt_pk_bf16_f32 v147, v150, v151\n"
"v_mfma_f32_16x16x32_bf16 v[228:231], v[84:87], v[56:59], 0\n"
"v_cvt_pk_bf16_f32 v148, v152, v153\n"
"v_mfma_f32_16x16x32_bf16 v[228:231], v[68:71], v[40:43], v[228:231]\n"
"v_cvt_pk_bf16_f32 v149, v154, v155\n"
"v_mfma_f32_16x16x32_bf16 v[212:215], v[68:71], v[36:39], 0\n"
"s_add_u32 s44, s44, 128\n"
"s_add_u32 s46, s46, 32768\n"
"s_cmp_ge_u32 s46, 98304\n"
"s_cselect_b32 s50, 98304, 0\n"
"s_sub_u32 s46, s46, s50\n"
"s_add_u32 s45, s45, 131072\n"
"s_add_u32 s49, s49, 16384\n"
"s_cmp_ge_u32 s49, 163840\n"
"s_cselect_b32 s50, 65536, 0\n"
"s_sub_u32 s49, s49, s50\n"
"v_cvt_pk_bf16_f32 v150, v156, v157\n"
"v_mfma_f32_16x16x32_bf16 v[212:215], v[84:87], v[52:55], v[212:215]\n"
"v_cvt_pk_bf16_f32 v151, v158, v159\n"
"v_mfma_f32_16x16x32_bf16 v[196:199], v[84:87], v[48:51], 0\n"
"v_cvt_pk_bf16_f32 v160, v160, v161\n"
"v_mfma_f32_16x16x32_bf16 v[196:199], v[68:71], v[32:35], v[196:199]\n"
"v_cvt_pk_bf16_f32 v161, v162, v163\n"
"v_mfma_f32_16x16x32_bf16 v[200:203], v[72:75], v[32:35], 0\n"
"v_cvt_pk_bf16_f32 v162, v164, v165\n"
"v_mfma_f32_16x16x32_bf16 v[200:203], v[88:91], v[48:51], v[200:203]\n"
"v_cvt_pk_bf16_f32 v163, v166, v167\n"
"v_mfma_f32_16x16x32_bf16 v[216:219], v[88:91], v[52:55], 0\n"
"v_cvt_pk_bf16_f32 v164, v168, v169\n"
"v_mfma_f32_16x16x32_bf16 v[216:219], v[72:75], v[36:39], v[216:219]\n"
"v_cvt_pk_bf16_f32 v165, v170, v171\n"
"v_mfma_f32_16x16x32_bf16 v[232:235], v[72:75], v[40:43], 0\n"
"v_cvt_pk_bf16_f32 v166, v172, v173\n"
"v_mfma_f32_16x16x32_bf16 v[232:235], v[88:91], v[56:59], v[232:235]\n"
"v_cvt_pk_bf16_f32 v167, v174, v175\n"
"v_mfma_f32_16x16x32_bf16 v[248:251], v[88:91], v[60:63], 0\n"
"v_cvt_pk_bf16_f32 v176, v176, v177\n"
"v_mfma_f32_16x16x32_bf16 v[248:251], v[72:75], v[44:47], v[248:251]\n"
"v_cvt_pk_bf16_f32 v177, v178, v179\n"
"v_mfma_f32_16x16x32_bf16 v[252:255], v[76:79], v[44:47], 0\n"
"v_cvt_pk_bf16_f32 v178, v180, v181\n"
"v_mfma_f32_16x16x32_bf16 v[252:255], v[92:95], v[60:63], v[252:255]\n"
"v_cvt_pk_bf16_f32 v179, v182, v183\n"
"v_mfma_f32_16x16x32_bf16 v[236:239], v[92:95], v[56:59], 0\n"
"v_cvt_pk_bf16_f32 v180, v184, v185\n"
"v_mfma_f32_16x16x32_bf16 v[236:239], v[76:79], v[40:43], v[236:239]\n"
"v_cvt_pk_bf16_f32 v181, v186, v187\n"
"v_mfma_f32_16x16x32_bf16 v[220:223], v[76:79], v[36:39], 0\n"
"v_cvt_pk_bf16_f32 v182, v188, v189\n"
"v_mfma_f32_16x16x32_bf16 v[220:223], v[92:95], v[52:55], v[220:223]\n"
"v_cvt_pk_bf16_f32 v183, v190, v191\n"
"v_mfma_f32_16x16x32_bf16 v[204:207], v[92:95], v[48:51], 0\n"
"v_mfma_f32_16x16x32_bf16 v[204:207], v[76:79], v[32:35], v[204:207]\n"
"s_barrier\n"
"s_lshl_b32 s50, s63, 1\n"
"s_add_u32 s50, s50, 0\n"
"s_lshl_b32 s50, s50, 18\n"
"s_lshl_b32 s51, s21, 11\n"
"s_add_u32 s50, s50, s51\n"
"s_add_u32 s36, s96, s50\n"
"s_addc_u32 s37, s97, 0\n"
"s_and_b32 s37, s37, 0xffff\n"
"s_mov_b32 s38, 247808\n"
"s_mov_b32 s39, 0x20000\n"
"v_lshlrev_b32 v13, 4, v1\n"
"s_xor_b32 s50, s62, 1\n"
"s_lshl_b32 s50, s50, 2\n"
"s_add_u32 s50, s50, s62\n"
"s_lshl_b32 s88, s50, 14\n"
"s_xor_b32 s50, s62, 2\n"
"s_lshl_b32 s50, s50, 2\n"
"s_add_u32 s50, s50, s62\n"
"s_lshl_b32 s89, s50, 14\n"
"s_xor_b32 s50, s62, 3\n"
"s_lshl_b32 s50, s50, 2\n"
"s_add_u32 s50, s50, s62\n"
"s_lshl_b32 s90, s50, 14\n"
"s_xor_b32 m0, s46, 0\n"
"ds_read_b64_tr_b16 v[64:65], v26 offset:0\n"
"buffer_load_dwordx4 v8, s[24:27], s44 offen sc1 lds\n"
"ds_read_b64_tr_b16 v[66:67], v26 offset:512\n"
"ds_read_b64_tr_b16 v[68:69], v27 offset:0\n"
"ds_read_b64_tr_b16 v[70:71], v27 offset:512\n"
"s_xor_b32 m0, s46, 1024\n"
"ds_read_b64_tr_b16 v[72:73], v28 offset:0\n"
"buffer_load_dwordx4 v9, s[24:27], s44 offen sc1 lds\n"
"ds_read_b64_tr_b16 v[74:75], v28 offset:512\n"
"ds_read_b64_tr_b16 v[76:77], v29 offset:0\n"
"ds_read_b64_tr_b16 v[78:79], v29 offset:512\n"
"s_xor_b32 m0, s46, 2048\n"
"ds_read_b128 v[32:35], v24 offset:0\n"
"buffer_load_dwordx4 v10, s[24:27], s44 offen sc1 lds\n"
"ds_read_b128 v[36:39], v24 offset:2048\n"
"ds_read_b128 v[40:43], v24 offset:4096\n"
"ds_read_b128 v[44:47], v24 offset:6144\n"
"s_xor_b32 m0, s46, 3072\n"
"ds_read_b64_tr_b16 v[80:81], v26 offset:4096\n"
"buffer_load_dwordx4 v11, s[24:27], s44 offen sc1 lds\n"
"ds_read_b64_tr_b16 v[82:83], v26 offset:4608\n"
"ds_read_b64_tr_b16 v[84:85], v27 offset:4096\n"
"ds_read_b64_tr_b16 v[86:87], v27 offset:4608\n"
"s_add_u32 m0, s49, 0\n"
"ds_read_b64_tr_b16 v[88:89], v28 offset:4096\n"
"buffer_load_dwordx4 v20, s[28:31], s45 offen lds\n"
"ds_read_b64_tr_b16 v[90:91], v28 offset:4608\n"
"ds_read_b64_tr_b16 v[92:93], v29 offset:4096\n"
"ds_read_b64_tr_b16 v[94:95], v29 offset:4608\n"
"s_add_u32 m0, s49, 1024\n"
"ds_read_b128 v[48:51], v25 offset:0\n"
"buffer_load_dwordx4 v21, s[28:31], s45 offen lds\n"
"ds_read_b128 v[52:55], v25 offset:2048\n"
"ds_read_b128 v[56:59], v25 offset:4096\n"
"ds_read_b128 v[60:63], v25 offset:6144\n"
"buffer_store_dwordx4 v[144:147], v13, s[36:39], s88 offen offset:0 sc1\n"
"s_waitcnt vmcnt(9) lgkmcnt(0)\n"
"s_barrier\n"
"v_mfma_f32_16x16x32_bf16 v[192:195], v[64:67], v[32:35], v[192:195]\n"
"v_mfma_f32_16x16x32_bf16 v[192:195], v[80:83], v[48:51], v[192:195]\n"
"s_add_u32 s68, s68, 1\n"
"s_cmp_eq_u32 s68, 3\n"
"s_cselect_b32 s48, 4294901760, s86\n"
"s_cselect_b32 s68, 0, s68\n"
"v_mfma_f32_16x16x32_bf16 v[208:211], v[80:83], v[52:55], v[208:211]\n"
"s_add_u32 s69, s69, 1\n"
"s_cmp_eq_u32 s69, 4\n"
"s_cselect_b32 s60, 4294918144, s87\n"
"s_cselect_b32 s69, 0, s69\n"
"v_mfma_f32_16x16x32_bf16 v[208:211], v[64:67], v[36:39], v[208:211]\n"
"v_mfma_f32_16x16x32_bf16 v[224:227], v[64:67], v[40:43], v[224:227]\n"
"v_add_u32 v24, s48, v24\n"
"v_mfma_f32_16x16x32_bf16 v[224:227], v[80:83], v[56:59], v[224:227]\n"
"v_add_u32 v25, s48, v25\n"
"v_mfma_f32_16x16x32_bf16 v[240:243], v[80:83], v[60:63], v[240:243]\n"
"v_add_u32 v26, s60, v26\n"
"v_mfma_f32_16x16x32_bf16 v[240:243], v[64:67], v[44:47], v[240:243]\n"
"v_add_u32 v27, s60, v27\n"
"v_mfma_f32_16x16x32_bf16 v[244:247], v[68:71], v[44:47], v[244:247]\n"
"v_add_u32 v28, s60, v28\n"
"v_mfma_f32_16x16x32_bf16 v[244:247], v[84:87], v[60:63], v[244:247]\n"
"v_add_u32 v29, s60, v29\n"
"v_mfma_f32_16x16x32_bf16 v[228:231], v[84:87], v[56:59], v[228:231]\n"
"v_mfma_f32_16x16x32_bf16 v[228:231], v[68:71], v[40:43], v[228:231]\n"
"v_mfma_f32_16x16x32_bf16 v[212:215], v[68:71], v[36:39], v[212:215]\n"
"s_add_u32 s44, s44, 128\n"
"s_add_u32 s46, s46, 32768\n"
"s_cmp_ge_u32 s46, 98304\n"
"s_cselect_b32 s50, 98304, 0\n"
"s_sub_u32 s46, s46, s50\n"
"s_add_u32 s45, s45, 131072\n"
"s_add_u32 s49, s49, 16384\n"
"s_cmp_ge_u32 s49, 163840\n"
"s_cselect_b32 s50, 65536, 0\n"
"s_sub_u32 s49, s49, s50\n"
"v_mfma_f32_16x16x32_bf16 v[212:215], v[84:87], v[52:55], v[212:215]\n"
"v_mfma_f32_16x16x32_bf16 v[196:199], v[84:87], v[48:51], v[196:199]\n"
"v_mfma_f32_16x16x32_bf16 v[196:199], v[68:71], v[32:35], v[196:199]\n"
"v_mfma_f32_16x16x32_bf16 v[200:203], v[72:75], v[32:35], v[200:203]\n"
"v_mfma_f32_16x16x32_bf16 v[200:203], v[88:91], v[48:51], v[200:203]\n"
"v_mfma_f32_16x16x32_bf16 v[216:219], v[88:91], v[52:55], v[216:219]\n"
"v_mfma_f32_16x16x32_bf16 v[216:219], v[72:75], v[36:39], v[216:219]\n"
"v_mfma_f32_16x16x32_bf16 v[232:235], v[72:75], v[40:43], v[232:235]\n"
"v_mfma_f32_16x16x32_bf16 v[232:235], v[88:91], v[56:59], v[232:235]\n"
"v_mfma_f32_16x16x32_bf16 v[248:251], v[88:91], v[60:63], v[248:251]\n"
"v_mfma_f32_16x16x32_bf16 v[248:251], v[72:75], v[44:47], v[248:251]\n"
"v_mfma_f32_16x16x32_bf16 v[252:255], v[76:79], v[44:47], v[252:255]\n"
"v_mfma_f32_16x16x32_bf16 v[252:255], v[92:95], v[60:63], v[252:255]\n"
"v_mfma_f32_16x16x32_bf16 v[236:239], v[92:95], v[56:59], v[236:239]\n"
"v_mfma_f32_16x16x32_bf16 v[236:239], v[76:79], v[40:43], v[236:239]\n"
"v_mfma_f32_16x16x32_bf16 v[220:223], v[76:79], v[36:39], v[220:223]\n"
"v_mfma_f32_16x16x32_bf16 v[220:223], v[92:95], v[52:55], v[220:223]\n"
"v_mfma_f32_16x16x32_bf16 v[204:207], v[92:95], v[48:51], v[204:207]\n"
"v_mfma_f32_16x16x32_bf16 v[204:207], v[76:79], v[32:35], v[204:207]\n"
"s_barrier\n"
"s_xor_b32 m0, s46, 0\n"
"ds_read_b64_tr_b16 v[64:65], v26 offset:0\n"
"buffer_load_dwordx4 v8, s[24:27], s44 offen sc1 lds\n"
"ds_read_b64_tr_b16 v[66:67], v26 offset:512\n"
"ds_read_b64_tr_b16 v[68:69], v27 offset:0\n"
"ds_read_b64_tr_b16 v[70:71], v27 offset:512\n"
"s_xor_b32 m0, s46, 1024\n"
"ds_read_b64_tr_b16 v[72:73], v28 offset:0\n"
"buffer_load_dwordx4 v9, s[24:27], s44 offen sc1 lds\n"
"ds_read_b64_tr_b16 v[74:75], v28 offset:512\n"
"ds_read_b64_tr_b16 v[76:77], v29 offset:0\n"
"ds_read_b64_tr_b16 v[78:79], v29 offset:512\n"
"s_xor_b32 m0, s46, 2048\n"
"ds_read_b128 v[32:35], v24 offset:0\n"
"buffer_load_dwordx4 v10, s[24:27], s44 offen sc1 lds\n"
"ds_read_b128 v[36:39], v24 offset:2048\n"
"ds_read_b128 v[40:43], v24 offset:4096\n"
"ds_read_b128 v[44:47], v24 offset:6144\n"
"s_xor_b32 m0, s46, 3072\n"
"ds_read_b64_tr_b16 v[80:81], v26 offset:4096\n"
"buffer_load_dwordx4 v11, s[24:27], s44 offen sc1 lds\n"
"ds_read_b64_tr_b16 v[82:83], v26 offset:4608\n"
"ds_read_b64_tr_b16 v[84:85], v27 offset:4096\n"
"ds_read_b64_tr_b16 v[86:87], v27 offset:4608\n"
"s_add_u32 m0, s49, 0\n"
"ds_read_b64_tr_b16 v[88:89], v28 offset:4096\n"
"buffer_load_dwordx4 v20, s[28:31], s45 offen lds\n"
"ds_read_b64_tr_b16 v[90:91], v28 offset:4608\n"
"ds_read_b64_tr_b16 v[92:93], v29 offset:4096\n"
"ds_read_b64_tr_b16 v[94:95], v29 offset:4608\n"
"s_add_u32 m0, s49, 1024\n"
"ds_read_b128 v[48:51], v25 offset:0\n"
"buffer_load_dwordx4 v21, s[28:31], s45 offen lds\n"
"ds_read_b128 v[52:55], v25 offset:2048\n"
"ds_read_b128 v[56:59], v25 offset:4096\n"
"ds_read_b128 v[60:63], v25 offset:6144\n"
"buffer_store_dwordx4 v[148:151], v13, s[36:39], s88 offen offset:1024 sc1\n"
"s_waitcnt vmcnt(10) lgkmcnt(0)\n"
"s_barrier\n"
"v_mfma_f32_16x16x32_bf16 v[192:195], v[64:67], v[32:35], v[192:195]\n"
"v_mfma_f32_16x16x32_bf16 v[192:195], v[80:83], v[48:51], v[192:195]\n"
"s_add_u32 s68, s68, 1\n"
"s_cmp_eq_u32 s68, 3\n"
"s_cselect_b32 s48, 4294901760, s86\n"
"s_cselect_b32 s68, 0, s68\n"
"v_mfma_f32_16x16x32_bf16 v[208:211], v[80:83], v[52:55], v[208:211]\n"
"s_add_u32 s69, s69, 1\n"
"s_cmp_eq_u32 s69, 4\n"
"s_cselect_b32 s60, 4294918144, s87\n"
"s_cselect_b32 s69, 0, s69\n"
"v_mfma_f32_16x16x32_bf16 v[208:211], v[64:67], v[36:39], v[208:211]\n"
"v_mfma_f32_16x16x32_bf16 v[224:227], v[64:67], v[40:43], v[224:227]\n"
"v_add_u32 v24, s48, v24\n"
"v_mfma_f32_16x16x32_bf16 v[224:227], v[80:83], v[56:59], v[224:227]\n"
"v_add_u32 v25, s48, v25\n"
"v_mfma_f32_16x16x32_bf16 v[240:243], v[80:83], v[60:63], v[240:243]\n"
"v_add_u32 v26, s60, v26\n"
"v_mfma_f32_16x16x32_bf16 v[240:243], v[64:67], v[44:47], v[240:243]\n"
"v_add_u32 v27, s60, v27\n"
"v_mfma_f32_16x16x32_bf16 v[244:247], v[68:71], v[44:47], v[244:247]\n"
"v_add_u32 v28, s60, v28\n"
"v_mfma_f32_16x16x32_bf16 v[244:247], v[84:87], v[60:63], v[244:247]\n"
"v_add_u32 v29, s60, v29\n"
"v_mfma_f32_16x16x32_bf16 v[228:231], v[84:87], v[56:59], v[228:231]\n"
"v_mfma_f32_16x16x32_bf16 v[228:231], v[68:71], v[40:43], v[228:231]\n"
"v_mfma_f32_16x16x32_bf16 v[212:215], v[68:71], v[36:39], v[212:215]\n"
"s_add_u32 s44, s44, 128\n"
"s_add_u32 s46, s46, 32768\n"
"s_cmp_ge_u32 s46, 98304\n"
"s_cselect_b32 s50, 98304, 0\n"
"s_sub_u32 s46, s46, s50\n"
"s_add_u32 s45, s45, 131072\n"
"s_add_u32 s49, s49, 16384\n"
"s_cmp_ge_u32 s49, 163840\n"
"s_cselect_b32 s50, 65536, 0\n"
"s_sub_u32 s49, s49, s50\n"
"v_mfma_f32_16x16x32_bf16 v[212:215], v[84:87], v[52:55], v[212:215]\n"
"v_mfma_f32_16x16x32_bf16 v[196:199], v[84:87], v[48:51], v[196:199]\n"
"v_mfma_f32_16x16x32_bf16 v[196:199], v[68:71], v[32:35], v[196:199]\n"
"v_mfma_f32_16x16x32_bf16 v[200:203], v[72:75], v[32:35], v[200:203]\n"
"v_mfma_f32_16x16x32_bf16 v[200:203], v[88:91], v[48:51], v[200:203]\n"
"v_mfma_f32_16x16x32_bf16 v[216:219], v[88:91], v[52:55], v[216:219]\n"
"v_mfma_f32_16x16x32_bf16 v[216:219], v[72:75], v[36:39], v[216:219]\n"
"v_mfma_f32_16x16x32_bf16 v[232:235], v[72:75], v[40:43], v[232:235]\n"
"v_mfma_f32_16x16x32_bf16 v[232:235], v[88:91], v[56:59], v[232:235]\n"
"v_mfma_f32_16x16x32_bf16 v[248:251], v[88:91], v[60:63], v[248:251]\n"
"v_mfma_f32_16x16x32_bf16 v[248:251], v[72:75], v[44:47], v[248:251]\n"
"v_mfma_f32_16x16x32_bf16 v[252:255], v[76:79], v[44:47], v[252:255]\n"
"v_mfma_f32_16x16x32_bf16 v[252:255], v[92:95], v[60:63], v[252:255]\n"
"v_mfma_f32_16x16x32_bf16 v[236:239], v[92:95], v[56:59], v[236:239]\n"
"v_mfma_f32_16x16x32_bf16 v[236:239], v[76:79], v[40:43], v[236:239]\n"
"v_mfma_f32_16x16x32_bf16 v[220:223], v[76:79], v[36:39], v[220:223]\n"
"v_mfma_f32_16x16x32_bf16 v[220:223], v[92:95], v[52:55], v[220:223]\n"
"v_mfma_f32_16x16x32_bf16 v[204:207], v[92:95], v[48:51], v[204:207]\n"
"v_mfma_f32_16x16x32_bf16 v[204:207], v[76:79], v[32:35], v[204:207]\n"
"s_barrier\n"
"s_xor_b32 m0, s46, 0\n"
"ds_read_b64_tr_b16 v[64:65], v26 offset:0\n"
"buffer_load_dwordx4 v8, s[24:27], s44 offen sc1 lds\n"
"ds_read_b64_tr_b16 v[66:67], v26 offset:512\n"
"ds_read_b64_tr_b16 v[68:69], v27 offset:0\n"
"ds_read_b64_tr_b16 v[70:71], v27 offset:512\n"
"s_xor_b32 m0, s46, 1024\n"
"ds_read_b64_tr_b16 v[72:73], v28 offset:0\n"
"buffer_load_dwordx4 v9, s[24:27], s44 offen sc1 lds\n"
"ds_read_b64_tr_b16 v[74:75], v28 offset:512\n"
"ds_read_b64_tr_b16 v[76:77], v29 offset:0\n"
"ds_read_b64_tr_b16 v[78:79], v29 offset:512\n"
"s_xor_b32 m0, s46, 2048\n"
"ds_read_b128 v[32:35], v24 offset:0\n"
"buffer_load_dwordx4 v10, s[24:27], s44 offen sc1 lds\n"
"ds_read_b128 v[36:39], v24 offset:2048\n"
"ds_read_b128 v[40:43], v24 offset:4096\n"
"ds_read_b128 v[44:47], v24 offset:6144\n"
"s_xor_b32 m0, s46, 3072\n"
"ds_read_b64_tr_b16 v[80:81], v26 offset:4096\n"
"buffer_load_dwordx4 v11, s[24:27], s44 offen sc1 lds\n"
"ds_read_b64_tr_b16 v[82:83], v26 offset:4608\n"
"ds_read_b64_tr_b16 v[84:85], v27 offset:4096\n"
"ds_read_b64_tr_b16 v[86:87], v27 offset:4608\n"
"s_add_u32 m0, s49, 0\n"
"ds_read_b64_tr_b16 v[88:89], v28 offset:4096\n"
"buffer_load_dwordx4 v20, s[28:31], s45 offen lds\n"
"ds_read_b64_tr_b16 v[90:91], v28 offset:4608\n"
"ds_read_b64_tr_b16 v[92:93], v29 offset:4096\n"
"ds_read_b64_tr_b16 v[94:95], v29 offset:4608\n"
"s_add_u32 m0, s49, 1024\n"
"ds_read_b128 v[48:51], v25 offset:0\n"
"buffer_load_dwordx4 v21, s[28:31], s45 offen lds\n"
"ds_read_b128 v[52:55], v25 offset:2048\n"
"ds_read_b128 v[56:59], v25 offset:4096\n"
"ds_read_b128 v[60:63], v25 offset:6144\n"
"buffer_store_dwordx4 v[160:163], v13, s[36:39], s89 offen offset:0 sc1\n"
"s_waitcnt vmcnt(10) lgkmcnt(0)\n"
"s_barrier\n"
"v_mfma_f32_16x16x32_bf16 v[192:195], v[64:67], v[32:35], v[192:195]\n"
"v_mfma_f32_16x16x32_bf16 v[192:195], v[80:83], v[48:51], v[192:195]\n"
"s_add_u32 s68, s68, 1\n"
"s_cmp_eq_u32 s68, 3\n"
"s_cselect_b32 s48, 4294901760, s86\n"
"s_cselect_b32 s68, 0, s68\n"
"v_mfma_f32_16x16x32_bf16 v[208:211], v[80:83], v[52:55], v[208:211]\n"
"s_add_u32 s69, s69, 1\n"
"s_cmp_eq_u32 s69, 4\n"
"s_cselect_b32 s60, 4294918144, s87\n"
"s_cselect_b32 s69, 0, s69\n"
"v_mfma_f32_16x16x32_bf16 v[208:211], v[64:67], v[36:39], v[208:211]\n"
"v_mfma_f32_16x16x32_bf16 v[224:227], v[64:67], v[40:43], v[224:227]\n"
"v_add_u32 v24, s48, v24\n"
"v_mfma_f32_16x16x32_bf16 v[224:227], v[80:83], v[56:59], v[224:227]\n"
"v_add_u32 v25, s48, v25\n"
"v_mfma_f32_16x16x32_bf16 v[240:243], v[80:83], v[60:63], v[240:243]\n"
"v_add_u32 v26, s60, v26\n"
"v_mfma_f32_16x16x32_bf16 v[240:243], v[64:67], v[44:47], v[240:243]\n"
"v_add_u32 v27, s60, v27\n"
"v_mfma_f32_16x16x32_bf16 v[244:247], v[68:71], v[44:47], v[244:247]\n"
"v_add_u32 v28, s60, v28\n"
"v_mfma_f32_16x16x32_bf16 v[244:247], v[84:87], v[60:63], v[244:247]\n"
"v_add_u32 v29, s60, v29\n"
"v_mfma_f32_16x16x32_bf16 v[228:231], v[84:87], v[56:59], v[228:231]\n"
"v_mfma_f32_16x16x32_bf16 v[228:231], v[68:71], v[40:43], v[228:231]\n"
"v_mfma_f32_16x16x32_bf16 v[212:215], v[68:71], v[36:39], v[212:215]\n"
"s_add_u32 s44, s44, 128\n"
"s_add_u32 s46, s46, 32768\n"
"s_cmp_ge_u32 s46, 98304\n"
"s_cselect_b32 s50, 98304, 0\n"
"s_sub_u32 s46, s46, s50\n"
"s_add_u32 s45, s45, 131072\n"
"s_add_u32 s49, s49, 16384\n"
"s_cmp_ge_u32 s49, 163840\n"
"s_cselect_b32 s50, 65536, 0\n"
"s_sub_u32 s49, s49, s50\n"
"v_mfma_f32_16x16x32_bf16 v[212:215], v[84:87], v[52:55], v[212:215]\n"
"v_mfma_f32_16x16x32_bf16 v[196:199], v[84:87], v[48:51], v[196:199]\n"
"v_mfma_f32_16x16x32_bf16 v[196:199], v[68:71], v[32:35], v[196:199]\n"
"v_mfma_f32_16x16x32_bf16 v[200:203], v[72:75], v[32:35], v[200:203]\n"
"v_mfma_f32_16x16x32_bf16 v[200:203], v[88:91], v[48:51], v[200:203]\n"
"v_mfma_f32_16x16x32_bf16 v[216:219], v[88:91], v[52:55], v[216:219]\n"
"v_mfma_f32_16x16x32_bf16 v[216:219], v[72:75], v[36:39], v[216:219]\n"
"v_mfma_f32_16x16x32_bf16 v[232:235], v[72:75], v[40:43], v[232:235]\n"
"v_mfma_f32_16x16x32_bf16 v[232:235], v[88:91], v[56:59], v[232:235]\n"
"v_mfma_f32_16x16x32_bf16 v[248:251], v[88:91], v[60:63], v[248:251]\n"
"v_mfma_f32_16x16x32_bf16 v[248:251], v[72:75], v[44:47], v[248:251]\n"
"v_mfma_f32_16x16x32_bf16 v[252:255], v[76:79], v[44:47], v[252:255]\n"
"v_mfma_f32_16x16x32_bf16 v[252:255], v[92:95], v[60:63], v[252:255]\n"
"v_mfma_f32_16x16x32_bf16 v[236:239], v[92:95], v[56:59], v[236:239]\n"
"v_mfma_f32_16x16x32_bf16 v[236:239], v[76:79], v[40:43], v[236:239]\n"
"v_mfma_f32_16x16x32_bf16 v[220:223], v[76:79], v[36:39], v[220:223]\n"
"v_mfma_f32_16x16x32_bf16 v[220:223], v[92:95], v[52:55], v[220:223]\n"
"v_mfma_f32_16x16x32_bf16 v[204:207], v[92:95], v[48:51], v[204:207]\n"
"v_mfma_f32_16x16x32_bf16 v[204:207], v[76:79], v[32:35], v[204:207]\n"
"s_barrier\n"
"s_xor_b32 m0, s46, 0\n"
"ds_read_b64_tr_b16 v[64:65], v26 offset:0\n"
"buffer_load_dwordx4 v8, s[24:27], s44 offen sc1 lds\n"
"ds_read_b64_tr_b16 v[66:67], v26 offset:512\n"
"ds_read_b64_tr_b16 v[68:69], v27 offset:0\n"
"ds_read_b64_tr_b16 v[70:71], v27 offset:512\n"
"s_xor_b32 m0, s46, 1024\n"
"ds_read_b64_tr_b16 v[72:73], v28 offset:0\n"
"buffer_load_dwordx4 v9, s[24:27], s44 offen sc1 lds\n"
"ds_read_b64_tr_b16 v[74:75], v28 offset:512\n"
"ds_read_b64_tr_b16 v[76:77], v29 offset:0\n"
"ds_read_b64_tr_b16 v[78:79], v29 offset:512\n"
"s_xor_b32 m0, s46, 2048\n"
"ds_read_b128 v[32:35], v24 offset:0\n"
"buffer_load_dwordx4 v10, s[24:27], s44 offen sc1 lds\n"
"ds_read_b128 v[36:39], v24 offset:2048\n"
"ds_read_b128 v[40:43], v24 offset:4096\n"
"ds_read_b128 v[44:47], v24 offset:6144\n"
"s_xor_b32 m0, s46, 3072\n"
"ds_read_b64_tr_b16 v[80:81], v26 offset:4096\n"
"buffer_load_dwordx4 v11, s[24:27], s44 offen sc1 lds\n"
"ds_read_b64_tr_b16 v[82:83], v26 offset:4608\n"
"ds_read_b64_tr_b16 v[84:85], v27 offset:4096\n"
"ds_read_b64_tr_b16 v[86:87], v27 offset:4608\n"
"s_add_u32 m0, s49, 0\n"
"ds_read_b64_tr_b16 v[88:89], v28 offset:4096\n"
"buffer_load_dwordx4 v20, s[28:31], s45 offen lds\n"
"ds_read_b64_tr_b16 v[90:91], v28 offset:4608\n"
"ds_read_b64_tr_b16 v[92:93], v29 offset:4096\n"
"ds_read_b64_tr_b16 v[94:95], v29 offset:4608\n"
"s_add_u32 m0, s49, 1024\n"
"ds_read_b128 v[48:51], v25 offset:0\n"
"buffer_load_dwordx4 v21, s[28:31], s45 offen lds\n"
"ds_read_b128 v[52:55], v25 offset:2048\n"
"ds_read_b128 v[56:59], v25 offset:4096\n"
"ds_read_b128 v[60:63], v25 offset:6144\n"
"buffer_store_dwordx4 v[164:167], v13, s[36:39], s89 offen offset:1024 sc1\n"
"s_waitcnt vmcnt(10) lgkmcnt(0)\n"
"s_barrier\n"
"v_mfma_f32_16x16x32_bf16 v[192:195], v[64:67], v[32:35], v[192:195]\n"
"v_mfma_f32_16x16x32_bf16 v[192:195], v[80:83], v[48:51], v[192:195]\n"
"s_add_u32 s68, s68, 1\n"
"s_cmp_eq_u32 s68, 3\n"
"s_cselect_b32 s48, 4294901760, s86\n"
"s_cselect_b32 s68, 0, s68\n"
"v_mfma_f32_16x16x32_bf16 v[208:211], v[80:83], v[52:55], v[208:211]\n"
"s_add_u32 s69, s69, 1\n"
"s_cmp_eq_u32 s69, 4\n"
"s_cselect_b32 s60, 4294918144, s87\n"
"s_cselect_b32 s69, 0, s69\n"
"v_mfma_f32_16x16x32_bf16 v[208:211], v[64:67], v[36:39], v[208:211]\n"
"v_mfma_f32_16x16x32_bf16 v[224:227], v[64:67], v[40:43], v[224:227]\n"
"v_add_u32 v24, s48, v24\n"
"v_mfma_f32_16x16x32_bf16 v[224:227], v[80:83], v[56:59], v[224:227]\n"
"v_add_u32 v25, s48, v25\n"
"v_mfma_f32_16x16x32_bf16 v[240:243], v[80:83], v[60:63], v[240:243]\n"
"v_add_u32 v26, s60, v26\n"
"v_mfma_f32_16x16x32_bf16 v[240:243], v[64:67], v[44:47], v[240:243]\n"
"v_add_u32 v27, s60, v27\n"
"v_mfma_f32_16x16x32_bf16 v[244:247], v[68:71], v[44:47], v[244:247]\n"
"v_add_u32 v28, s60, v28\n"
"v_mfma_f32_16x16x32_bf16 v[244:247], v[84:87], v[60:63], v[244:247]\n"
"v_add_u32 v29, s60, v29\n"
"v_mfma_f32_16x16x32_bf16 v[228:231], v[84:87], v[56:59], v[228:231]\n"
"v_mfma_f32_16x16x32_bf16 v[228:231], v[68:71], v[40:43], v[228:231]\n"
"v_mfma_f32_16x16x32_bf16 v[212:215], v[68:71], v[36:39], v[212:215]\n"
"s_add_u32 s44, s44, 128\n"
"s_add_u32 s46, s46, 32768\n"
"s_cmp_ge_u32 s46, 98304\n"
"s_cselect_b32 s50, 98304, 0\n"
"s_sub_u32 s46, s46, s50\n"
"s_add_u32 s45, s45, 131072\n"
"s_add_u32 s49, s49, 16384\n"
"s_cmp_ge_u32 s49, 163840\n"
"s_cselect_b32 s50, 65536, 0\n"
"s_sub_u32 s49, s49, s50\n"
"v_mfma_f32_16x16x32_bf16 v[212:215], v[84:87], v[52:55], v[212:215]\n"
"v_mfma_f32_16x16x32_bf16 v[196:199], v[84:87], v[48:51], v[196:199]\n"
"v_mfma_f32_16x16x32_bf16 v[196:199], v[68:71], v[32:35], v[196:199]\n"
"v_mfma_f32_16x16x32_bf16 v[200:203], v[72:75], v[32:35], v[200:203]\n"
"v_mfma_f32_16x16x32_bf16 v[200:203], v[88:91], v[48:51], v[200:203]\n"
"v_mfma_f32_16x16x32_bf16 v[216:219], v[88:91], v[52:55], v[216:219]\n"
"v_mfma_f32_16x16x32_bf16 v[216:219], v[72:75], v[36:39], v[216:219]\n"
"v_mfma_f32_16x16x32_bf16 v[232:235], v[72:75], v[40:43], v[232:235]\n"
"v_mfma_f32_16x16x32_bf16 v[232:235], v[88:91], v[56:59], v[232:235]\n"
"v_mfma_f32_16x16x32_bf16 v[248:251], v[88:91], v[60:63], v[248:251]\n"
"v_mfma_f32_16x16x32_bf16 v[248:251], v[72:75], v[44:47], v[248:251]\n"
"v_mfma_f32_16x16x32_bf16 v[252:255], v[76:79], v[44:47], v[252:255]\n"
"v_mfma_f32_16x16x32_bf16 v[252:255], v[92:95], v[60:63], v[252:255]\n"
"v_mfma_f32_16x16x32_bf16 v[236:239], v[92:95], v[56:59], v[236:239]\n"
"v_mfma_f32_16x16x32_bf16 v[236:239], v[76:79], v[40:43], v[236:239]\n"
"v_mfma_f32_16x16x32_bf16 v[220:223], v[76:79], v[36:39], v[220:223]\n"
"v_mfma_f32_16x16x32_bf16 v[220:223], v[92:95], v[52:55], v[220:223]\n"
"v_mfma_f32_16x16x32_bf16 v[204:207], v[92:95], v[48:51], v[204:207]\n"
"v_mfma_f32_16x16x32_bf16 v[204:207], v[76:79], v[32:35], v[204:207]\n"
"s_barrier\n"
"s_xor_b32 m0, s46, 0\n"
"ds_read_b64_tr_b16 v[64:65], v26 offset:0\n"
"buffer_load_dwordx4 v8, s[24:27], s44 offen sc1 lds\n"
"ds_read_b64_tr_b16 v[66:67], v26 offset:512\n"
"ds_read_b64_tr_b16 v[68:69], v27 offset:0\n"
"ds_read_b64_tr_b16 v[70:71], v27 offset:512\n"
"s_xor_b32 m0, s46, 1024\n"
"ds_read_b64_tr_b16 v[72:73], v28 offset:0\n"
"buffer_load_dwordx4 v9, s[24:27], s44 offen sc1 lds\n"
"ds_read_b64_tr_b16 v[74:75], v28 offset:512\n"
"ds_read_b64_tr_b16 v[76:77], v29 offset:0\n"
"ds_read_b64_tr_b16 v[78:79], v29 offset:512\n"
"s_xor_b32 m0, s46, 2048\n"
"ds_read_b128 v[32:35], v24 offset:0\n"
"buffer_load_dwordx4 v10, s[24:27], s44 offen sc1 lds\n"
"ds_read_b128 v[36:39], v24 offset:2048\n"
"ds_read_b128 v[40:43], v24 offset:4096\n"
"ds_read_b128 v[44:47], v24 offset:6144\n"
"s_xor_b32 m0, s46, 3072\n"
"ds_read_b64_tr_b16 v[80:81], v26 offset:4096\n"
"buffer_load_dwordx4 v11, s[24:27], s44 offen sc1 lds\n"
"ds_read_b64_tr_b16 v[82:83], v26 offset:4608\n"
"ds_read_b64_tr_b16 v[84:85], v27 offset:4096\n"
"ds_read_b64_tr_b16 v[86:87], v27 offset:4608\n"
"s_add_u32 m0, s49, 0\n"
"ds_read_b64_tr_b16 v[88:89], v28 offset:4096\n"
"buffer_load_dwordx4 v20, s[28:31], s45 offen lds\n"
"ds_read_b64_tr_b16 v[90:91], v28 offset:4608\n"
"ds_read_b64_tr_b16 v[92:93], v29 offset:4096\n"
"ds_read_b64_tr_b16 v[94:95], v29 offset:4608\n"
"s_add_u32 m0, s49, 1024\n"
"ds_read_b128 v[48:51], v25 offset:0\n"
"buffer_load_dwordx4 v21, s[28:31], s45 offen lds\n"
"ds_read_b128 v[52:55], v25 offset:2048\n"
"ds_read_b128 v[56:59], v25 offset:4096\n"
"ds_read_b128 v[60:63], v25 offset:6144\n"
"buffer_store_dwordx4 v[176:179], v13, s[36:39], s90 offen offset:0 sc1\n"
"s_waitcnt vmcnt(10) lgkmcnt(0)\n"
"s_barrier\n"
"v_mfma_f32_16x16x32_bf16 v[192:195], v[64:67], v[32:35], v[192:195]\n"
"v_mfma_f32_16x16x32_bf16 v[192:195], v[80:83], v[48:51], v[192:195]\n"
"s_add_u32 s68, s68, 1\n"
"s_cmp_eq_u32 s68, 3\n"
"s_cselect_b32 s48, 4294901760, s86\n"
"s_cselect_b32 s68, 0, s68\n"
"v_mfma_f32_16x16x32_bf16 v[208:211], v[80:83], v[52:55], v[208:211]\n"
"s_add_u32 s69, s69, 1\n"
"s_cmp_eq_u32 s69, 4\n"
"s_cselect_b32 s60, 4294918144, s87\n"
"s_cselect_b32 s69, 0, s69\n"
"v_mfma_f32_16x16x32_bf16 v[208:211], v[64:67], v[36:39], v[208:211]\n"
"v_mfma_f32_16x16x32_bf16 v[224:227], v[64:67], v[40:43], v[224:227]\n"
"v_add_u32 v24, s48, v24\n"
"v_mfma_f32_16x16x32_bf16 v[224:227], v[80:83], v[56:59], v[224:227]\n"
"v_add_u32 v25, s48, v25\n"
"v_mfma_f32_16x16x32_bf16 v[240:243], v[80:83], v[60:63], v[240:243]\n"
"v_add_u32 v26, s60, v26\n"
"v_mfma_f32_16x16x32_bf16 v[240:243], v[64:67], v[44:47], v[240:243]\n"
"v_add_u32 v27, s60, v27\n"
"v_mfma_f32_16x16x32_bf16 v[244:247], v[68:71], v[44:47], v[244:247]\n"
"v_add_u32 v28, s60, v28\n"
"v_mfma_f32_16x16x32_bf16 v[244:247], v[84:87], v[60:63], v[244:247]\n"
"v_add_u32 v29, s60, v29\n"
"v_mfma_f32_16x16x32_bf16 v[228:231], v[84:87], v[56:59], v[228:231]\n"
"v_mfma_f32_16x16x32_bf16 v[228:231], v[68:71], v[40:43], v[228:231]\n"
"v_mfma_f32_16x16x32_bf16 v[212:215], v[68:71], v[36:39], v[212:215]\n"
"s_add_u32 s44, s44, 128\n"
"s_add_u32 s46, s46, 32768\n"
"s_cmp_ge_u32 s46, 98304\n"
"s_cselect_b32 s50, 98304, 0\n"
"s_sub_u32 s46, s46, s50\n"
"s_add_u32 s45, s45, 131072\n"
"s_add_u32 s49, s49, 16384\n"
"s_cmp_ge_u32 s49, 163840\n"
"s_cselect_b32 s50, 65536, 0\n"
"s_sub_u32 s49, s49, s50\n"
"v_mfma_f32_16x16x32_bf16 v[212:215], v[84:87], v[52:55], v[212:215]\n"
"v_mfma_f32_16x16x32_bf16 v[196:199], v[84:87], v[48:51], v[196:199]\n"
"v_mfma_f32_16x16x32_bf16 v[196:199], v[68:71], v[32:35], v[196:199]\n"
"v_mfma_f32_16x16x32_bf16 v[200:203], v[72:75], v[32:35], v[200:203]\n"
"v_mfma_f32_16x16x32_bf16 v[200:203], v[88:91], v[48:51], v[200:203]\n"
"v_mfma_f32_16x16x32_bf16 v[216:219], v[88:91], v[52:55], v[216:219]\n"
"v_mfma_f32_16x16x32_bf16 v[216:219], v[72:75], v[36:39], v[216:219]\n"
"v_mfma_f32_16x16x32_bf16 v[232:235], v[72:75], v[40:43], v[232:235]\n"
"v_mfma_f32_16x16x32_bf16 v[232:235], v[88:91], v[56:59], v[232:235]\n"
"v_mfma_f32_16x16x32_bf16 v[248:251], v[88:91], v[60:63], v[248:251]\n"
"v_mfma_f32_16x16x32_bf16 v[248:251], v[72:75], v[44:47], v[248:251]\n"
"v_mfma_f32_16x16x32_bf16 v[252:255], v[76:79], v[44:47], v[252:255]\n"
"v_mfma_f32_16x16x32_bf16 v[252:255], v[92:95], v[60:63], v[252:255]\n"
"v_mfma_f32_16x16x32_bf16 v[236:239], v[92:95], v[56:59], v[236:239]\n"
"v_mfma_f32_16x16x32_bf16 v[236:239], v[76:79], v[40:43], v[236:239]\n"
"v_mfma_f32_16x16x32_bf16 v[220:223], v[76:79], v[36:39], v[220:223]\n"
"v_mfma_f32_16x16x32_bf16 v[220:223], v[92:95], v[52:55], v[220:223]\n"
"v_mfma_f32_16x16x32_bf16 v[204:207], v[92:95], v[48:51], v[204:207]\n"
"v_mfma_f32_16x16x32_bf16 v[204:207], v[76:79], v[32:35], v[204:207]\n"
"s_barrier\n"
"s_xor_b32 m0, s46, 0\n"
"ds_read_b64_tr_b16 v[64:65], v26 offset:0\n"
"buffer_load_dwordx4 v8, s[24:27], s44 offen sc1 lds\n"
"ds_read_b64_tr_b16 v[66:67], v26 offset:512\n"
"ds_read_b64_tr_b16 v[68:69], v27 offset:0\n"
"ds_read_b64_tr_b16 v[70:71], v27 offset:512\n"
"s_xor_b32 m0, s46, 1024\n"
"ds_read_b64_tr_b16 v[72:73], v28 offset:0\n"
"buffer_load_dwordx4 v9, s[24:27], s44 offen sc1 lds\n"
"ds_read_b64_tr_b16 v[74:75], v28 offset:512\n"
"ds_read_b64_tr_b16 v[76:77], v29 offset:0\n"
"ds_read_b64_tr_b16 v[78:79], v29 offset:512\n"
"s_xor_b32 m0, s46, 2048\n"
"ds_read_b128 v[32:35], v24 offset:0\n"
"buffer_load_dwordx4 v10, s[24:27], s44 offen sc1 lds\n"
"ds_read_b128 v[36:39], v24 offset:2048\n"
"ds_read_b128 v[40:43], v24 offset:4096\n"
"ds_read_b128 v[44:47], v24 offset:6144\n"
"s_xor_b32 m0, s46, 3072\n"
"ds_read_b64_tr_b16 v[80:81], v26 offset:4096\n"
"buffer_load_dwordx4 v11, s[24:27], s44 offen sc1 lds\n"
"ds_read_b64_tr_b16 v[82:83], v26 offset:4608\n"
"ds_read_b64_tr_b16 v[84:85], v27 offset:4096\n"
"ds_read_b64_tr_b16 v[86:87], v27 offset:4608\n"
"s_add_u32 m0, s49, 0\n"
"ds_read_b64_tr_b16 v[88:89], v28 offset:4096\n"
"buffer_load_dwordx4 v20, s[28:31], s45 offen lds\n"
"ds_read_b64_tr_b16 v[90:91], v28 offset:4608\n"
"ds_read_b64_tr_b16 v[92:93], v29 offset:4096\n"
"ds_read_b64_tr_b16 v[94:95], v29 offset:4608\n"
"s_add_u32 m0, s49, 1024\n"
"ds_read_b128 v[48:51], v25 offset:0\n"
"buffer_load_dwordx4 v21, s[28:31], s45 offen lds\n"
"ds_read_b128 v[52:55], v25 offset:2048\n"
"ds_read_b128 v[56:59], v25 offset:4096\n"
"ds_read_b128 v[60:63], v25 offset:6144\n"
"buffer_store_dwordx4 v[180:183], v13, s[36:39], s90 offen offset:1024 sc1\n"
"s_waitcnt vmcnt(10) lgkmcnt(0)\n"
"s_barrier\n"
"v_mfma_f32_16x16x32_bf16 v[192:195], v[64:67], v[32:35], v[192:195]\n"
"v_mfma_f32_16x16x32_bf16 v[192:195], v[80:83], v[48:51], v[192:195]\n"
"s_add_u32 s68, s68, 1\n"
"s_cmp_eq_u32 s68, 3\n"
"s_cselect_b32 s48, 4294901760, s86\n"
"s_cselect_b32 s68, 0, s68\n"
"v_mfma_f32_16x16x32_bf16 v[208:211], v[80:83], v[52:55], v[208:211]\n"
"s_add_u32 s69, s69, 1\n"
"s_cmp_eq_u32 s69, 4\n"
"s_cselect_b32 s60, 4294918144, s87\n"
"s_cselect_b32 s69, 0, s69\n"
"v_mfma_f32_16x16x32_bf16 v[208:211], v[64:67], v[36:39], v[208:211]\n"
"v_mfma_f32_16x16x32_bf16 v[224:227], v[64:67], v[40:43], v[224:227]\n"
"v_add_u32 v24, s48, v24\n"
"v_mfma_f32_16x16x32_bf16 v[224:227], v[80:83], v[56:59], v[224:227]\n"
"v_add_u32 v25, s48, v25\n"
"v_mfma_f32_16x16x32_bf16 v[240:243], v[80:83], v[60:63], v[240:243]\n"
"v_add_u32 v26, s60, v26\n"
"v_mfma_f32_16x16x32_bf16 v[240:243], v[64:67], v[44:47], v[240:243]\n"
"v_add_u32 v27, s60, v27\n"
"v_mfma_f32_16x16x32_bf16 v[244:247], v[68:71], v[44:47], v[244:247]\n"
"v_add_u32 v28, s60, v28\n"
"v_mfma_f32_16x16x32_bf16 v[244:247], v[84:87], v[60:63], v[244:247]\n"
"v_add_u32 v29, s60, v29\n"
"v_mfma_f32_16x16x32_bf16 v[228:231], v[84:87], v[56:59], v[228:231]\n"
"v_mfma_f32_16x16x32_bf16 v[228:231], v[68:71], v[40:43], v[228:231]\n"
"v_mfma_f32_16x16x32_bf16 v[212:215], v[68:71], v[36:39], v[212:215]\n"
"s_add_u32 s44, s44, 128\n"
"s_add_u32 s46, s46, 32768\n"
"s_cmp_ge_u32 s46, 98304\n"
"s_cselect_b32 s50, 98304, 0\n"
"s_sub_u32 s46, s46, s50\n"
"s_add_u32 s45, s45, 131072\n"
"s_add_u32 s49, s49, 16384\n"
"s_cmp_ge_u32 s49, 163840\n"
"s_cselect_b32 s50, 65536, 0\n"
"s_sub_u32 s49, s49, s50\n"
"v_mfma_f32_16x16x32_bf16 v[212:215], v[84:87], v[52:55], v[212:215]\n"
"v_mfma_f32_16x16x32_bf16 v[196:199], v[84:87], v[48:51], v[196:199]\n"
"v_mfma_f32_16x16x32_bf16 v[196:199], v[68:71], v[32:35], v[196:199]\n"
"v_mfma_f32_16x16x32_bf16 v[200:203], v[72:75], v[32:35], v[200:203]\n"
"v_mfma_f32_16x16x32_bf16 v[200:203], v[88:91], v[48:51], v[200:203]\n"
"v_mfma_f32_16x16x32_bf16 v[216:219], v[88:91], v[52:55], v[216:219]\n"
"v_mfma_f32_16x16x32_bf16 v[216:219], v[72:75], v[36:39], v[216:219]\n"
"v_mfma_f32_16x16x32_bf16 v[232:235], v[72:75], v[40:43], v[232:235]\n"
"v_mfma_f32_16x16x32_bf16 v[232:235], v[88:91], v[56:59], v[232:235]\n"
"v_mfma_f32_16x16x32_bf16 v[248:251], v[88:91], v[60:63], v[248:251]\n"
"v_mfma_f32_16x16x32_bf16 v[248:251], v[72:75], v[44:47], v[248:251]\n"
"v_mfma_f32_16x16x32_bf16 v[252:255], v[76:79], v[44:47], v[252:255]\n"
"v_mfma_f32_16x16x32_bf16 v[252:255], v[92:95], v[60:63], v[252:255]\n"
"v_mfma_f32_16x16x32_bf16 v[236:239], v[92:95], v[56:59], v[236:239]\n"
"v_mfma_f32_16x16x32_bf16 v[236:239], v[76:79], v[40:43], v[236:239]\n"
"v_mfma_f32_16x16x32_bf16 v[220:223], v[76:79], v[36:39], v[220:223]\n"
"v_mfma_f32_16x16x32_bf16 v[220:223], v[92:95], v[52:55], v[220:223]\n"
"v_mfma_f32_16x16x32_bf16 v[204:207], v[92:95], v[48:51], v[204:207]\n"
"v_mfma_f32_16x16x32_bf16 v[204:207], v[76:79], v[32:35], v[204:207]\n"
"s_barrier\n"
"s_xor_b32 m0, s46, 0\n"
"ds_read_b64_tr_b16 v[64:65], v26 offset:0\n"
"buffer_load_dwordx4 v8, s[24:27], s44 offen sc1 lds\n"
"ds_read_b64_tr_b16 v[66:67], v26 offset:512\n"
"ds_read_b64_tr_b16 v[68:69], v27 offset:0\n"
"ds_read_b64_tr_b16 v[70:71], v27 offset:512\n"
"s_xor_b32 m0, s46, 1024\n"
"ds_read_b64_tr_b16 v[72:73], v28 offset:0\n"
"buffer_load_dwordx4 v9, s[24:27], s44 offen sc1 lds\n"
"ds_read_b64_tr_b16 v[74:75], v28 offset:512\n"
"ds_read_b64_tr_b16 v[76:77], v29 offset:0\n"
"ds_read_b64_tr_b16 v[78:79], v29 offset:512\n"
"s_xor_b32 m0, s46, 2048\n"
"ds_read_b128 v[32:35], v24 offset:0\n"
"buffer_load_dwordx4 v10, s[24:27], s44 offen sc1 lds\n"
"ds_read_b128 v[36:39], v24 offset:2048\n"
"ds_read_b128 v[40:43], v24 offset:4096\n"
"ds_read_b128 v[44:47], v24 offset:6144\n"
"s_xor_b32 m0, s46, 3072\n"
"ds_read_b64_tr_b16 v[80:81], v26 offset:4096\n"
"buffer_load_dwordx4 v11, s[24:27], s44 offen sc1 lds\n"
"ds_read_b64_tr_b16 v[82:83], v26 offset:4608\n"
"ds_read_b64_tr_b16 v[84:85], v27 offset:4096\n"
"ds_read_b64_tr_b16 v[86:87], v27 offset:4608\n"
"s_add_u32 m0, s49, 0\n"
"ds_read_b64_tr_b16 v[88:89], v28 offset:4096\n"
"buffer_load_dwordx4 v20, s[28:31], s45 offen lds\n"
"ds_read_b64_tr_b16 v[90:91], v28 offset:4608\n"
"ds_read_b64_tr_b16 v[92:93], v29 offset:4096\n"
"ds_read_b64_tr_b16 v[94:95], v29 offset:4608\n"
"s_add_u32 m0, s49, 1024\n"
"ds_read_b128 v[48:51], v25 offset:0\n"
"buffer_load_dwordx4 v21, s[28:31], s45 offen lds\n"
"ds_read_b128 v[52:55], v25 offset:2048\n"
"ds_read_b128 v[56:59], v25 offset:4096\n"
"ds_read_b128 v[60:63], v25 offset:6144\n"
"s_waitcnt vmcnt(9) lgkmcnt(0)\n"
"s_barrier\n"
"v_mfma_f32_16x16x32_bf16 v[192:195], v[64:67], v[32:35], v[192:195]\n"
"v_mfma_f32_16x16x32_bf16 v[192:195], v[80:83], v[48:51], v[192:195]\n"
"s_add_u32 s68, s68, 1\n"
"s_cmp_eq_u32 s68, 3\n"
"s_cselect_b32 s48, 4294901760, s86\n"
"s_cselect_b32 s68, 0, s68\n"
"v_mfma_f32_16x16x32_bf16 v[208:211], v[80:83], v[52:55], v[208:211]\n"
"s_add_u32 s69, s69, 1\n"
"s_cmp_eq_u32 s69, 4\n"
"s_cselect_b32 s60, 4294918144, s87\n"
"s_cselect_b32 s69, 0, s69\n"
"v_mfma_f32_16x16x32_bf16 v[208:211], v[64:67], v[36:39], v[208:211]\n"
"v_mfma_f32_16x16x32_bf16 v[224:227], v[64:67], v[40:43], v[224:227]\n"
"v_add_u32 v24, s48, v24\n"
"v_mfma_f32_16x16x32_bf16 v[224:227], v[80:83], v[56:59], v[224:227]\n"
"v_add_u32 v25, s48, v25\n"
"v_mfma_f32_16x16x32_bf16 v[240:243], v[80:83], v[60:63], v[240:243]\n"
"v_add_u32 v26, s60, v26\n"
"v_mfma_f32_16x16x32_bf16 v[240:243], v[64:67], v[44:47], v[240:243]\n"
"v_add_u32 v27, s60, v27\n"
"v_mfma_f32_16x16x32_bf16 v[244:247], v[68:71], v[44:47], v[244:247]\n"
"v_add_u32 v28, s60, v28\n"
"v_mfma_f32_16x16x32_bf16 v[244:247], v[84:87], v[60:63], v[244:247]\n"
"v_add_u32 v29, s60, v29\n"
"v_mfma_f32_16x16x32_bf16 v[228:231], v[84:87], v[56:59], v[228:231]\n"
"v_mfma_f32_16x16x32_bf16 v[228:231], v[68:71], v[40:43], v[228:231]\n"
"v_mfma_f32_16x16x32_bf16 v[212:215], v[68:71], v[36:39], v[212:215]\n"
"s_add_u32 s44, s44, 128\n"
"s_add_u32 s46, s46, 32768\n"
"s_cmp_ge_u32 s46, 98304\n"
"s_cselect_b32 s50, 98304, 0\n"
"s_sub_u32 s46, s46, s50\n"
"s_add_u32 s45, s45, 131072\n"
"s_add_u32 s49, s49, 16384\n"
"s_cmp_ge_u32 s49, 163840\n"
"s_cselect_b32 s50, 65536, 0\n"
"s_sub_u32 s49, s49, s50\n"
"v_mfma_f32_16x16x32_bf16 v[212:215], v[84:87], v[52:55], v[212:215]\n"
"v_mfma_f32_16x16x32_bf16 v[196:199], v[84:87], v[48:51], v[196:199]\n"
"v_mfma_f32_16x16x32_bf16 v[196:199], v[68:71], v[32:35], v[196:199]\n"
"v_mfma_f32_16x16x32_bf16 v[200:203], v[72:75], v[32:35], v[200:203]\n"
"v_mfma_f32_16x16x32_bf16 v[200:203], v[88:91], v[48:51], v[200:203]\n"
"v_mfma_f32_16x16x32_bf16 v[216:219], v[88:91], v[52:55], v[216:219]\n"
"v_mfma_f32_16x16x32_bf16 v[216:219], v[72:75], v[36:39], v[216:219]\n"
"v_mfma_f32_16x16x32_bf16 v[232:235], v[72:75], v[40:43], v[232:235]\n"
"v_mfma_f32_16x16x32_bf16 v[232:235], v[88:91], v[56:59], v[232:235]\n"
"v_mfma_f32_16x16x32_bf16 v[248:251], v[88:91], v[60:63], v[248:251]\n"
"v_mfma_f32_16x16x32_bf16 v[248:251], v[72:75], v[44:47], v[248:251]\n"
"v_mfma_f32_16x16x32_bf16 v[252:255], v[76:79], v[44:47], v[252:255]\n"
"v_mfma_f32_16x16x32_bf16 v[252:255], v[92:95], v[60:63], v[252:255]\n"
"v_mfma_f32_16x16x32_bf16 v[236:239], v[92:95], v[56:59], v[236:239]\n"
"v_mfma_f32_16x16x32_bf16 v[236:239], v[76:79], v[40:43], v[236:239]\n"
"v_mfma_f32_16x16x32_bf16 v[220:223], v[76:79], v[36:39], v[220:223]\n"
"v_mfma_f32_16x16x32_bf16 v[220:223], v[92:95], v[52:55], v[220:223]\n"
"v_mfma_f32_16x16x32_bf16 v[204:207], v[92:95], v[48:51], v[204:207]\n"
"v_mfma_f32_16x16x32_bf16 v[204:207], v[76:79], v[32:35], v[204:207]\n"
"s_barrier\n"
"s_xor_b32 m0, s46, 0\n"
"ds_read_b64_tr_b16 v[64:65], v26 offset:0\n"
"buffer_load_dwordx4 v8, s[24:27], s44 offen sc1 lds\n"
"ds_read_b64_tr_b16 v[66:67], v26 offset:512\n"
"ds_read_b64_tr_b16 v[68:69], v27 offset:0\n"
"ds_read_b64_tr_b16 v[70:71], v27 offset:512\n"
"s_xor_b32 m0, s46, 1024\n"
"ds_read_b64_tr_b16 v[72:73], v28 offset:0\n"
"buffer_load_dwordx4 v9, s[24:27], s44 offen sc1 lds\n"
"ds_read_b64_tr_b16 v[74:75], v28 offset:512\n"
"ds_read_b64_tr_b16 v[76:77], v29 offset:0\n"
"ds_read_b64_tr_b16 v[78:79], v29 offset:512\n"
"s_xor_b32 m0, s46, 2048\n"
"ds_read_b128 v[32:35], v24 offset:0\n"
"buffer_load_dwordx4 v10, s[24:27], s44 offen sc1 lds\n"
"ds_read_b128 v[36:39], v24 offset:2048\n"
"ds_read_b128 v[40:43], v24 offset:4096\n"
"ds_read_b128 v[44:47], v24 offset:6144\n"
"s_xor_b32 m0, s46, 3072\n"
"ds_read_b64_tr_b16 v[80:81], v26 offset:4096\n"
"buffer_load_dwordx4 v11, s[24:27], s44 offen sc1 lds\n"
"ds_read_b64_tr_b16 v[82:83], v26 offset:4608\n"
"ds_read_b64_tr_b16 v[84:85], v27 offset:4096\n"
"ds_read_b64_tr_b16 v[86:87], v27 offset:4608\n"
"s_add_u32 m0, s49, 0\n"
"ds_read_b64_tr_b16 v[88:89], v28 offset:4096\n"
"buffer_load_dwordx4 v20, s[28:31], s45 offen lds\n"
"ds_read_b64_tr_b16 v[90:91], v28 offset:4608\n"
"ds_read_b64_tr_b16 v[92:93], v29 offset:4096\n"
"ds_read_b64_tr_b16 v[94:95], v29 offset:4608\n"
"s_add_u32 m0, s49, 1024\n"
"ds_read_b128 v[48:51], v25 offset:0\n"
"buffer_load_dwordx4 v21, s[28:31], s45 offen lds\n"
"ds_read_b128 v[52:55], v25 offset:2048\n"
"ds_read_b128 v[56:59], v25 offset:4096\n"
"ds_read_b128 v[60:63], v25 offset:6144\n"
"s_waitcnt vmcnt(8) lgkmcnt(0)\n"
"s_barrier\n"
"v_mfma_f32_16x16x32_bf16 v[192:195], v[64:67], v[32:35], v[192:195]\n"
"v_mfma_f32_16x16x32_bf16 v[192:195], v[80:83], v[48:51], v[192:195]\n"
"s_add_u32 s68, s68, 1\n"
"s_cmp_eq_u32 s68, 3\n"
"s_cselect_b32 s48, 4294901760, s86\n"
"s_cselect_b32 s68, 0, s68\n"
"v_mfma_f32_16x16x32_bf16 v[208:211], v[80:83], v[52:55], v[208:211]\n"
"s_add_u32 s69, s69, 1\n"
"s_cmp_eq_u32 s69, 4\n"
"s_cselect_b32 s60, 4294918144, s87\n"
"s_cselect_b32 s69, 0, s69\n"
"v_mfma_f32_16x16x32_bf16 v[208:211], v[64:67], v[36:39], v[208:211]\n"
"v_mfma_f32_16x16x32_bf16 v[224:227], v[64:67], v[40:43], v[224:227]\n"
"v_add_u32 v24, s48, v24\n"
"v_mfma_f32_16x16x32_bf16 v[224:227], v[80:83], v[56:59], v[224:227]\n"
"v_add_u32 v25, s48, v25\n"
"v_mfma_f32_16x16x32_bf16 v[240:243], v[80:83], v[60:63], v[240:243]\n"
"v_add_u32 v26, s60, v26\n"
"v_mfma_f32_16x16x32_bf16 v[240:243], v[64:67], v[44:47], v[240:243]\n"
"v_add_u32 v27, s60, v27\n"
"v_mfma_f32_16x16x32_bf16 v[244:247], v[68:71], v[44:47], v[244:247]\n"
"v_add_u32 v28, s60, v28\n"
"v_mfma_f32_16x16x32_bf16 v[244:247], v[84:87], v[60:63], v[244:247]\n"
"v_add_u32 v29, s60, v29\n"
"v_mfma_f32_16x16x32_bf16 v[228:231], v[84:87], v[56:59], v[228:231]\n"
"v_mfma_f32_16x16x32_bf16 v[228:231], v[68:71], v[40:43], v[228:231]\n"
"v_mfma_f32_16x16x32_bf16 v[212:215], v[68:71], v[36:39], v[212:215]\n"
"s_add_u32 s44, s44, 128\n"
"s_add_u32 s46, s46, 32768\n"
"s_cmp_ge_u32 s46, 98304\n"
"s_cselect_b32 s50, 98304, 0\n"
"s_sub_u32 s46, s46, s50\n"
"s_add_u32 s45, s45, 131072\n"
"s_add_u32 s49, s49, 16384\n"
"s_cmp_ge_u32 s49, 163840\n"
"s_cselect_b32 s50, 65536, 0\n"
"s_sub_u32 s49, s49, s50\n"
"v_mfma_f32_16x16x32_bf16 v[212:215], v[84:87], v[52:55], v[212:215]\n"
"v_mfma_f32_16x16x32_bf16 v[196:199], v[84:87], v[48:51], v[196:199]\n"
"v_mfma_f32_16x16x32_bf16 v[196:199], v[68:71], v[32:35], v[196:199]\n"
"v_mfma_f32_16x16x32_bf16 v[200:203], v[72:75], v[32:35], v[200:203]\n"
"v_mfma_f32_16x16x32_bf16 v[200:203], v[88:91], v[48:51], v[200:203]\n"
"v_mfma_f32_16x16x32_bf16 v[216:219], v[88:91], v[52:55], v[216:219]\n"
"v_mfma_f32_16x16x32_bf16 v[216:219], v[72:75], v[36:39], v[216:219]\n"
"v_mfma_f32_16x16x32_bf16 v[232:235], v[72:75], v[40:43], v[232:235]\n"
"v_mfma_f32_16x16x32_bf16 v[232:235], v[88:91], v[56:59], v[232:235]\n"
"v_mfma_f32_16x16x32_bf16 v[248:251], v[88:91], v[60:63], v[248:251]\n"
"v_mfma_f32_16x16x32_bf16 v[248:251], v[72:75], v[44:47], v[248:251]\n"
"v_mfma_f32_16x16x32_bf16 v[252:255], v[76:79], v[44:47], v[252:255]\n"
"v_mfma_f32_16x16x32_bf16 v[252:255], v[92:95], v[60:63], v[252:255]\n"
"v_mfma_f32_16x16x32_bf16 v[236:239], v[92:95], v[56:59], v[236:239]\n"
"v_mfma_f32_16x16x32_bf16 v[236:239], v[76:79], v[40:43], v[236:239]\n"
"v_mfma_f32_16x16x32_bf16 v[220:223], v[76:79], v[36:39], v[220:223]\n"
"v_mfma_f32_16x16x32_bf16 v[220:223], v[92:95], v[52:55], v[220:223]\n"
"v_mfma_f32_16x16x32_bf16 v[204:207], v[92:95], v[48:51], v[204:207]\n"
"v_mfma_f32_16x16x32_bf16 v[204:207], v[76:79], v[32:35], v[204:207]\n"
"s_barrier\n"
"s_lshl_b32 s50, s63, 1\n"
"s_add_u32 s50, s50, 0\n"
"s_lshl_b32 s50, s50, 3\n"
"s_add_u32 s50, s50, s21\n"
"s_lshl_b32 s50, s50, 7\n"
"s_add_u32 s50, s50, 4096\n"
"s_lshl_b32 s51, s62, 2\n"
"s_add_u32 s50, s50, s51\n"
"v_mov_b32 v2, s50\n"
"v_mov_b32 v3, s61\n"
"s_mov_b64 exec, 1\n"
"global_store_dword v2, v3, s[6:7] sc1\n"
"s_mov_b64 exec, -1\n"
"s_xor_b32 m0, s46, 0\n"
"ds_read_b64_tr_b16 v[64:65], v26 offset:0\n"
"buffer_load_dwordx4 v8, s[24:27], s44 offen sc1 lds\n"
"ds_read_b64_tr_b16 v[66:67], v26 offset:512\n"
"ds_read_b64_tr_b16 v[68:69], v27 offset:0\n"
"ds_read_b64_tr_b16 v[70:71], v27 offset:512\n"
"s_xor_b32 m0, s46, 1024\n"
"ds_read_b64_tr_b16 v[72:73], v28 offset:0\n"
"buffer_load_dwordx4 v9, s[24:27], s44 offen sc1 lds\n"
"ds_read_b64_tr_b16 v[74:75], v28 offset:512\n"
"ds_read_b64_tr_b16 v[76:77], v29 offset:0\n"
"ds_read_b64_tr_b16 v[78:79], v29 offset:512\n"
"s_xor_b32 m0, s46, 2048\n"
"ds_read_b128 v[32:35], v24 offset:0\n"
"buffer_load_dwordx4 v10, s[24:27], s44 offen sc1 lds\n"
"ds_read_b128 v[36:39], v24 offset:2048\n"
"ds_read_b128 v[40:43], v24 offset:4096\n"
"ds_read_b128 v[44:47], v24 offset:6144\n"
"s_xor_b32 m0, s46, 3072\n"
"ds_read_b64_tr_b16 v[80:81], v26 offset:4096\n"
"buffer_load_dwordx4 v11, s[24:27], s44 offen sc1 lds\n"
"ds_read_b64_tr_b16 v[82:83], v26 offset:4608\n"
"ds_read_b64_tr_b16 v[84:85], v27 offset:4096\n"
"ds_read_b64_tr_b16 v[86:87], v27 offset:4608\n"
"s_add_u32 m0, s49, 0\n"
"ds_read_b64_tr_b16 v[88:89], v28 offset:4096\n"
"buffer_load_dwordx4 v20, s[28:31], s45 offen lds\n"
"ds_read_b64_tr_b16 v[90:91], v28 offset:4608\n"
"ds_read_b64_tr_b16 v[92:93], v29 offset:4096\n"
"ds_read_b64_tr_b16 v[94:95], v29 offset:4608\n"
"s_add_u32 m0, s49, 1024\n"
"ds_read_b128 v[48:51], v25 offset:0\n"
"buffer_load_dwordx4 v21, s[28:31], s45 offen lds\n"
"ds_read_b128 v[52:55], v25 offset:2048\n"
"ds_read_b128 v[56:59], v25 offset:4096\n"
"ds_read_b128 v[60:63], v25 offset:6144\n"
"s_waitcnt vmcnt(9) lgkmcnt(0)\n"
"s_barrier\n"
"v_mfma_f32_16x16x32_bf16 v[192:195], v[64:67], v[32:35], v[192:195]\n"
"v_mfma_f32_16x16x32_bf16 v[192:195], v[80:83], v[48:51], v[192:195]\n"
"s_add_u32 s68, s68, 1\n"
"s_cmp_eq_u32 s68, 3\n"
"s_cselect_b32 s48, 4294901760, s86\n"
"s_cselect_b32 s68, 0, s68\n"
"v_mfma_f32_16x16x32_bf16 v[208:211], v[80:83], v[52:55], v[208:211]\n"
"s_add_u32 s69, s69, 1\n"
"s_cmp_eq_u32 s69, 4\n"
"s_cselect_b32 s60, 4294918144, s87\n"
"s_cselect_b32 s69, 0, s69\n"
"v_mfma_f32_16x16x32_bf16 v[208:211], v[64:67], v[36:39], v[208:211]\n"
"v_mfma_f32_16x16x32_bf16 v[224:227], v[64:67], v[40:43], v[224:227]\n"
"v_add_u32 v24, s48, v24\n"
"v_mfma_f32_16x16x32_bf16 v[224:227], v[80:83], v[56:59], v[224:227]\n"
"v_add_u32 v25, s48, v25\n"
"v_mfma_f32_16x16x32_bf16 v[240:243], v[80:83], v[60:63], v[240:243]\n"
"v_add_u32 v26, s60, v26\n"
"v_mfma_f32_16x16x32_bf16 v[240:243], v[64:67], v[44:47], v[240:243]\n"
"v_add_u32 v27, s60, v27\n"
"v_mfma_f32_16x16x32_bf16 v[244:247], v[68:71], v[44:47], v[244:247]\n"
"v_add_u32 v28, s60, v28\n"
"v_mfma_f32_16x16x32_bf16 v[244:247], v[84:87], v[60:63], v[244:247]\n"
"v_add_u32 v29, s60, v29\n"
"v_mfma_f32_16x16x32_bf16 v[228:231], v[84:87], v[56:59], v[228:231]\n"
"v_mfma_f32_16x16x32_bf16 v[228:231], v[68:71], v[40:43], v[228:231]\n"
"v_mfma_f32_16x16x32_bf16 v[212:215], v[68:71], v[36:39], v[212:215]\n"
"s_add_u32 s44, s44, 128\n"
"s_add_u32 s46, s46, 32768\n"
"s_cmp_ge_u32 s46, 98304\n"
"s_cselect_b32 s50, 98304, 0\n"
"s_sub_u32 s46, s46, s50\n"
"s_add_u32 s45, s45, 131072\n"
"s_add_u32 s49, s49, 16384\n"
"s_cmp_ge_u32 s49, 163840\n"
"s_cselect_b32 s50, 65536, 0\n"
"s_sub_u32 s49, s49, s50\n"
"v_mfma_f32_16x16x32_bf16 v[212:215], v[84:87], v[52:55], v[212:215]\n"
"v_mfma_f32_16x16x32_bf16 v[196:199], v[84:87], v[48:51], v[196:199]\n"
"v_mfma_f32_16x16x32_bf16 v[196:199], v[68:71], v[32:35], v[196:199]\n"
"v_mfma_f32_16x16x32_bf16 v[200:203], v[72:75], v[32:35], v[200:203]\n"
"v_mfma_f32_16x16x32_bf16 v[200:203], v[88:91], v[48:51], v[200:203]\n"
"v_mfma_f32_16x16x32_bf16 v[216:219], v[88:91], v[52:55], v[216:219]\n"
"v_mfma_f32_16x16x32_bf16 v[216:219], v[72:75], v[36:39], v[216:219]\n"
"v_mfma_f32_16x16x32_bf16 v[232:235], v[72:75], v[40:43], v[232:235]\n"
"v_mfma_f32_16x16x32_bf16 v[232:235], v[88:91], v[56:59], v[232:235]\n"
"v_mfma_f32_16x16x32_bf16 v[248:251], v[88:91], v[60:63], v[248:251]\n"
"v_mfma_f32_16x16x32_bf16 v[248:251], v[72:75], v[44:47], v[248:251]\n"
"v_mfma_f32_16x16x32_bf16 v[252:255], v[76:79], v[44:47], v[252:255]\n"
"v_mfma_f32_16x16x32_bf16 v[252:255], v[92:95], v[60:63], v[252:255]\n"
"v_mfma_f32_16x16x32_bf16 v[236:239], v[92:95], v[56:59], v[236:239]\n"
"v_mfma_f32_16x16x32_bf16 v[236:239], v[76:79], v[40:43], v[236:239]\n"
"v_mfma_f32_16x16x32_bf16 v[220:223], v[76:79], v[36:39], v[220:223]\n"
"v_mfma_f32_16x16x32_bf16 v[220:223], v[92:95], v[52:55], v[220:223]\n"
"v_mfma_f32_16x16x32_bf16 v[204:207], v[92:95], v[48:51], v[204:207]\n"
"v_mfma_f32_16x16x32_bf16 v[204:207], v[76:79], v[32:35], v[204:207]\n"
"s_barrier\n"
"s_mov_b32 s47, 2\n"
"P2_loop0:\n"
"s_xor_b32 m0, s46, 0\n"
"ds_read_b64_tr_b16 v[64:65], v26 offset:0\n"
"buffer_load_dwordx4 v8, s[24:27], s44 offen sc1 lds\n"
"ds_read_b64_tr_b16 v[66:67], v26 offset:512\n"
"ds_read_b64_tr_b16 v[68:69], v27 offset:0\n"
"ds_read_b64_tr_b16 v[70:71], v27 offset:512\n"
"s_xor_b32 m0, s46, 1024\n"
"ds_read_b64_tr_b16 v[72:73], v28 offset:0\n"
"buffer_load_dwordx4 v9, s[24:27], s44 offen sc1 lds\n"
"ds_read_b64_tr_b16 v[74:75], v28 offset:512\n"
"ds_read_b64_tr_b16 v[76:77], v29 offset:0\n"
"ds_read_b64_tr_b16 v[78:79], v29 offset:512\n"
"s_xor_b32 m0, s46, 2048\n"
"ds_read_b128 v[32:35], v24 offset:0\n"
"buffer_load_dwordx4 v10, s[24:27], s44 offen sc1 lds\n"
"ds_read_b128 v[36:39], v24 offset:2048\n"
"ds_read_b128 v[40:43], v24 offset:4096\n"
"ds_read_b128 v[44:47], v24 offset:6144\n"
"s_xor_b32 m0, s46, 3072\n"
"ds_read_b64_tr_b16 v[80:81], v26 offset:4096\n"
"buffer_load_dwordx4 v11, s[24:27], s44 offen sc1 lds\n"
"ds_read_b64_tr_b16 v[82:83], v26 offset:4608\n"
"ds_read_b64_tr_b16 v[84:85], v27 offset:4096\n"
"ds_read_b64_tr_b16 v[86:87], v27 offset:4608\n"
"s_add_u32 m0, s49, 0\n"
"ds_read_b64_tr_b16 v[88:89], v28 offset:4096\n"
"buffer_load_dwordx4 v20, s[28:31], s45 offen lds\n"
"ds_read_b64_tr_b16 v[90:91], v28 offset:4608\n"
"ds_read_b64_tr_b16 v[92:93], v29 offset:4096\n"
"ds_read_b64_tr_b16 v[94:95], v29 offset:4608\n"
"s_add_u32 m0, s49, 1024\n"
"ds_read_b128 v[48:51], v25 offset:0\n"
"buffer_load_dwordx4 v21, s[28:31], s45 offen lds\n"
"ds_read_b128 v[52:55], v25 offset:2048\n"
"ds_read_b128 v[56:59], v25 offset:4096\n"
"ds_read_b128 v[60:63], v25 offset:6144\n"
"s_waitcnt vmcnt(8) lgkmcnt(0)\n"
"s_barrier\n"
"v_mfma_f32_16x16x32_bf16 v[192:195], v[64:67], v[32:35], v[192:195]\n"
"v_mfma_f32_16x16x32_bf16 v[192:195], v[80:83], v[48:51], v[192:195]\n"
"s_add_u32 s68, s68, 1\n"
"s_cmp_eq_u32 s68, 3\n"
"s_cselect_b32 s48, 4294901760, s86\n"
"s_cselect_b32 s68, 0, s68\n"
"v_mfma_f32_16x16x32_bf16 v[208:211], v[80:83], v[52:55], v[208:211]\n"
"s_add_u32 s69, s69, 1\n"
"s_cmp_eq_u32 s69, 4\n"
"s_cselect_b32 s60, 4294918144, s87\n"
"s_cselect_b32 s69, 0, s69\n"
"v_mfma_f32_16x16x32_bf16 v[208:211], v[64:67], v[36:39], v[208:211]\n"
"v_mfma_f32_16x16x32_bf16 v[224:227], v[64:67], v[40:43], v[224:227]\n"
"v_add_u32 v24, s48, v24\n"
"v_mfma_f32_16x16x32_bf16 v[224:227], v[80:83], v[56:59], v[224:227]\n"
"v_add_u32 v25, s48, v25\n"
"v_mfma_f32_16x16x32_bf16 v[240:243], v[80:83], v[60:63], v[240:243]\n"
"v_add_u32 v26, s60, v26\n"
"v_mfma_f32_16x16x32_bf16 v[240:243], v[64:67], v[44:47], v[240:243]\n"
"v_add_u32 v27, s60, v27\n"
"v_mfma_f32_16x16x32_bf16 v[244:247], v[68:71], v[44:47], v[244:247]\n"
"v_add_u32 v28, s60, v28\n"
"v_mfma_f32_16x16x32_bf16 v[244:247], v[84:87], v[60:63], v[244:247]\n"
"v_add_u32 v29, s60, v29\n"
"v_mfma_f32_16x16x32_bf16 v[228:231], v[84:87], v[56:59], v[228:231]\n"
"v_mfma_f32_16x16x32_bf16 v[228:231], v[68:71], v[40:43], v[228:231]\n"
"v_mfma_f32_16x16x32_bf16 v[212:215], v[68:71], v[36:39], v[212:215]\n"
"s_add_u32 s44, s44, 128\n"
"s_add_u32 s46, s46, 32768\n"
"s_cmp_ge_u32 s46, 98304\n"
"s_cselect_b32 s50, 98304, 0\n"
"s_sub_u32 s46, s46, s50\n"
"s_add_u32 s45, s45, 131072\n"
"s_add_u32 s49, s49, 16384\n"
"s_cmp_ge_u32 s49, 163840\n"
"s_cselect_b32 s50, 65536, 0\n"
"s_sub_u32 s49, s49, s50\n"
"v_mfma_f32_16x16x32_bf16 v[212:215], v[84:87], v[52:55], v[212:215]\n"
"v_mfma_f32_16x16x32_bf16 v[196:199], v[84:87], v[48:51], v[196:199]\n"
"v_mfma_f32_16x16x32_bf16 v[196:199], v[68:71], v[32:35], v[196:199]\n"
"v_mfma_f32_16x16x32_bf16 v[200:203], v[72:75], v[32:35], v[200:203]\n"
"v_mfma_f32_16x16x32_bf16 v[200:203], v[88:91], v[48:51], v[200:203]\n"
"v_mfma_f32_16x16x32_bf16 v[216:219], v[88:91], v[52:55], v[216:219]\n"
"v_mfma_f32_16x16x32_bf16 v[216:219], v[72:75], v[36:39], v[216:219]\n"
"v_mfma_f32_16x16x32_bf16 v[232:235], v[72:75], v[40:43], v[232:235]\n"
"v_mfma_f32_16x16x32_bf16 v[232:235], v[88:91], v[56:59], v[232:235]\n"
"v_mfma_f32_16x16x32_bf16 v[248:251], v[88:91], v[60:63], v[248:251]\n"
"v_mfma_f32_16x16x32_bf16 v[248:251], v[72:75], v[44:47], v[248:251]\n"
"v_mfma_f32_16x16x32_bf16 v[252:255], v[76:79], v[44:47], v[252:255]\n"
"v_mfma_f32_16x16x32_bf16 v[252:255], v[92:95], v[60:63], v[252:255]\n"
"v_mfma_f32_16x16x32_bf16 v[236:239], v[92:95], v[56:59], v[236:239]\n"
"v_mfma_f32_16x16x32_bf16 v[236:239], v[76:79], v[40:43], v[236:239]\n"
"v_mfma_f32_16x16x32_bf16 v[220:223], v[76:79], v[36:39], v[220:223]\n"
"v_mfma_f32_16x16x32_bf16 v[220:223], v[92:95], v[52:55], v[220:223]\n"
"v_mfma_f32_16x16x32_bf16 v[204:207], v[92:95], v[48:51], v[204:207]\n"
"v_mfma_f32_16x16x32_bf16 v[204:207], v[76:79], v[32:35], v[204:207]\n"
"s_barrier\n"
"s_sub_u32 s47, s47, 1\n"
"s_cmp_lg_u32 s47, 0\n"
"s_cbranch_scc1 P2_loop0\n"
"s_lshl_b32 s50, s63, 1\n"
"s_add_u32 s50, s50, 0\n"
"s_lshl_b32 s50, s50, 3\n"
"s_add_u32 s50, s50, s21\n"
"s_lshl_b32 s50, s50, 7\n"
"s_add_u32 s50, s50, 4096\n"
"v_and_b32 v15, 3, v1\n"
"v_lshlrev_b32 v15, 2, v15\n"
"v_add_u32 v15, s50, v15\n"
"global_load_dword v15, v15, s[6:7] sc1\n"
"s_xor_b32 m0, s46, 0\n"
"ds_read_b64_tr_b16 v[64:65], v26 offset:0\n"
"buffer_load_dwordx4 v8, s[24:27], s44 offen sc1 lds\n"
"ds_read_b64_tr_b16 v[66:67], v26 offset:512\n"
"ds_read_b64_tr_b16 v[68:69], v27 offset:0\n"
"ds_read_b64_tr_b16 v[70:71], v27 offset:512\n"
"s_xor_b32 m0, s46, 1024\n"
"ds_read_b64_tr_b16 v[72:73], v28 offset:0\n"
"buffer_load_dwordx4 v9, s[24:27], s44 offen sc1 lds\n"
"ds_read_b64_tr_b16 v[74:75], v28 offset:512\n"
"ds_read_b64_tr_b16 v[76:77], v29 offset:0\n"
"ds_read_b64_tr_b16 v[78:79], v29 offset:512\n"
"s_xor_b32 m0, s46, 2048\n"
"ds_read_b128 v[32:35], v24 offset:0\n"
"buffer_load_dwordx4 v10, s[24:27], s44 offen sc1 lds\n"
"ds_read_b128 v[36:39], v24 offset:2048\n"
"ds_read_b128 v[40:43], v24 offset:4096\n"
"ds_read_b128 v[44:47], v24 offset:6144\n"
"s_xor_b32 m0, s46, 3072\n"
"ds_read_b64_tr_b16 v[80:81], v26 offset:4096\n"
"buffer_load_dwordx4 v11, s[24:27], s44 offen sc1 lds\n"
"ds_read_b64_tr_b16 v[82:83], v26 offset:4608\n"
"ds_read_b64_tr_b16 v[84:85], v27 offset:4096\n"
"ds_read_b64_tr_b16 v[86:87], v27 offset:4608\n"
"s_add_u32 m0, s49, 0\n"
"ds_read_b64_tr_b16 v[88:89], v28 offset:4096\n"
"buffer_load_dwordx4 v20, s[28:31], s45 offen lds\n"
"ds_read_b64_tr_b16 v[90:91], v28 offset:4608\n"
"ds_read_b64_tr_b16 v[92:93], v29 offset:4096\n"
"ds_read_b64_tr_b16 v[94:95], v29 offset:4608\n"
"s_add_u32 m0, s49, 1024\n"
"ds_read_b128 v[48:51], v25 offset:0\n"
"buffer_load_dwordx4 v21, s[28:31], s45 offen lds\n"
"ds_read_b128 v[52:55], v25 offset:2048\n"
"ds_read_b128 v[56:59], v25 offset:4096\n"
"ds_read_b128 v[60:63], v25 offset:6144\n"
"s_waitcnt vmcnt(9) lgkmcnt(0)\n"
"s_barrier\n"
"v_mfma_f32_16x16x32_bf16 v[192:195], v[64:67], v[32:35], v[192:195]\n"
"v_mfma_f32_16x16x32_bf16 v[192:195], v[80:83], v[48:51], v[192:195]\n"
"s_add_u32 s68, s68, 1\n"
"s_cmp_eq_u32 s68, 3\n"
"s_cselect_b32 s48, 4294901760, s86\n"
"s_cselect_b32 s68, 0, s68\n"
"v_mfma_f32_16x16x32_bf16 v[208:211], v[80:83], v[52:55], v[208:211]\n"
"s_add_u32 s69, s69, 1\n"
"s_cmp_eq_u32 s69, 4\n"
"s_cselect_b32 s60, 4294918144, s87\n"
"s_cselect_b32 s69, 0, s69\n"
"v_mfma_f32_16x16x32_bf16 v[208:211], v[64:67], v[36:39], v[208:211]\n"
"v_mfma_f32_16x16x32_bf16 v[224:227], v[64:67], v[40:43], v[224:227]\n"
"v_add_u32 v24, s48, v24\n"
"v_mfma_f32_16x16x32_bf16 v[224:227], v[80:83], v[56:59], v[224:227]\n"
"v_add_u32 v25, s48, v25\n"
"v_mfma_f32_16x16x32_bf16 v[240:243], v[80:83], v[60:63], v[240:243]\n"
"v_add_u32 v26, s60, v26\n"
"v_mfma_f32_16x16x32_bf16 v[240:243], v[64:67], v[44:47], v[240:243]\n"
"v_add_u32 v27, s60, v27\n"
"v_mfma_f32_16x16x32_bf16 v[244:247], v[68:71], v[44:47], v[244:247]\n"
"v_add_u32 v28, s60, v28\n"
"v_mfma_f32_16x16x32_bf16 v[244:247], v[84:87], v[60:63], v[244:247]\n"
"v_add_u32 v29, s60, v29\n"
"v_mfma_f32_16x16x32_bf16 v[228:231], v[84:87], v[56:59], v[228:231]\n"
"v_mfma_f32_16x16x32_bf16 v[228:231], v[68:71], v[40:43], v[228:231]\n"
"v_mfma_f32_16x16x32_bf16 v[212:215], v[68:71], v[36:39], v[212:215]\n"
"s_add_u32 s44, s44, 128\n"
"s_add_u32 s46, s46, 32768\n"
"s_cmp_ge_u32 s46, 98304\n"
"s_cselect_b32 s50, 98304, 0\n"
"s_sub_u32 s46, s46, s50\n"
"s_add_u32 s45, s45, 131072\n"
"s_add_u32 s49, s49, 16384\n"
"s_cmp_ge_u32 s49, 163840\n"
"s_cselect_b32 s50, 65536, 0\n"
"s_sub_u32 s49, s49, s50\n"
"v_mfma_f32_16x16x32_bf16 v[212:215], v[84:87], v[52:55], v[212:215]\n"
"v_mfma_f32_16x16x32_bf16 v[196:199], v[84:87], v[48:51], v[196:199]\n"
"v_mfma_f32_16x16x32_bf16 v[196:199], v[68:71], v[32:35], v[196:199]\n"
"v_mfma_f32_16x16x32_bf16 v[200:203], v[72:75], v[32:35], v[200:203]\n"
"v_mfma_f32_16x16x32_bf16 v[200:203], v[88:91], v[48:51], v[200:203]\n"
"v_mfma_f32_16x16x32_bf16 v[216:219], v[88:91], v[52:55], v[216:219]\n"
"v_mfma_f32_16x16x32_bf16 v[216:219], v[72:75], v[36:39], v[216:219]\n"
"v_mfma_f32_16x16x32_bf16 v[232:235], v[72:75], v[40:43], v[232:235]\n"
"v_mfma_f32_16x16x32_bf16 v[232:235], v[88:91], v[56:59], v[232:235]\n"
"v_mfma_f32_16x16x32_bf16 v[248:251], v[88:91], v[60:63], v[248:251]\n"
"v_mfma_f32_16x16x32_bf16 v[248:251], v[72:75], v[44:47], v[248:251]\n"
"v_mfma_f32_16x16x32_bf16 v[252:255], v[76:79], v[44:47], v[252:255]\n"
"v_mfma_f32_16x16x32_bf16 v[252:255], v[92:95], v[60:63], v[252:255]\n"
"v_mfma_f32_16x16x32_bf16 v[236:239], v[92:95], v[56:59], v[236:239]\n"
"v_mfma_f32_16x16x32_bf16 v[236:239], v[76:79], v[40:43], v[236:239]\n"
"v_mfma_f32_16x16x32_bf16 v[220:223], v[76:79], v[36:39], v[220:223]\n"
"v_mfma_f32_16x16x32_bf16 v[220:223], v[92:95], v[52:55], v[220:223]\n"
"v_mfma_f32_16x16x32_bf16 v[204:207], v[92:95], v[48:51], v[204:207]\n"
"v_mfma_f32_16x16x32_bf16 v[204:207], v[76:79], v[32:35], v[204:207]\n"
"s_barrier\n"
"s_xor_b32 m0, s46, 0\n"
"ds_read_b64_tr_b16 v[64:65], v26 offset:0\n"
"buffer_load_dwordx4 v8, s[24:27], s44 offen sc1 lds\n"
"ds_read_b64_tr_b16 v[66:67], v26 offset:512\n"
"ds_read_b64_tr_b16 v[68:69], v27 offset:0\n"
"ds_read_b64_tr_b16 v[70:71], v27 offset:512\n"
"ds_read_b64_tr_b16 v[72:73], v28 offset:0\n"
"ds_read_b64_tr_b16 v[74:75], v28 offset:512\n"
"s_xor_b32 m0, s46, 1024\n"
"ds_read_b64_tr_b16 v[76:77], v29 offset:0\n"
"buffer_load_dwordx4 v9, s[24:27], s44 offen sc1 lds\n"
"ds_read_b64_tr_b16 v[78:79], v29 offset:512\n"
"ds_read_b128 v[32:35], v24 offset:0\n"
"ds_read_b128 v[36:39], v24 offset:2048\n"
"ds_read_b128 v[40:43], v24 offset:4096\n"
"ds_read_b128 v[44:47], v24 offset:6144\n"
"s_xor_b32 m0, s46, 2048\n"
"ds_read_b64_tr_b16 v[80:81], v26 offset:4096\n"
"buffer_load_dwordx4 v10, s[24:27], s44 offen sc1 lds\n"
"ds_read_b64_tr_b16 v[82:83], v26 offset:4608\n"
"ds_read_b64_tr_b16 v[84:85], v27 offset:4096\n"
"ds_read_b64_tr_b16 v[86:87], v27 offset:4608\n"
"ds_read_b64_tr_b16 v[88:89], v28 offset:4096\n"
"ds_read_b64_tr_b16 v[90:91], v28 offset:4608\n"
"s_xor_b32 m0, s46, 3072\n"
"ds_read_b64_tr_b16 v[92:93], v29 offset:4096\n"
"buffer_load_dwordx4 v11, s[24:27], s44 offen sc1 lds\n"
"ds_read_b64_tr_b16 v[94:95], v29 offset:4608\n"
"ds_read_b128 v[48:51], v25 offset:0\n"
"ds_read_b128 v[52:55], v25 offset:2048\n"
"ds_read_b128 v[56:59], v25 offset:4096\n"
"ds_read_b128 v[60:63], v25 offset:6144\n"
"s_waitcnt vmcnt(6) lgkmcnt(0)\n"
"s_barrier\n"
"v_mfma_f32_16x16x32_bf16 v[192:195], v[64:67], v[32:35], v[192:195]\n"
"v_mfma_f32_16x16x32_bf16 v[192:195], v[80:83], v[48:51], v[192:195]\n"
"s_add_u32 s68, s68, 1\n"
"s_cmp_eq_u32 s68, 3\n"
"s_cselect_b32 s48, 4294901760, s86\n"
"s_cselect_b32 s68, 0, s68\n"
"v_mfma_f32_16x16x32_bf16 v[208:211], v[80:83], v[52:55], v[208:211]\n"
"s_add_u32 s69, s69, 1\n"
"s_cmp_eq_u32 s69, 4\n"
"s_cselect_b32 s60, 4294918144, s87\n"
"s_cselect_b32 s69, 0, s69\n"
"v_mfma_f32_16x16x32_bf16 v[208:211], v[64:67], v[36:39], v[208:211]\n"
"v_mfma_f32_16x16x32_bf16 v[224:227], v[64:67], v[40:43], v[224:227]\n"
"v_add_u32 v24, s48, v24\n"
"v_mfma_f32_16x16x32_bf16 v[224:227], v[80:83], v[56:59], v[224:227]\n"
"v_add_u32 v25, s48, v25\n"
"v_mfma_f32_16x16x32_bf16 v[240:243], v[80:83], v[60:63], v[240:243]\n"
"v_add_u32 v26, s60, v26\n"
"v_mfma_f32_16x16x32_bf16 v[240:243], v[64:67], v[44:47], v[240:243]\n"
"v_add_u32 v27, s60, v27\n"
"v_mfma_f32_16x16x32_bf16 v[244:247], v[68:71], v[44:47], v[244:247]\n"
"v_add_u32 v28, s60, v28\n"
"v_mfma_f32_16x16x32_bf16 v[244:247], v[84:87], v[60:63], v[244:247]\n"
"v_add_u32 v29, s60, v29\n"
"v_mfma_f32_16x16x32_bf16 v[228:231], v[84:87], v[56:59], v[228:231]\n"
"v_mfma_f32_16x16x32_bf16 v[228:231], v[68:71], v[40:43], v[228:231]\n"
"v_mfma_f32_16x16x32_bf16 v[212:215], v[68:71], v[36:39], v[212:215]\n"
"s_add_u32 s44, s44, 128\n"
"s_add_u32 s46, s46, 32768\n"
"s_cmp_ge_u32 s46, 98304\n"
"s_cselect_b32 s50, 98304, 0\n"
"s_sub_u32 s46, s46, s50\n"
"v_mfma_f32_16x16x32_bf16 v[212:215], v[84:87], v[52:55], v[212:215]\n"
"v_mfma_f32_16x16x32_bf16 v[196:199], v[84:87], v[48:51], v[196:199]\n"
"v_mfma_f32_16x16x32_bf16 v[196:199], v[68:71], v[32:35], v[196:199]\n"
"v_mfma_f32_16x16x32_bf16 v[200:203], v[72:75], v[32:35], v[200:203]\n"
"v_mfma_f32_16x16x32_bf16 v[200:203], v[88:91], v[48:51], v[200:203]\n"
"v_mfma_f32_16x16x32_bf16 v[216:219], v[88:91], v[52:55], v[216:219]\n"
"v_mfma_f32_16x16x32_bf16 v[216:219], v[72:75], v[36:39], v[216:219]\n"
"v_mfma_f32_16x16x32_bf16 v[232:235], v[72:75], v[40:43], v[232:235]\n"
"v_mfma_f32_16x16x32_bf16 v[232:235], v[88:91], v[56:59], v[232:235]\n"
"v_mfma_f32_16x16x32_bf16 v[248:251], v[88:91], v[60:63], v[248:251]\n"
"v_mfma_f32_16x16x32_bf16 v[248:251], v[72:75], v[44:47], v[248:251]\n"
"v_mfma_f32_16x16x32_bf16 v[252:255], v[76:79], v[44:47], v[252:255]\n"
"v_mfma_f32_16x16x32_bf16 v[252:255], v[92:95], v[60:63], v[252:255]\n"
"v_mfma_f32_16x16x32_bf16 v[236:239], v[92:95], v[56:59], v[236:239]\n"
"v_mfma_f32_16x16x32_bf16 v[236:239], v[76:79], v[40:43], v[236:239]\n"
"v_mfma_f32_16x16x32_bf16 v[220:223], v[76:79], v[36:39], v[220:223]\n"
"v_mfma_f32_16x16x32_bf16 v[220:223], v[92:95], v[52:55], v[220:223]\n"
"v_mfma_f32_16x16x32_bf16 v[204:207], v[92:95], v[48:51], v[204:207]\n"
"v_mfma_f32_16x16x32_bf16 v[204:207], v[76:79], v[32:35], v[204:207]\n"
"s_barrier\n"
"v_cmp_ne_u32 vcc, s61, v15\n"
"s_nop 1\n"
"s_cmp_eq_u64 vcc, 0\n"
"s_cbranch_scc1 P2_cg_ok\n"
"s_lshl_b32 s50, s63, 1\n"
"s_add_u32 s50, s50, 0\n"
"s_lshl_b32 s50, s50, 3\n"
"s_add_u32 s50, s50, s21\n"
"s_lshl_b32 s50, s50, 7\n"
"s_add_u32 s50, s50, 4096\n"
"v_and_b32 v2, 3, v1\n"
"v_lshlrev_b32 v2, 2, v2\n"
"v_add_u32 v2, s50, v2\n"
"s_mov_b32 s52, 0\n"
"P2_cg_spin:\n"
"global_load_dword v3, v2, s[6:7] sc1\n"
"s_waitcnt vmcnt(0)\n"
"v_cmp_ne_u32 vcc, s61, v3\n"
"s_nop 1\n"
"s_cmp_eq_u64 vcc, 0\n"
"s_cbranch_scc1 P2_cg_ok\n"
"s_add_u32 s52, s52, 1\n"
"s_cmp_lt_u32 s52, 0x8000\n"
"s_cbranch_scc0 P2_cg_ok\n"
"s_sleep 1\n"
"s_branch P2_cg_spin\n"
"P2_cg_ok:\n"
"s_lshl_b32 s50, s63, 1\n"
"s_add_u32 s50, s50, 0\n"
"s_lshl_b32 s50, s50, 2\n"
"s_add_u32 s50, s50, s62\n"
"s_lshl_b32 s50, s50, 16\n"
"s_lshl_b32 s51, s21, 11\n"
"s_add_u32 s50, s50, s51\n"
"s_add_u32 s40, s96, s50\n"
"s_addc_u32 s41, s97, 0\n"
"s_and_b32 s41, s41, 0xffff\n"
"s_mov_b32 s42, 51200\n"
"s_mov_b32 s43, 0x20000\n"
"v_lshlrev_b32 v13, 4, v1\n"
"s_add_u32 s50, s62, 1\n"
"s_and_b32 s50, s50, 3\n"
"s_lshl_b32 s50, s50, 14\n"
"buffer_load_dwordx4 v[144:147], v13, s[40:43], s50 offen offset:0 sc1\n"
"buffer_load_dwordx4 v[148:151], v13, s[40:43], s50 offen offset:1024 sc1\n"
"s_add_u32 s50, s62, 2\n"
"s_and_b32 s50, s50, 3\n"
"s_lshl_b32 s50, s50, 14\n"
"buffer_load_dwordx4 v[152:155], v13, s[40:43], s50 offen offset:0 sc1\n"
"buffer_load_dwordx4 v[156:159], v13, s[40:43], s50 offen offset:1024 sc1\n"
"s_add_u32 s50, s62, 3\n"
"s_and_b32 s50, s50, 3\n"
"s_lshl_b32 s50, s50, 14\n"
"buffer_load_dwordx4 v[160:163], v13, s[40:43], s50 offen offset:0 sc1\n"
"buffer_load_dwordx4 v[164:167], v13, s[40:43], s50 offen offset:1024 sc1\n"
"s_lshl_b32 s50, s22, 8\n"
"s_lshl_b32 s51, s34, 6\n"
"s_add_u32 s50, s50, s51\n"
"s_lshl_b32 s51, s62, 4\n"
"s_add_u32 s50, s50, s51\n"
"v_and_b32 v3, 15, v1\n"
"v_add_u32 v3, s50, v3\n"
"v_lshlrev_b32 v3, 11, v3\n"
"v_lshrrev_b32 v4, 4, v1\n"
"v_and_b32 v5, 1, v4\n"
"v_lshlrev_b32 v5, 5, v5\n"
"v_lshrrev_b32 v6, 1, v4\n"
"v_lshlrev_b32 v6, 4, v6\n"
"v_add_u32 v5, v5, v6\n"
"s_lshl_b32 s50, s23, 8\n"
"s_lshl_b32 s51, s35, 7\n"
"s_add_u32 s50, s50, s51\n"
"v_add_u32 v5, s50, v5\n"
"v_add_u32 v12, v3, v5\n"
"v_lshlrev_b32 v6, 3, v4\n"
"v_add_u32 v6, s50, v6\n"
"global_load_dwordx2 v[168:169], v6, s[12:13] offset:0\n"
"global_load_dwordx2 v[170:171], v6, s[12:13] offset:32\n"
"global_load_dwordx2 v[172:173], v6, s[12:13] offset:64\n"
"global_load_dwordx2 v[174:175], v6, s[12:13] offset:96\n"
"s_mov_b32 s36, s14\n"
"s_and_b32 s37, s15, 0xffff\n"
"s_mov_b32 s38, 4194304\n"
"s_mov_b32 s39, 0x20000\n"
"buffer_load_dwordx4 v[176:179], v12, s[36:39], 0 offen offset:0\n"
"buffer_load_dwordx4 v[180:183], v12, s[36:39], 0 offen offset:64\n"
"ds_read_b64_tr_b16 v[64:65], v26 offset:0\n"
"ds_read_b64_tr_b16 v[66:67], v26 offset:512\n"
"ds_read_b64_tr_b16 v[68:69], v27 offset:0\n"
"ds_read_b64_tr_b16 v[70:71], v27 offset:512\n"
"ds_read_b64_tr_b16 v[72:73], v28 offset:0\n"
"ds_read_b64_tr_b16 v[74:75], v28 offset:512\n"
"ds_read_b64_tr_b16 v[76:77], v29 offset:0\n"
"ds_read_b64_tr_b16 v[78:79], v29 offset:512\n"
"ds_read_b128 v[32:35], v24 offset:0\n"
"ds_read_b128 v[36:39], v24 offset:2048\n"
"ds_read_b128 v[40:43], v24 offset:4096\n"
"ds_read_b128 v[44:47], v24 offset:6144\n"
"ds_read_b64_tr_b16 v[80:81], v26 offset:4096\n"
"ds_read_b64_tr_b16 v[82:83], v26 offset:4608\n"
"ds_read_b64_tr_b16 v[84:85], v27 offset:4096\n"
"ds_read_b64_tr_b16 v[86:87], v27 offset:4608\n"
"ds_read_b64_tr_b16 v[88:89], v28 offset:4096\n"
"ds_read_b64_tr_b16 v[90:91], v28 offset:4608\n"
"ds_read_b64_tr_b16 v[92:93], v29 offset:4096\n"
"ds_read_b64_tr_b16 v[94:95], v29 offset:4608\n"
"ds_read_b128 v[48:51], v25 offset:0\n"
"ds_read_b128 v[52:55], v25 offset:2048\n"
"ds_read_b128 v[56:59], v25 offset:4096\n"
"ds_read_b128 v[60:63], v25 offset:6144\n"
"s_waitcnt vmcnt(12) lgkmcnt(0)\n"
"s_barrier\n"
"v_mfma_f32_16x16x32_bf16 v[192:195], v[64:67], v[32:35], v[192:195]\n"
"v_mfma_f32_16x16x32_bf16 v[192:195], v[80:83], v[48:51], v[192:195]\n"
"s_add_u32 s68, s68, 1\n"
"s_cmp_eq_u32 s68, 3\n"
"s_cselect_b32 s48, 4294901760, s86\n"
"s_cselect_b32 s68, 0, s68\n"
"v_mfma_f32_16x16x32_bf16 v[208:211], v[80:83], v[52:55], v[208:211]\n"
"s_add_u32 s69, s69, 1\n"
"s_cmp_eq_u32 s69, 4\n"
"s_cselect_b32 s60, 4294918144, s87\n"
"s_cselect_b32 s69, 0, s69\n"
"v_mfma_f32_16x16x32_bf16 v[208:211], v[64:67], v[36:39], v[208:211]\n"
"v_mfma_f32_16x16x32_bf16 v[224:227], v[64:67], v[40:43], v[224:227]\n"
"v_add_u32 v24, s48, v24\n"
"v_mfma_f32_16x16x32_bf16 v[224:227], v[80:83], v[56:59], v[224:227]\n"
"v_add_u32 v25, s48, v25\n"
"v_mfma_f32_16x16x32_bf16 v[240:243], v[80:83], v[60:63], v[240:243]\n"
"v_add_u32 v26, s60, v26\n"
"v_mfma_f32_16x16x32_bf16 v[240:243], v[64:67], v[44:47], v[240:243]\n"
"v_add_u32 v27, s60, v27\n"
"v_mfma_f32_16x16x32_bf16 v[244:247], v[68:71], v[44:47], v[244:247]\n"
"v_add_u32 v28, s60, v28\n"
"v_mfma_f32_16x16x32_bf16 v[244:247], v[84:87], v[60:63], v[244:247]\n"
"v_add_u32 v29, s60, v29\n"
"v_mfma_f32_16x16x32_bf16 v[228:231], v[84:87], v[56:59], v[228:231]\n"
"v_mfma_f32_16x16x32_bf16 v[228:231], v[68:71], v[40:43], v[228:231]\n"
"v_mfma_f32_16x16x32_bf16 v[212:215], v[68:71], v[36:39], v[212:215]\n"
"v_mfma_f32_16x16x32_bf16 v[212:215], v[84:87], v[52:55], v[212:215]\n"
"v_mfma_f32_16x16x32_bf16 v[196:199], v[84:87], v[48:51], v[196:199]\n"
"v_mfma_f32_16x16x32_bf16 v[196:199], v[68:71], v[32:35], v[196:199]\n"
"v_mfma_f32_16x16x32_bf16 v[200:203], v[72:75], v[32:35], v[200:203]\n"
"v_mfma_f32_16x16x32_bf16 v[200:203], v[88:91], v[48:51], v[200:203]\n"
"v_mfma_f32_16x16x32_bf16 v[216:219], v[88:91], v[52:55], v[216:219]\n"
"v_mfma_f32_16x16x32_bf16 v[216:219], v[72:75], v[36:39], v[216:219]\n"
"v_mfma_f32_16x16x32_bf16 v[232:235], v[72:75], v[40:43], v[232:235]\n"
"v_mfma_f32_16x16x32_bf16 v[232:235], v[88:91], v[56:59], v[232:235]\n"
"v_mfma_f32_16x16x32_bf16 v[248:251], v[88:91], v[60:63], v[248:251]\n"
"v_mfma_f32_16x16x32_bf16 v[248:251], v[72:75], v[44:47], v[248:251]\n"
"v_mfma_f32_16x16x32_bf16 v[252:255], v[76:79], v[44:47], v[252:255]\n"
"v_mfma_f32_16x16x32_bf16 v[252:255], v[92:95], v[60:63], v[252:255]\n"
"v_mfma_f32_16x16x32_bf16 v[236:239], v[92:95], v[56:59], v[236:239]\n"
"v_mfma_f32_16x16x32_bf16 v[236:239], v[76:79], v[40:43], v[236:239]\n"
"v_mfma_f32_16x16x32_bf16 v[220:223], v[76:79], v[36:39], v[220:223]\n"
"v_mfma_f32_16x16x32_bf16 v[220:223], v[92:95], v[52:55], v[220:223]\n"
"v_mfma_f32_16x16x32_bf16 v[204:207], v[92:95], v[48:51], v[204:207]\n"
"v_mfma_f32_16x16x32_bf16 v[204:207], v[76:79], v[32:35], v[204:207]\n"
"s_barrier\n"
"ds_read_b64_tr_b16 v[64:65], v26 offset:0\n"
"ds_read_b64_tr_b16 v[66:67], v26 offset:512\n"
"ds_read_b64_tr_b16 v[68:69], v27 offset:0\n"
"ds_read_b64_tr_b16 v[70:71], v27 offset:512\n"
"ds_read_b64_tr_b16 v[72:73], v28 offset:0\n"
"ds_read_b64_tr_b16 v[74:75], v28 offset:512\n"
"ds_read_b64_tr_b16 v[76:77], v29 offset:0\n"
"ds_read_b64_tr_b16 v[78:79], v29 offset:512\n"
"ds_read_b128 v[32:35], v24 offset:0\n"
"ds_read_b128 v[36:39], v24 offset:2048\n"
"ds_read_b128 v[40:43], v24 offset:4096\n"
"ds_read_b128 v[44:47], v24 offset:6144\n"
"ds_read_b64_tr_b16 v[80:81], v26 offset:4096\n"
"ds_read_b64_tr_b16 v[82:83], v26 offset:4608\n"
"ds_read_b64_tr_b16 v[84:85], v27 offset:4096\n"
"ds_read_b64_tr_b16 v[86:87], v27 offset:4608\n"
"ds_read_b64_tr_b16 v[88:89], v28 offset:4096\n"
"ds_read_b64_tr_b16 v[90:91], v28 offset:4608\n"
"ds_read_b64_tr_b16 v[92:93], v29 offset:4096\n"
"ds_read_b64_tr_b16 v[94:95], v29 offset:4608\n"
"ds_read_b128 v[48:51], v25 offset:0\n"
"ds_read_b128 v[52:55], v25 offset:2048\n"
"ds_read_b128 v[56:59], v25 offset:4096\n"
"ds_read_b128 v[60:63], v25 offset:6144\n"
"s_waitcnt lgkmcnt(0)\n"
"s_barrier\n"
"v_mfma_f32_16x16x32_bf16 v[192:195], v[64:67], v[32:35], v[192:195]\n"
"v_mfma_f32_16x16x32_bf16 v[192:195], v[80:83], v[48:51], v[192:195]\n"
"s_add_u32 s68, s68, 1\n"
"s_cmp_eq_u32 s68, 3\n"
"s_cselect_b32 s48, 4294901760, s86\n"
"s_cselect_b32 s68, 0, s68\n"
"v_mfma_f32_16x16x32_bf16 v[208:211], v[80:83], v[52:55], v[208:211]\n"
"s_add_u32 s69, s69, 1\n"
"s_cmp_eq_u32 s69, 4\n"
"s_cselect_b32 s60, 4294918144, s87\n"
"s_cselect_b32 s69, 0, s69\n"
"v_mfma_f32_16x16x32_bf16 v[208:211], v[64:67], v[36:39], v[208:211]\n"
"v_mfma_f32_16x16x32_bf16 v[224:227], v[64:67], v[40:43], v[224:227]\n"
"v_add_u32 v24, s48, v24\n"
"v_mfma_f32_16x16x32_bf16 v[224:227], v[80:83], v[56:59], v[224:227]\n"
"v_add_u32 v25, s48, v25\n"
"v_mfma_f32_16x16x32_bf16 v[240:243], v[80:83], v[60:63], v[240:243]\n"
"v_add_u32 v26, s60, v26\n"
"v_mfma_f32_16x16x32_bf16 v[240:243], v[64:67], v[44:47], v[240:243]\n"
"v_add_u32 v27, s60, v27\n"
"v_mfma_f32_16x16x32_bf16 v[244:247], v[68:71], v[44:47], v[244:247]\n"
"v_add_u32 v28, s60, v28\n"
"v_mfma_f32_16x16x32_bf16 v[244:247], v[84:87], v[60:63], v[244:247]\n"
"v_add_u32 v29, s60, v29\n"
"v_mfma_f32_16x16x32_bf16 v[228:231], v[84:87], v[56:59], v[228:231]\n"
"v_mfma_f32_16x16x32_bf16 v[228:231], v[68:71], v[40:43], v[228:231]\n"
"v_mfma_f32_16x16x32_bf16 v[212:215], v[68:71], v[36:39], v[212:215]\n"
"v_mfma_f32_16x16x32_bf16 v[212:215], v[84:87], v[52:55], v[212:215]\n"
"v_mfma_f32_16x16x32_bf16 v[196:199], v[84:87], v[48:51], v[196:199]\n"
"v_mfma_f32_16x16x32_bf16 v[196:199], v[68:71], v[32:35], v[196:199]\n"
"v_mfma_f32_16x16x32_bf16 v[200:203], v[72:75], v[32:35], v[200:203]\n"
"v_mfma_f32_16x16x32_bf16 v[200:203], v[88:91], v[48:51], v[200:203]\n"
"v_mfma_f32_16x16x32_bf16 v[216:219], v[88:91], v[52:55], v[216:219]\n"
"v_mfma_f32_16x16x32_bf16 v[216:219], v[72:75], v[36:39], v[216:219]\n"
"v_mfma_f32_16x16x32_bf16 v[232:235], v[72:75], v[40:43], v[232:235]\n"
"v_mfma_f32_16x16x32_bf16 v[232:235], v[88:91], v[56:59], v[232:235]\n"
"v_mfma_f32_16x16x32_bf16 v[248:251], v[88:91], v[60:63], v[248:251]\n"
"v_mfma_f32_16x16x32_bf16 v[248:251], v[72:75], v[44:47], v[248:251]\n"
"v_mfma_f32_16x16x32_bf16 v[252:255], v[76:79], v[44:47], v[252:255]\n"
"v_mfma_f32_16x16x32_bf16 v[252:255], v[92:95], v[60:63], v[252:255]\n"
"v_mfma_f32_16x16x32_bf16 v[236:239], v[92:95], v[56:59], v[236:239]\n"
"v_mfma_f32_16x16x32_bf16 v[236:239], v[76:79], v[40:43], v[236:239]\n"
"v_mfma_f32_16x16x32_bf16 v[220:223], v[76:79], v[36:39], v[220:223]\n"
"v_mfma_f32_16x16x32_bf16 v[220:223], v[92:95], v[52:55], v[220:223]\n"
"v_mfma_f32_16x16x32_bf16 v[204:207], v[92:95], v[48:51], v[204:207]\n"
"v_mfma_f32_16x16x32_bf16 v[204:207], v[76:79], v[32:35], v[204:207]\n"
"s_barrier\n"
"s_cmp_eq_u32 s35, 0\n"
"s_cbranch_scc0 P2_lag\n"
"s_barrier\n"
"P2_lag:\n"
"s_nop 7\n"
"v_cvt_pk_bf16_f32 v208, v208, v209\n"
"v_cvt_pk_bf16_f32 v209, v210, v211\n"
"v_cvt_pk_bf16_f32 v210, v212, v213\n"
"v_cvt_pk_bf16_f32 v211, v214, v215\n"
"v_cvt_pk_bf16_f32 v212, v216, v217\n"
"v_cvt_pk_bf16_f32 v213, v218, v219\n"
"v_cvt_pk_bf16_f32 v214, v220, v221\n"
"v_cvt_pk_bf16_f32 v215, v222, v223\n"
"v_cvt_pk_bf16_f32 v224, v224, v225\n"
"v_cvt_pk_bf16_f32 v225, v226, v227\n"
"v_cvt_pk_bf16_f32 v226, v228, v229\n"
"v_cvt_pk_bf16_f32 v227, v230, v231\n"
"v_cvt_pk_bf16_f32 v228, v232, v233\n"
"v_cvt_pk_bf16_f32 v229, v234, v235\n"
"v_cvt_pk_bf16_f32 v230, v236, v237\n"
"v_cvt_pk_bf16_f32 v231, v238, v239\n"
"v_cvt_pk_bf16_f32 v240, v240, v241\n"
"v_cvt_pk_bf16_f32 v241, v242, v243\n"
"v_cvt_pk_bf16_f32 v242, v244, v245\n"
"v_cvt_pk_bf16_f32 v243, v246, v247\n"
"v_cvt_pk_bf16_f32 v244, v248, v249\n"
"v_cvt_pk_bf16_f32 v245, v250, v251\n"
"v_cvt_pk_bf16_f32 v246, v252, v253\n"
"v_cvt_pk_bf16_f32 v247, v254, v255\n"
"s_lshl_b32 s50, s63, 1\n"
"s_add_u32 s50, s50, 1\n"
"s_lshl_b32 s50, s50, 18\n"
"s_lshl_b32 s51, s21, 11\n"
"s_add_u32 s50, s50, s51\n"
"s_add_u32 s36, s96, s50\n"
"s_addc_u32 s37, s97, 0\n"
"s_and_b32 s37, s37, 0xffff\n"
"s_mov_b32 s38, 247808\n"
"s_mov_b32 s39, 0x20000\n"
"v_lshlrev_b32 v13, 4, v1\n"
"s_xor_b32 s50, s62, 1\n"
"s_lshl_b32 s50, s50, 2\n"
"s_add_u32 s50, s50, s62\n"
"s_lshl_b32 s88, s50, 14\n"
"s_xor_b32 s50, s62, 2\n"
"s_lshl_b32 s50, s50, 2\n"
"s_add_u32 s50, s50, s62\n"
"s_lshl_b32 s89, s50, 14\n"
"s_xor_b32 s50, s62, 3\n"
"s_lshl_b32 s50, s50, 2\n"
"s_add_u32 s50, s50, s62\n"
"s_lshl_b32 s90, s50, 14\n"
"buffer_store_dwordx4 v[208:211], v13, s[36:39], s88 offen offset:0 sc1\n"
"buffer_store_dwordx4 v[212:215], v13, s[36:39], s88 offen offset:1024 sc1\n"
"buffer_store_dwordx4 v[224:227], v13, s[36:39], s89 offen offset:0 sc1\n"
"buffer_store_dwordx4 v[228:231], v13, s[36:39], s89 offen offset:1024 sc1\n"
"buffer_store_dwordx4 v[240:243], v13, s[36:39], s90 offen offset:0 sc1\n"
"buffer_store_dwordx4 v[244:247], v13, s[36:39], s90 offen offset:1024 sc1\n"
"s_waitcnt vmcnt(6)\n"
"v_lshlrev_b32 v32, 16, v168\n"
"v_and_b32 v33, 0xffff0000, v168\n"
"v_lshlrev_b32 v34, 16, v169\n"
"v_and_b32 v35, 0xffff0000, v169\n"
"v_lshlrev_b32 v36, 16, v170\n"
"v_and_b32 v37, 0xffff0000, v170\n"
"v_lshlrev_b32 v38, 16, v171\n"
"v_and_b32 v39, 0xffff0000, v171\n"
"v_lshlrev_b32 v40, 16, v172\n"
"v_and_b32 v41, 0xffff0000, v172\n"
"v_lshlrev_b32 v42, 16, v173\n"
"v_and_b32 v43, 0xffff0000, v173\n"
"v_lshlrev_b32 v44, 16, v174\n"
"v_and_b32 v45, 0xffff0000, v174\n"
"v_lshlrev_b32 v46, 16, v175\n"
"v_and_b32 v47, 0xffff0000, v175\n"
"v_lshlrev_b32 v2, 16, v144\n"
"v_and_b32 v3, 0xffff0000, v144\n"
"v_lshlrev_b32 v4, 16, v145\n"
"v_and_b32 v5, 0xffff0000, v145\n"
"v_pk_add_f32 v[128:129], v[128:129], v[2:3]\n"
"v_pk_add_f32 v[130:131], v[130:131], v[4:5]\n"
"v_lshlrev_b32 v2, 16, v146\n"
"v_and_b32 v3, 0xffff0000, v146\n"
"v_lshlrev_b32 v4, 16, v147\n"
"v_and_b32 v5, 0xffff0000, v147\n"
"v_pk_add_f32 v[132:133], v[132:133], v[2:3]\n"
"v_pk_add_f32 v[134:135], v[134:135], v[4:5]\n"
"v_lshlrev_b32 v2, 16, v148\n"
"v_and_b32 v3, 0xffff0000, v148\n"
"v_lshlrev_b32 v4, 16, v149\n"
"v_and_b32 v5, 0xffff0000, v149\n"
"v_pk_add_f32 v[136:137], v[136:137], v[2:3]\n"
"v_pk_add_f32 v[138:139], v[138:139], v[4:5]\n"
"v_lshlrev_b32 v2, 16, v150\n"
"v_and_b32 v3, 0xffff0000, v150\n"
"v_lshlrev_b32 v4, 16, v151\n"
"v_and_b32 v5, 0xffff0000, v151\n"
"v_pk_add_f32 v[140:141], v[140:141], v[2:3]\n"
"v_pk_add_f32 v[142:143], v[142:143], v[4:5]\n"
"v_lshlrev_b32 v2, 16, v152\n"
"v_and_b32 v3, 0xffff0000, v152\n"
"v_lshlrev_b32 v4, 16, v153\n"
"v_and_b32 v5, 0xffff0000, v153\n"
"v_pk_add_f32 v[128:129], v[128:129], v[2:3]\n"
"v_pk_add_f32 v[130:131], v[130:131], v[4:5]\n"
"v_lshlrev_b32 v2, 16, v154\n"
"v_and_b32 v3, 0xffff0000, v154\n"
"v_lshlrev_b32 v4, 16, v155\n"
"v_and_b32 v5, 0xffff0000, v155\n"
"v_pk_add_f32 v[132:133], v[132:133], v[2:3]\n"
"v_pk_add_f32 v[134:135], v[134:135], v[4:5]\n"
"v_lshlrev_b32 v2, 16, v156\n"
"v_and_b32 v3, 0xffff0000, v156\n"
"v_lshlrev_b32 v4, 16, v157\n"
"v_and_b32 v5, 0xffff0000, v157\n"
"v_pk_add_f32 v[136:137], v[136:137], v[2:3]\n"
"v_pk_add_f32 v[138:139], v[138:139], v[4:5]\n"
"v_lshlrev_b32 v2, 16, v158\n"
"v_and_b32 v3, 0xffff0000, v158\n"
"v_lshlrev_b32 v4, 16, v159\n"
"v_and_b32 v5, 0xffff0000, v159\n"
"v_pk_add_f32 v[140:141], v[140:141], v[2:3]\n"
"v_pk_add_f32 v[142:143], v[142:143], v[4:5]\n"
"v_lshlrev_b32 v2, 16, v160\n"
"v_and_b32 v3, 0xffff0000, v160\n"
"v_lshlrev_b32 v4, 16, v161\n"
"v_and_b32 v5, 0xffff0000, v161\n"
"v_pk_add_f32 v[128:129], v[128:129], v[2:3]\n"
"v_pk_add_f32 v[130:131], v[130:131], v[4:5]\n"
"v_lshlrev_b32 v2, 16, v162\n"
"v_and_b32 v3, 0xffff0000, v162\n"
"v_lshlrev_b32 v4, 16, v163\n"
"v_and_b32 v5, 0xffff0000, v163\n"
"v_pk_add_f32 v[132:133], v[132:133], v[2:3]\n"
"v_pk_add_f32 v[134:135], v[134:135], v[4:5]\n"
"v_lshlrev_b32 v2, 16, v164\n"
"v_and_b32 v3, 0xffff0000, v164\n"
"v_lshlrev_b32 v4, 16, v165\n"
"v_and_b32 v5, 0xffff0000, v165\n"
"v_pk_add_f32 v[136:137], v[136:137], v[2:3]\n"
"v_pk_add_f32 v[138:139], v[138:139], v[4:5]\n"
"v_lshlrev_b32 v2, 16, v166\n"
"v_and_b32 v3, 0xffff0000, v166\n"
"v_lshlrev_b32 v4, 16, v167\n"
"v_and_b32 v5, 0xffff0000, v167\n"
"v_pk_add_f32 v[140:141], v[140:141], v[2:3]\n"
"v_pk_add_f32 v[142:143], v[142:143], v[4:5]\n"
"s_mov_b32 s52, 0xbfb8aa3b\n"
"s_mov_b32 s53, 0xbfb8aa3b\n"
"v_mov_b32 v6, 1.0\n"
"v_mov_b32 v7, 1.0\n"
"v_pk_add_f32 v[128:129], v[128:129], v[32:33]\n"
"v_pk_add_f32 v[130:131], v[130:131], v[34:35]\n"
"v_pk_add_f32 v[132:133], v[132:133], v[36:37]\n"
"v_pk_add_f32 v[134:135], v[134:135], v[38:39]\n"
"v_pk_add_f32 v[136:137], v[136:137], v[40:41]\n"
"v_pk_add_f32 v[138:139], v[138:139], v[42:43]\n"
"v_pk_add_f32 v[140:141], v[140:141], v[44:45]\n"
"v_pk_add_f32 v[142:143], v[142:143], v[46:47]\n"
"v_pk_mul_f32 v[48:49], v[128:129], s[52:53]\n"
"v_pk_mul_f32 v[50:51], v[130:131], s[52:53]\n"
"v_pk_mul_f32 v[52:53], v[132:133], s[52:53]\n"
"v_pk_mul_f32 v[54:55], v[134:135], s[52:53]\n"
"v_pk_mul_f32 v[56:57], v[136:137], s[52:53]\n"
"v_pk_mul_f32 v[58:59], v[138:139], s[52:53]\n"
"v_pk_mul_f32 v[60:61], v[140:141], s[52:53]\n"
"v_pk_mul_f32 v[62:63], v[142:143], s[52:53]\n"
"v_exp_f32 v48, v48\n"
"v_exp_f32 v49, v49\n"
"v_exp_f32 v50, v50\n"
"v_exp_f32 v51, v51\n"
"v_exp_f32 v52, v52\n"
"v_exp_f32 v53, v53\n"
"v_exp_f32 v54, v54\n"
"v_exp_f32 v55, v55\n"
"v_exp_f32 v56, v56\n"
"v_exp_f32 v57, v57\n"
"v_exp_f32 v58, v58\n"
"v_exp_f32 v59, v59\n"
"v_exp_f32 v60, v60\n"
"v_exp_f32 v61, v61\n"
"v_exp_f32 v62, v62\n"
"v_exp_f32 v63, v63\n"
"v_pk_add_f32 v[48:49], v[48:49], v[6:7]\n"
"v_pk_add_f32 v[50:51], v[50:51], v[6:7]\n"
"v_pk_add_f32 v[52:53], v[52:53], v[6:7]\n"
"v_pk_add_f32 v[54:55], v[54:55], v[6:7]\n"
"v_pk_add_f32 v[56:57], v[56:57], v[6:7]\n"
"v_pk_add_f32 v[58:59], v[58:59], v[6:7]\n"
"v_pk_add_f32 v[60:61], v[60:61], v[6:7]\n"
"v_pk_add_f32 v[62:63], v[62:63], v[6:7]\n"
"v_rcp_f32 v48, v48\n"
"v_rcp_f32 v49, v49\n"
"v_rcp_f32 v50, v50\n"
"v_rcp_f32 v51, v51\n"
"v_rcp_f32 v52, v52\n"
"v_rcp_f32 v53, v53\n"
"v_rcp_f32 v54, v54\n"
"v_rcp_f32 v55, v55\n"
"v_rcp_f32 v56, v56\n"
"v_rcp_f32 v57, v57\n"
"v_rcp_f32 v58, v58\n"
"v_rcp_f32 v59, v59\n"
"v_rcp_f32 v60, v60\n"
"v_rcp_f32 v61, v61\n"
"v_rcp_f32 v62, v62\n"
"v_rcp_f32 v63, v63\n"
"v_pk_mul_f32 v[128:129], v[128:129], v[48:49]\n"
"v_pk_mul_f32 v[130:131], v[130:131], v[50:51]\n"
"v_pk_mul_f32 v[132:133], v[132:133], v[52:53]\n"
"v_pk_mul_f32 v[134:135], v[134:135], v[54:55]\n"
"v_pk_mul_f32 v[136:137], v[136:137], v[56:57]\n"
"v_pk_mul_f32 v[138:139], v[138:139], v[58:59]\n"
"v_pk_mul_f32 v[140:141], v[140:141], v[60:61]\n"
"v_pk_mul_f32 v[142:143], v[142:143], v[62:63]\n"
"s_waitcnt vmcnt(0)\n"
"s_lshl_b32 s50, s63, 1\n"
"s_add_u32 s50, s50, 1\n"
"s_lshl_b32 s50, s50, 3\n"
"s_add_u32 s50, s50, s21\n"
"s_lshl_b32 s50, s50, 7\n"
"s_add_u32 s50, s50, 4096\n"
"s_lshl_b32 s51, s62, 2\n"
"s_add_u32 s50, s50, s51\n"
"v_mov_b32 v2, s50\n"
"v_mov_b32 v3, s61\n"
"s_mov_b64 exec, 1\n"
"global_store_dword v2, v3, s[6:7] sc1\n"
"s_mov_b64 exec, -1\n"
"s_lshl_b32 s50, s63, 1\n"
"s_add_u32 s50, s50, 1\n"
"s_lshl_b32 s50, s50, 3\n"
"s_add_u32 s50, s50, s21\n"
"s_lshl_b32 s50, s50, 7\n"
"s_add_u32 s50, s50, 4096\n"
"v_and_b32 v2, 3, v1\n"
"v_lshlrev_b32 v2, 2, v2\n"
"v_add_u32 v2, s50, v2\n"
"s_mov_b32 s52, 0\n"
"P2_ps:\n"
"global_load_dword v3, v2, s[6:7] sc1\n"
"s_waitcnt vmcnt(0)\n"
"v_cmp_ne_u32 vcc, s61, v3\n"
"s_nop 1\n"
"s_cmp_eq_u64 vcc, 0\n"
"s_cbranch_scc1 P2_ps_ok\n"
"s_add_u32 s52, s52, 1\n"
"s_cmp_lt_u32 s52, 0x8000\n"
"s_cbranch_scc0 P2_ps_ok\n"
"s_branch P2_ps\n"
"P2_ps_ok:\n"
"s_lshl_b32 s50, s63, 1\n"
"s_add_u32 s50, s50, 1\n"
"s_lshl_b32 s50, s50, 2\n"
"s_add_u32 s50, s50, s62\n"
"s_lshl_b32 s50, s50, 16\n"
"s_lshl_b32 s51, s21, 11\n"
"s_add_u32 s50, s50, s51\n"
"s_add_u32 s40, s96, s50\n"
"s_addc_u32 s41, s97, 0\n"
"s_and_b32 s41, s41, 0xffff\n"
"s_mov_b32 s42, 51200\n"
"s_mov_b32 s43, 0x20000\n"
"v_lshlrev_b32 v13, 4, v1\n"
"s_add_u32 s50, s62, 1\n"
"s_and_b32 s50, s50, 3\n"
"s_lshl_b32 s50, s50, 14\n"
"buffer_load_dwordx4 v[64:67], v13, s[40:43], s50 offen offset:0 sc1\n"
"buffer_load_dwordx4 v[68:71], v13, s[40:43], s50 offen offset:1024 sc1\n"
"s_add_u32 s50, s62, 2\n"
"s_and_b32 s50, s50, 3\n"
"s_lshl_b32 s50, s50, 14\n"
"buffer_load_dwordx4 v[72:75], v13, s[40:43], s50 offen offset:0 sc1\n"
"buffer_load_dwordx4 v[76:79], v13, s[40:43], s50 offen offset:1024 sc1\n"
"s_add_u32 s50, s62, 3\n"
"s_and_b32 s50, s50, 3\n"
"s_lshl_b32 s50, s50, 14\n"
"buffer_load_dwordx4 v[80:83], v13, s[40:43], s50 offen offset:0 sc1\n"
"buffer_load_dwordx4 v[84:87], v13, s[40:43], s50 offen offset:1024 sc1\n"
"s_mov_b32 s36, s16\n"
"s_and_b32 s37, s17, 0xffff\n"
"s_mov_b32 s38, 4194304\n"
"s_mov_b32 s39, 0x20000\n"
"s_waitcnt vmcnt(0)\n"
"v_lshlrev_b32 v2, 16, v64\n"
"v_and_b32 v3, 0xffff0000, v64\n"
"v_lshlrev_b32 v4, 16, v65\n"
"v_and_b32 v5, 0xffff0000, v65\n"
"v_pk_add_f32 v[192:193], v[192:193], v[2:3]\n"
"v_pk_add_f32 v[194:195], v[194:195], v[4:5]\n"
"v_lshlrev_b32 v2, 16, v66\n"
"v_and_b32 v3, 0xffff0000, v66\n"
"v_lshlrev_b32 v4, 16, v67\n"
"v_and_b32 v5, 0xffff0000, v67\n"
"v_pk_add_f32 v[196:197], v[196:197], v[2:3]\n"
"v_pk_add_f32 v[198:199], v[198:199], v[4:5]\n"
"v_lshlrev_b32 v2, 16, v68\n"
"v_and_b32 v3, 0xffff0000, v68\n"
"v_lshlrev_b32 v4, 16, v69\n"
"v_and_b32 v5, 0xffff0000, v69\n"
"v_pk_add_f32 v[200:201], v[200:201], v[2:3]\n"
"v_pk_add_f32 v[202:203], v[202:203], v[4:5]\n"
"v_lshlrev_b32 v2, 16, v70\n"
"v_and_b32 v3, 0xffff0000, v70\n"
"v_lshlrev_b32 v4, 16, v71\n"
"v_and_b32 v5, 0xffff0000, v71\n"
"v_pk_add_f32 v[204:205], v[204:205], v[2:3]\n"
"v_pk_add_f32 v[206:207], v[206:207], v[4:5]\n"
"v_lshlrev_b32 v2, 16, v72\n"
"v_and_b32 v3, 0xffff0000, v72\n"
"v_lshlrev_b32 v4, 16, v73\n"
"v_and_b32 v5, 0xffff0000, v73\n"
"v_pk_add_f32 v[192:193], v[192:193], v[2:3]\n"
"v_pk_add_f32 v[194:195], v[194:195], v[4:5]\n"
"v_lshlrev_b32 v2, 16, v74\n"
"v_and_b32 v3, 0xffff0000, v74\n"
"v_lshlrev_b32 v4, 16, v75\n"
"v_and_b32 v5, 0xffff0000, v75\n"
"v_pk_add_f32 v[196:197], v[196:197], v[2:3]\n"
"v_pk_add_f32 v[198:199], v[198:199], v[4:5]\n"
"v_lshlrev_b32 v2, 16, v76\n"
"v_and_b32 v3, 0xffff0000, v76\n"
"v_lshlrev_b32 v4, 16, v77\n"
"v_and_b32 v5, 0xffff0000, v77\n"
"v_pk_add_f32 v[200:201], v[200:201], v[2:3]\n"
"v_pk_add_f32 v[202:203], v[202:203], v[4:5]\n"
"v_lshlrev_b32 v2, 16, v78\n"
"v_and_b32 v3, 0xffff0000, v78\n"
"v_lshlrev_b32 v4, 16, v79\n"
"v_and_b32 v5, 0xffff0000, v79\n"
"v_pk_add_f32 v[204:205], v[204:205], v[2:3]\n"
"v_pk_add_f32 v[206:207], v[206:207], v[4:5]\n"
"v_lshlrev_b32 v2, 16, v80\n"
"v_and_b32 v3, 0xffff0000, v80\n"
"v_lshlrev_b32 v4, 16, v81\n"
"v_and_b32 v5, 0xffff0000, v81\n"
"v_pk_add_f32 v[192:193], v[192:193], v[2:3]\n"
"v_pk_add_f32 v[194:195], v[194:195], v[4:5]\n"
"v_lshlrev_b32 v2, 16, v82\n"
"v_and_b32 v3, 0xffff0000, v82\n"
"v_lshlrev_b32 v4, 16, v83\n"
"v_and_b32 v5, 0xffff0000, v83\n"
"v_pk_add_f32 v[196:197], v[196:197], v[2:3]\n"
"v_pk_add_f32 v[198:199], v[198:199], v[4:5]\n"
"v_lshlrev_b32 v2, 16, v84\n"
"v_and_b32 v3, 0xffff0000, v84\n"
"v_lshlrev_b32 v4, 16, v85\n"
"v_and_b32 v5, 0xffff0000, v85\n"
"v_pk_add_f32 v[200:201], v[200:201], v[2:3]\n"
"v_pk_add_f32 v[202:203], v[202:203], v[4:5]\n"
"v_lshlrev_b32 v2, 16, v86\n"
"v_and_b32 v3, 0xffff0000, v86\n"
"v_lshlrev_b32 v4, 16, v87\n"
"v_and_b32 v5, 0xffff0000, v87\n"
"v_pk_add_f32 v[204:205], v[204:205], v[2:3]\n"
"v_pk_add_f32 v[206:207], v[206:207], v[4:5]\n"
"v_pk_mul_f32 v[192:193], v[192:193], v[128:129]\n"
"v_pk_mul_f32 v[194:195], v[194:195], v[130:131]\n"
"v_pk_mul_f32 v[196:197], v[196:197], v[132:133]\n"
"v_pk_mul_f32 v[198:199], v[198:199], v[134:135]\n"
"v_pk_mul_f32 v[200:201], v[200:201], v[136:137]\n"
"v_pk_mul_f32 v[202:203], v[202:203], v[138:139]\n"
"v_pk_mul_f32 v[204:205], v[204:205], v[140:141]\n"
"v_pk_mul_f32 v[206:207], v[206:207], v[142:143]\n"
"v_cvt_pk_bf16_f32 v64, v192, v193\n"
"v_cvt_pk_bf16_f32 v65, v194, v195\n"
"v_cvt_pk_bf16_f32 v66, v196, v197\n"
"v_cvt_pk_bf16_f32 v67, v198, v199\n"
"s_nop 1\n"
"v_permlane16_swap_b32 v64, v66\n"
"v_permlane16_swap_b32 v65, v67\n"
"s_nop 0\n"
"v_lshlrev_b32 v2, 16, v64\n"
"v_and_b32 v3, 0xffff0000, v64\n"
"v_lshlrev_b32 v4, 16, v176\n"
"v_and_b32 v5, 0xffff0000, v176\n"
"v_pk_add_f32 v[2:3], v[2:3], v[4:5]\n"
"v_cvt_pk_bf16_f32 v72, v2, v3\n"
"v_lshlrev_b32 v2, 16, v65\n"
"v_and_b32 v3, 0xffff0000, v65\n"
"v_lshlrev_b32 v4, 16, v177\n"
"v_and_b32 v5, 0xffff0000, v177\n"
"v_pk_add_f32 v[2:3], v[2:3], v[4:5]\n"
"v_cvt_pk_bf16_f32 v73, v2, v3\n"
"v_lshlrev_b32 v2, 16, v66\n"
"v_and_b32 v3, 0xffff0000, v66\n"
"v_lshlrev_b32 v4, 16, v178\n"
"v_and_b32 v5, 0xffff0000, v178\n"
"v_pk_add_f32 v[2:3], v[2:3], v[4:5]\n"
"v_cvt_pk_bf16_f32 v74, v2, v3\n"
"v_lshlrev_b32 v2, 16, v67\n"
"v_and_b32 v3, 0xffff0000, v67\n"
"v_lshlrev_b32 v4, 16, v179\n"
"v_and_b32 v5, 0xffff0000, v179\n"
"v_pk_add_f32 v[2:3], v[2:3], v[4:5]\n"
"v_cvt_pk_bf16_f32 v75, v2, v3\n"
"buffer_store_dwordx4 v[72:75], v12, s[36:39], 0 offen offset:0\n"
"v_cvt_pk_bf16_f32 v68, v200, v201\n"
"v_cvt_pk_bf16_f32 v69, v202, v203\n"
"v_cvt_pk_bf16_f32 v70, v204, v205\n"
"v_cvt_pk_bf16_f32 v71, v206, v207\n"
"s_nop 1\n"
"v_permlane16_swap_b32 v68, v70\n"
"v_permlane16_swap_b32 v69, v71\n"
"s_nop 0\n"
"v_lshlrev_b32 v2, 16, v68\n"
"v_and_b32 v3, 0xffff0000, v68\n"
"v_lshlrev_b32 v4, 16, v180\n"
"v_and_b32 v5, 0xffff0000, v180\n"
"v_pk_add_f32 v[2:3], v[2:3], v[4:5]\n"
"v_cvt_pk_bf16_f32 v76, v2, v3\n"
"v_lshlrev_b32 v2, 16, v69\n"
"v_and_b32 v3, 0xffff0000, v69\n"
"v_lshlrev_b32 v4, 16, v181\n"
"v_and_b32 v5, 0xffff0000, v181\n"
"v_pk_add_f32 v[2:3], v[2:3], v[4:5]\n"
"v_cvt_pk_bf16_f32 v77, v2, v3\n"
"v_lshlrev_b32 v2, 16, v70\n"
"v_and_b32 v3, 0xffff0000, v70\n"
"v_lshlrev_b32 v4, 16, v182\n"
"v_and_b32 v5, 0xffff0000, v182\n"
"v_pk_add_f32 v[2:3], v[2:3], v[4:5]\n"
"v_cvt_pk_bf16_f32 v78, v2, v3\n"
"v_lshlrev_b32 v2, 16, v71\n"
"v_and_b32 v3, 0xffff0000, v71\n"
"v_lshlrev_b32 v4, 16, v183\n"
"v_and_b32 v5, 0xffff0000, v183\n"
"v_pk_add_f32 v[2:3], v[2:3], v[4:5]\n"
"v_cvt_pk_bf16_f32 v79, v2, v3\n"
"buffer_store_dwordx4 v[76:79], v12, s[36:39], 0 offen offset:64\n"
"s_branch FZ_end\n"
"AT_slow:\n"
"v_mov_b32 v192, 0\n"
"v_mov_b32 v193, 0\n"
"v_mov_b32 v194, 0\n"
"v_mov_b32 v195, 0\n"
"v_mov_b32 v196, 0\n"
"v_mov_b32 v197, 0\n"
"v_mov_b32 v198, 0\n"
"v_mov_b32 v199, 0\n"
"v_mov_b32 v200, 0\n"
"v_mov_b32 v201, 0\n"
"v_mov_b32 v202, 0\n"
"v_mov_b32 v203, 0\n"
"v_mov_b32 v204, 0\n"
"v_mov_b32 v205, 0\n"
"v_mov_b32 v206, 0\n"
"v_mov_b32 v207, 0\n"
"v_mov_b32 v208, 0\n"
"v_mov_b32 v209, 0\n"
"v_mov_b32 v210, 0\n"
"v_mov_b32 v211, 0\n"
"v_mov_b32 v212, 0\n"
"v_mov_b32 v213, 0\n"
"v_mov_b32 v214, 0\n"
"v_mov_b32 v215, 0\n"
"v_mov_b32 v216, 0\n"
"v_mov_b32 v217, 0\n"
"v_mov_b32 v218, 0\n"
"v_mov_b32 v219, 0\n"
"v_mov_b32 v220, 0\n"
"v_mov_b32 v221, 0\n"
"v_mov_b32 v222, 0\n"
"v_mov_b32 v223, 0\n"
"v_mov_b32 v224, 0\n"
"v_mov_b32 v225, 0\n"
"v_mov_b32 v226, 0\n"
"v_mov_b32 v227, 0\n"
"v_mov_b32 v228, 0\n"
"v_mov_b32 v229, 0\n"
"v_mov_b32 v230, 0\n"
"v_mov_b32 v231, 0\n"
"v_mov_b32 v232, 0\n"
"v_mov_b32 v233, 0\n"
"v_mov_b32 v234, 0\n"
"v_mov_b32 v235, 0\n"
"v_mov_b32 v236, 0\n"
"v_mov_b32 v237, 0\n"
"v_mov_b32 v238, 0\n"
"v_mov_b32 v239, 0\n"
"v_mov_b32 v240, 0\n"
"v_mov_b32 v241, 0\n"
"v_mov_b32 v242, 0\n"
"v_mov_b32 v243, 0\n"
"v_mov_b32 v244, 0\n"
"v_mov_b32 v245, 0\n"
"v_mov_b32 v246, 0\n"
"v_mov_b32 v247, 0\n"
"v_mov_b32 v248, 0\n"
"v_mov_b32 v249, 0\n"
"v_mov_b32 v250, 0\n"
"v_mov_b32 v251, 0\n"
"v_mov_b32 v252, 0\n"
"v_mov_b32 v253, 0\n"
"v_mov_b32 v254, 0\n"
"v_mov_b32 v255, 0\n"
"v_mov_b32 v112, 0\n"
"v_mov_b32 v113, 0\n"
"v_mov_b32 v114, 0\n"
"v_mov_b32 v115, 0\n"
"v_mov_b32 v116, 0\n"
"v_mov_b32 v117, 0\n"
"v_mov_b32 v118, 0\n"
"v_mov_b32 v119, 0\n"
"v_mov_b32 v2, 0xff800000\n"
"v_mov_b32 v3, 0xff800000\n"
"v_mov_b32 v24, v2\n"
"v_mov_b32 v25, v3\n"
"v_and_b32 v2, 15, v1\n"
"v_lshrrev_b32 v3, 4, v1\n"
"v_and_b32 v4, 1, v3\n"
"v_lshlrev_b32 v4, 1, v4\n"
"v_lshrrev_b32 v5, 1, v3\n"
"v_or_b32 v4, v4, v5\n"
"v_xor_b32 v4, v4, v2\n"
"v_lshlrev_b32 v4, 4, v4\n"
"v_lshlrev_b32 v5, 8, v2\n"
"v_add_u32 v16, v4, v5\n"
"v_xor_b32 v17, 64, v16\n"
"v_xor_b32 v18, 128, v16\n"
"v_xor_b32 v19, 192, v16\n"
"v_lshrrev_b32 v4, 2, v2\n"
"v_and_b32 v5, 3, v1\n"
"v_lshlrev_b32 v6, 2, v3\n"
"v_add_u32 v6, v6, v4\n"
"v_lshlrev_b32 v6, 7, v6\n"
"v_and_b32 v3, 1, v3\n"
"v_lshlrev_b32 v3, 1, v3\n"
"v_lshrrev_b32 v4, 1, v4\n"
"v_add_u32 v3, v3, v4\n"
"v_lshlrev_b32 v3, 1, v3\n"
"v_lshrrev_b32 v4, 1, v5\n"
"v_xor_b32 v3, v3, v4\n"
"v_lshlrev_b32 v3, 4, v3\n"
"v_and_b32 v5, 1, v5\n"
"v_lshlrev_b32 v5, 3, v5\n"
"v_add_u32 v6, v6, v3\n"
"v_add_u32 v6, v6, v5\n"
"v_add_u32 v20, 16384, v6\n"
"v_xor_b32 v21, 32, v20\n"
"v_xor_b32 v22, 64, v20\n"
"v_xor_b32 v23, 96, v20\n"
"v_mov_b32 v120, 0x3f803f80\n"
"v_mov_b32 v121, 0x3f803f80\n"
"v_mov_b32 v122, 0x3f803f80\n"
"v_mov_b32 v123, 0x3f803f80\n"
"v_mov_b32 v2, v24\n"
"v_mov_b32 v3, v25\n"
"s_mov_b32 s47, 0\n"
"s_mov_b32 s48, 0\n"
"AT_slow_loop:\n"
"s_add_u32 m0, s60, 0\n"
"s_nop 0\n"
"buffer_load_dwordx4 v124, s[36:39], s48 offen lds\n"
"s_add_u32 m0, s60, 1024\n"
"s_nop 0\n"
"buffer_load_dwordx4 v125, s[36:39], s48 offen lds\n"
"s_add_u32 m0, s60, 2048\n"
"s_nop 0\n"
"buffer_load_dwordx4 v126, s[36:39], s48 offen lds\n"
"s_add_u32 m0, s60, 3072\n"
"s_nop 0\n"
"buffer_load_dwordx4 v127, s[36:39], s48 offen lds\n"
"s_waitcnt vmcnt(0)\n"
"s_barrier\n"
"ds_read_b128 v[160:163], v16 offset:0\n"
"ds_read_b128 v[164:167], v17 offset:0\n"
"ds_read_b128 v[168:171], v18 offset:0\n"
"ds_read_b128 v[172:175], v19 offset:0\n"
"s_waitcnt lgkmcnt(0)\n"
"v_mfma_f32_16x16x32_bf16 v[64:67], v[160:163], v[32:35], 0\n"
"v_mfma_f32_16x16x32_bf16 v[64:67], v[164:167], v[36:39], v[64:67]\n"
"v_mfma_f32_16x16x32_bf16 v[64:67], v[168:171], v[40:43], v[64:67]\n"
"v_mfma_f32_16x16x32_bf16 v[64:67], v[172:175], v[44:47], v[64:67]\n"
"v_mfma_f32_16x16x32_bf16 v[80:83], v[160:163], v[48:51], 0\n"
"v_mfma_f32_16x16x32_bf16 v[80:83], v[164:167], v[52:55], v[80:83]\n"
"v_mfma_f32_16x16x32_bf16 v[80:83], v[168:171], v[56:59], v[80:83]\n"
"v_mfma_f32_16x16x32_bf16 v[80:83], v[172:175], v[60:63], v[80:83]\n"
"ds_read_b128 v[160:163], v16 offset:4096\n"
"ds_read_b128 v[164:167], v17 offset:4096\n"
"ds_read_b128 v[168:171], v18 offset:4096\n"
"ds_read_b128 v[172:175], v19 offset:4096\n"
"s_waitcnt lgkmcnt(0)\n"
"v_mfma_f32_16x16x32_bf16 v[68:71], v[160:163], v[32:35], 0\n"
"v_mfma_f32_16x16x32_bf16 v[68:71], v[164:167], v[36:39], v[68:71]\n"
"v_mfma_f32_16x16x32_bf16 v[68:71], v[168:171], v[40:43], v[68:71]\n"
"v_mfma_f32_16x16x32_bf16 v[68:71], v[172:175], v[44:47], v[68:71]\n"
"v_mfma_f32_16x16x32_bf16 v[84:87], v[160:163], v[48:51], 0\n"
"v_mfma_f32_16x16x32_bf16 v[84:87], v[164:167], v[52:55], v[84:87]\n"
"v_mfma_f32_16x16x32_bf16 v[84:87], v[168:171], v[56:59], v[84:87]\n"
"v_mfma_f32_16x16x32_bf16 v[84:87], v[172:175], v[60:63], v[84:87]\n"
"ds_read_b128 v[160:163], v16 offset:8192\n"
"ds_read_b128 v[164:167], v17 offset:8192\n"
"ds_read_b128 v[168:171], v18 offset:8192\n"
"ds_read_b128 v[172:175], v19 offset:8192\n"
"s_waitcnt lgkmcnt(0)\n"
"v_mfma_f32_16x16x32_bf16 v[72:75], v[160:163], v[32:35], 0\n"
"v_mfma_f32_16x16x32_bf16 v[72:75], v[164:167], v[36:39], v[72:75]\n"
"v_mfma_f32_16x16x32_bf16 v[72:75], v[168:171], v[40:43], v[72:75]\n"
"v_mfma_f32_16x16x32_bf16 v[72:75], v[172:175], v[44:47], v[72:75]\n"
"v_mfma_f32_16x16x32_bf16 v[88:91], v[160:163], v[48:51], 0\n"
"v_mfma_f32_16x16x32_bf16 v[88:91], v[164:167], v[52:55], v[88:91]\n"
"v_mfma_f32_16x16x32_bf16 v[88:91], v[168:171], v[56:59], v[88:91]\n"
"v_mfma_f32_16x16x32_bf16 v[88:91], v[172:175], v[60:63], v[88:91]\n"
"ds_read_b128 v[160:163], v16 offset:12288\n"
"ds_read_b128 v[164:167], v17 offset:12288\n"
"ds_read_b128 v[168:171], v18 offset:12288\n"
"ds_read_b128 v[172:175], v19 offset:12288\n"
"s_waitcnt lgkmcnt(0)\n"
"v_mfma_f32_16x16x32_bf16 v[76:79], v[160:163], v[32:35], 0\n"
"v_mfma_f32_16x16x32_bf16 v[76:79], v[164:167], v[36:39], v[76:79]\n"
"v_mfma_f32_16x16x32_bf16 v[76:79], v[168:171], v[40:43], v[76:79]\n"
"v_mfma_f32_16x16x32_bf16 v[76:79], v[172:175], v[44:47], v[76:79]\n"
"v_mfma_f32_16x16x32_bf16 v[92:95], v[160:163], v[48:51], 0\n"
"v_mfma_f32_16x16x32_bf16 v[92:95], v[164:167], v[52:55], v[92:95]\n"
"v_mfma_f32_16x16x32_bf16 v[92:95], v[168:171], v[56:59], v[92:95]\n"
"v_mfma_f32_16x16x32_bf16 v[92:95], v[172:175], v[60:63], v[92:95]\n"
"s_nop 7\n"
"s_nop 7\n"
"v_max3_f32 v4, v64, v65, v66\n"
"v_max3_f32 v4, v4, v67, v68\n"
"v_max3_f32 v4, v4, v69, v70\n"
"v_max3_f32 v4, v4, v71, v72\n"
"v_max3_f32 v4, v4, v73, v74\n"
"v_max3_f32 v4, v4, v75, v76\n"
"v_max3_f32 v4, v4, v77, v78\n"
"v_max_f32 v4, v4, v79\n"
"v_mov_b32 v5, v4\n"
"s_nop 1\n"
"v_permlane32_swap_b32 v4, v5\n"
"s_nop 1\n"
"v_max_f32 v4, v4, v5\n"
"v_mov_b32 v5, v4\n"
"s_nop 1\n"
"v_permlane16_swap_b32 v4, v5\n"
"s_nop 1\n"
"v_max_f32 v4, v4, v5\n"
"v_max_f32 v4, v4, v2\n"
"v_sub_f32 v5, v2, v4\n"
"v_exp_f32 v5, v5\n"
"v_mov_b32 v2, v4\n"
"v_sub_f32 v64, v64, v4\n"
"v_sub_f32 v65, v65, v4\n"
"v_sub_f32 v66, v66, v4\n"
"v_sub_f32 v67, v67, v4\n"
"v_sub_f32 v68, v68, v4\n"
"v_sub_f32 v69, v69, v4\n"
"v_sub_f32 v70, v70, v4\n"
"v_sub_f32 v71, v71, v4\n"
"v_sub_f32 v72, v72, v4\n"
"v_sub_f32 v73, v73, v4\n"
"v_sub_f32 v74, v74, v4\n"
"v_sub_f32 v75, v75, v4\n"
"v_sub_f32 v76, v76, v4\n"
"v_sub_f32 v77, v77, v4\n"
"v_sub_f32 v78, v78, v4\n"
"v_sub_f32 v79, v79, v4\n"
"v_exp_f32 v64, v64\n"
"v_exp_f32 v65, v65\n"
"v_exp_f32 v66, v66\n"
"v_exp_f32 v67, v67\n"
"v_exp_f32 v68, v68\n"
"v_exp_f32 v69, v69\n"
"v_exp_f32 v70, v70\n"
"v_exp_f32 v71, v71\n"
"v_exp_f32 v72, v72\n"
"v_exp_f32 v73, v73\n"
"v_exp_f32 v74, v74\n"
"v_exp_f32 v75, v75\n"
"v_exp_f32 v76, v76\n"
"v_exp_f32 v77, v77\n"
"v_exp_f32 v78, v78\n"
"v_exp_f32 v79, v79\n"
"v_mul_f32 v192, v5, v192\n"
"v_mul_f32 v193, v5, v193\n"
"v_mul_f32 v194, v5, v194\n"
"v_mul_f32 v195, v5, v195\n"
"v_mul_f32 v200, v5, v200\n"
"v_mul_f32 v201, v5, v201\n"
"v_mul_f32 v202, v5, v202\n"
"v_mul_f32 v203, v5, v203\n"
"v_mul_f32 v208, v5, v208\n"
"v_mul_f32 v209, v5, v209\n"
"v_mul_f32 v210, v5, v210\n"
"v_mul_f32 v211, v5, v211\n"
"v_mul_f32 v216, v5, v216\n"
"v_mul_f32 v217, v5, v217\n"
"v_mul_f32 v218, v5, v218\n"
"v_mul_f32 v219, v5, v219\n"
"v_mul_f32 v224, v5, v224\n"
"v_mul_f32 v225, v5, v225\n"
"v_mul_f32 v226, v5, v226\n"
"v_mul_f32 v227, v5, v227\n"
"v_mul_f32 v232, v5, v232\n"
"v_mul_f32 v233, v5, v233\n"
"v_mul_f32 v234, v5, v234\n"
"v_mul_f32 v235, v5, v235\n"
"v_mul_f32 v240, v5, v240\n"
"v_mul_f32 v241, v5, v241\n"
"v_mul_f32 v242, v5, v242\n"
"v_mul_f32 v243, v5, v243\n"
"v_mul_f32 v248, v5, v248\n"
"v_mul_f32 v249, v5, v249\n"
"v_mul_f32 v250, v5, v250\n"
"v_mul_f32 v251, v5, v251\n"
"v_mul_f32 v112, v5, v112\n"
"v_mul_f32 v113, v5, v113\n"
"v_mul_f32 v114, v5, v114\n"
"v_mul_f32 v115, v5, v115\n"
"v_cvt_pk_bf16_f32 v96, v64, v65\n"
"v_cvt_pk_bf16_f32 v97, v66, v67\n"
"v_cvt_pk_bf16_f32 v98, v68, v69\n"
"v_cvt_pk_bf16_f32 v99, v70, v71\n"
"v_cvt_pk_bf16_f32 v100, v72, v73\n"
"v_cvt_pk_bf16_f32 v101, v74, v75\n"
"v_cvt_pk_bf16_f32 v102, v76, v77\n"
"v_cvt_pk_bf16_f32 v103, v78, v79\n"
"v_max3_f32 v4, v80, v81, v82\n"
"v_max3_f32 v4, v4, v83, v84\n"
"v_max3_f32 v4, v4, v85, v86\n"
"v_max3_f32 v4, v4, v87, v88\n"
"v_max3_f32 v4, v4, v89, v90\n"
"v_max3_f32 v4, v4, v91, v92\n"
"v_max3_f32 v4, v4, v93, v94\n"
"v_max_f32 v4, v4, v95\n"
"v_mov_b32 v5, v4\n"
"s_nop 1\n"
"v_permlane32_swap_b32 v4, v5\n"
"s_nop 1\n"
"v_max_f32 v4, v4, v5\n"
"v_mov_b32 v5, v4\n"
"s_nop 1\n"
"v_permlane16_swap_b32 v4, v5\n"
"s_nop 1\n"
"v_max_f32 v4, v4, v5\n"
"v_max_f32 v4, v4, v3\n"
"v_sub_f32 v5, v3, v4\n"
"v_exp_f32 v5, v5\n"
"v_mov_b32 v3, v4\n"
"v_sub_f32 v80, v80, v4\n"
"v_sub_f32 v81, v81, v4\n"
"v_sub_f32 v82, v82, v4\n"
"v_sub_f32 v83, v83, v4\n"
"v_sub_f32 v84, v84, v4\n"
"v_sub_f32 v85, v85, v4\n"
"v_sub_f32 v86, v86, v4\n"
"v_sub_f32 v87, v87, v4\n"
"v_sub_f32 v88, v88, v4\n"
"v_sub_f32 v89, v89, v4\n"
"v_sub_f32 v90, v90, v4\n"
"v_sub_f32 v91, v91, v4\n"
"v_sub_f32 v92, v92, v4\n"
"v_sub_f32 v93, v93, v4\n"
"v_sub_f32 v94, v94, v4\n"
"v_sub_f32 v95, v95, v4\n"
"v_exp_f32 v80, v80\n"
"v_exp_f32 v81, v81\n"
"v_exp_f32 v82, v82\n"
"v_exp_f32 v83, v83\n"
"v_exp_f32 v84, v84\n"
"v_exp_f32 v85, v85\n"
"v_exp_f32 v86, v86\n"
"v_exp_f32 v87, v87\n"
"v_exp_f32 v88, v88\n"
"v_exp_f32 v89, v89\n"
"v_exp_f32 v90, v90\n"
"v_exp_f32 v91, v91\n"
"v_exp_f32 v92, v92\n"
"v_exp_f32 v93, v93\n"
"v_exp_f32 v94, v94\n"
"v_exp_f32 v95, v95\n"
"v_mul_f32 v196, v5, v196\n"
"v_mul_f32 v197, v5, v197\n"
"v_mul_f32 v198, v5, v198\n"
"v_mul_f32 v199, v5, v199\n"
"v_mul_f32 v204, v5, v204\n"
"v_mul_f32 v205, v5, v205\n"
"v_mul_f32 v206, v5, v206\n"
"v_mul_f32 v207, v5, v207\n"
"v_mul_f32 v212, v5, v212\n"
"v_mul_f32 v213, v5, v213\n"
"v_mul_f32 v214, v5, v214\n"
"v_mul_f32 v215, v5, v215\n"
"v_mul_f32 v220, v5, v220\n"
"v_mul_f32 v221, v5, v221\n"
"v_mul_f32 v222, v5, v222\n"
"v_mul_f32 v223, v5, v223\n"
"v_mul_f32 v228, v5, v228\n"
"v_mul_f32 v229, v5, v229\n"
"v_mul_f32 v230, v5, v230\n"
"v_mul_f32 v231, v5, v231\n"
"v_mul_f32 v236, v5, v236\n"
"v_mul_f32 v237, v5, v237\n"
"v_mul_f32 v238, v5, v238\n"
"v_mul_f32 v239, v5, v239\n"
"v_mul_f32 v244, v5, v244\n"
"v_mul_f32 v245, v5, v245\n"
"v_mul_f32 v246, v5, v246\n"
"v_mul_f32 v247, v5, v247\n"
"v_mul_f32 v252, v5, v252\n"
"v_mul_f32 v253, v5, v253\n"
"v_mul_f32 v254, v5, v254\n"
"v_mul_f32 v255, v5, v255\n"
"v_mul_f32 v116, v5, v116\n"
"v_mul_f32 v117, v5, v117\n"
"v_mul_f32 v118, v5, v118\n"
"v_mul_f32 v119, v5, v119\n"
"v_cvt_pk_bf16_f32 v104, v80, v81\n"
"v_cvt_pk_bf16_f32 v105, v82, v83\n"
"v_cvt_pk_bf16_f32 v106, v84, v85\n"
"v_cvt_pk_bf16_f32 v107, v86, v87\n"
"v_cvt_pk_bf16_f32 v108, v88, v89\n"
"v_cvt_pk_bf16_f32 v109, v90, v91\n"
"v_cvt_pk_bf16_f32 v110, v92, v93\n"
"v_cvt_pk_bf16_f32 v111, v94, v95\n"
"s_nop 1\n"
"ds_read_b64_tr_b16 v[160:161], v20 offset:0\n"
"ds_read_b64_tr_b16 v[162:163], v20 offset:2048\n"
"ds_read_b64_tr_b16 v[164:165], v20 offset:4096\n"
"ds_read_b64_tr_b16 v[166:167], v20 offset:6144\n"
"s_waitcnt lgkmcnt(0)\n"
"v_mfma_f32_16x16x32_bf16 v[192:195], v[160:163], v[96:99], v[192:195]\n"
"v_mfma_f32_16x16x32_bf16 v[192:195], v[164:167], v[100:103], v[192:195]\n"
"v_mfma_f32_16x16x32_bf16 v[196:199], v[160:163], v[104:107], v[196:199]\n"
"v_mfma_f32_16x16x32_bf16 v[196:199], v[164:167], v[108:111], v[196:199]\n"
"ds_read_b64_tr_b16 v[160:161], v21 offset:0\n"
"ds_read_b64_tr_b16 v[162:163], v21 offset:2048\n"
"ds_read_b64_tr_b16 v[164:165], v21 offset:4096\n"
"ds_read_b64_tr_b16 v[166:167], v21 offset:6144\n"
"s_waitcnt lgkmcnt(0)\n"
"v_mfma_f32_16x16x32_bf16 v[200:203], v[160:163], v[96:99], v[200:203]\n"
"v_mfma_f32_16x16x32_bf16 v[200:203], v[164:167], v[100:103], v[200:203]\n"
"v_mfma_f32_16x16x32_bf16 v[204:207], v[160:163], v[104:107], v[204:207]\n"
"v_mfma_f32_16x16x32_bf16 v[204:207], v[164:167], v[108:111], v[204:207]\n"
"ds_read_b64_tr_b16 v[160:161], v22 offset:0\n"
"ds_read_b64_tr_b16 v[162:163], v22 offset:2048\n"
"ds_read_b64_tr_b16 v[164:165], v22 offset:4096\n"
"ds_read_b64_tr_b16 v[166:167], v22 offset:6144\n"
"s_waitcnt lgkmcnt(0)\n"
"v_mfma_f32_16x16x32_bf16 v[208:211], v[160:163], v[96:99], v[208:211]\n"
"v_mfma_f32_16x16x32_bf16 v[208:211], v[164:167], v[100:103], v[208:211]\n"
"v_mfma_f32_16x16x32_bf16 v[212:215], v[160:163], v[104:107], v[212:215]\n"
"v_mfma_f32_16x16x32_bf16 v[212:215], v[164:167], v[108:111], v[212:215]\n"
"ds_read_b64_tr_b16 v[160:161], v23 offset:0\n"
"ds_read_b64_tr_b16 v[162:163], v23 offset:2048\n"
"ds_read_b64_tr_b16 v[164:165], v23 offset:4096\n"
"ds_read_b64_tr_b16 v[166:167], v23 offset:6144\n"
"s_waitcnt lgkmcnt(0)\n"
"v_mfma_f32_16x16x32_bf16 v[216:219], v[160:163], v[96:99], v[216:219]\n"
"v_mfma_f32_16x16x32_bf16 v[216:219], v[164:167], v[100:103], v[216:219]\n"
"v_mfma_f32_16x16x32_bf16 v[220:223], v[160:163], v[104:107], v[220:223]\n"
"v_mfma_f32_16x16x32_bf16 v[220:223], v[164:167], v[108:111], v[220:223]\n"
"ds_read_b64_tr_b16 v[160:161], v20 offset:8192\n"
"ds_read_b64_tr_b16 v[162:163], v20 offset:10240\n"
"ds_read_b64_tr_b16 v[164:165], v20 offset:12288\n"
"ds_read_b64_tr_b16 v[166:167], v20 offset:14336\n"
"s_waitcnt lgkmcnt(0)\n"
"v_mfma_f32_16x16x32_bf16 v[224:227], v[160:163], v[96:99], v[224:227]\n"
"v_mfma_f32_16x16x32_bf16 v[224:227], v[164:167], v[100:103], v[224:227]\n"
"v_mfma_f32_16x16x32_bf16 v[228:231], v[160:163], v[104:107], v[228:231]\n"
"v_mfma_f32_16x16x32_bf16 v[228:231], v[164:167], v[108:111], v[228:231]\n"
"ds_read_b64_tr_b16 v[160:161], v21 offset:8192\n"
"ds_read_b64_tr_b16 v[162:163], v21 offset:10240\n"
"ds_read_b64_tr_b16 v[164:165], v21 offset:12288\n"
"ds_read_b64_tr_b16 v[166:167], v21 offset:14336\n"
"s_waitcnt lgkmcnt(0)\n"
"v_mfma_f32_16x16x32_bf16 v[232:235], v[160:163], v[96:99], v[232:235]\n"
"v_mfma_f32_16x16x32_bf16 v[232:235], v[164:167], v[100:103], v[232:235]\n"
"v_mfma_f32_16x16x32_bf16 v[236:239], v[160:163], v[104:107], v[236:239]\n"
"v_mfma_f32_16x16x32_bf16 v[236:239], v[164:167], v[108:111], v[236:239]\n"
"ds_read_b64_tr_b16 v[160:161], v22 offset:8192\n"
"ds_read_b64_tr_b16 v[162:163], v22 offset:10240\n"
"ds_read_b64_tr_b16 v[164:165], v22 offset:12288\n"
"ds_read_b64_tr_b16 v[166:167], v22 offset:14336\n"
"s_waitcnt lgkmcnt(0)\n"
"v_mfma_f32_16x16x32_bf16 v[240:243], v[160:163], v[96:99], v[240:243]\n"
"v_mfma_f32_16x16x32_bf16 v[240:243], v[164:167], v[100:103], v[240:243]\n"
"v_mfma_f32_16x16x32_bf16 v[244:247], v[160:163], v[104:107], v[244:247]\n"
"v_mfma_f32_16x16x32_bf16 v[244:247], v[164:167], v[108:111], v[244:247]\n"
"ds_read_b64_tr_b16 v[160:161], v23 offset:8192\n"
"ds_read_b64_tr_b16 v[162:163], v23 offset:10240\n"
"ds_read_b64_tr_b16 v[164:165], v23 offset:12288\n"
"ds_read_b64_tr_b16 v[166:167], v23 offset:14336\n"
"s_waitcnt lgkmcnt(0)\n"
"v_mfma_f32_16x16x32_bf16 v[248:251], v[160:163], v[96:99], v[248:251]\n"
"v_mfma_f32_16x16x32_bf16 v[248:251], v[164:167], v[100:103], v[248:251]\n"
"v_mfma_f32_16x16x32_bf16 v[252:255], v[160:163], v[104:107], v[252:255]\n"
"v_mfma_f32_16x16x32_bf16 v[252:255], v[164:167], v[108:111], v[252:255]\n"
"v_mfma_f32_16x16x32_bf16 v[112:115], v[120:123], v[96:99], v[112:115]\n"
"v_mfma_f32_16x16x32_bf16 v[112:115], v[120:123], v[100:103], v[112:115]\n"
"v_mfma_f32_16x16x32_bf16 v[116:119], v[120:123], v[104:107], v[116:119]\n"
"v_mfma_f32_16x16x32_bf16 v[116:119], v[120:123], v[108:111], v[116:119]\n"
"s_nop 7\n"
"s_nop 7\n"
"s_barrier\n"
"s_add_u32 s48, s48, 16384\n"
"s_add_u32 s47, s47, 1\n"
"s_cmp_lt_u32 s47, 8\n"
"s_cbranch_scc1 AT_slow_loop\n"
"s_branch AT_join\n"
"FZ_end:\n"
  :: "{v0}"(tid), "{s[4:5]}"(ka), "{s20}"(bid), "{s91}"((unsigned)(size_t)smem)
  : "v1", "v2", "v3", "v4", "v5", "v6", "v7", "v8", "v9", "v10", "v11", "v12", "v13", "v14", "v15", "v16", "v17", "v18", "v19", "v20", "v21", "v22", "v23", "v24", "v25", "v26", "v27", "v28", "v29", "v30", "v31", "v32", "v33", "v34", "v35", "v36", "v37", "v38", "v39", "v40", "v41", "v42", "v43", "v44", "v45", "v46", "v47", "v48", "v49", "v50", "v51", "v52", "v53", "v54", "v55", "v56", "v57", "v58", "v59", "v60", "v61", "v62", "v63", "v64", "v65", "v66", "v67", "v68", "v69", "v70", "v71", "v72", "v73", "v74", "v75", "v76", "v77", "v78", "v79", "v80", "v81", "v82", "v83", "v84", "v85", "v86", "v87", "v88", "v89", "v90", "v91", "v92", "v93", "v94", "v95", "v96", "v97", "v98", "v99", "v100", "v101", "v102", "v103", "v104", "v105", "v106", "v107", "v108", "v109", "v110", "v111", "v112", "v113", "v114", "v115", "v116", "v117", "v118", "v119", "v120", "v121", "v122", "v123", "v124", "v125", "v126", "v127", "v128", "v129", "v130", "v131", "v132", "v133", "v134", "v135", "v136", "v137", "v138", "v139", "v140", "v141", "v142", "v143", "v144", "v145", "v146", "v147", "v148", "v149", "v150", "v151", "v152", "v153", "v154", "v155", "v156", "v157", "v158", "v159", "v160", "v161", "v162", "v163", "v164", "v165", "v166", "v167", "v168", "v169", "v170", "v171", "v172", "v173", "v174", "v175", "v176", "v177", "v178", "v179", "v180", "v181", "v182", "v183", "v184", "v185", "v186", "v187", "v188", "v189", "v190", "v191", "v192", "v193", "v194", "v195", "v196", "v197", "v198", "v199", "v200", "v201", "v202", "v203", "v204", "v205", "v206", "v207", "v208", "v209", "v210", "v211", "v212", "v213", "v214", "v215", "v216", "v217", "v218", "v219", "v220", "v221", "v222", "v223", "v224", "v225", "v226", "v227", "v228", "v229", "v230", "v231", "v232", "v233", "v234", "v235", "v236", "v237", "v238", "v239", "v240", "v241", "v242", "v243", "v244", "v245", "v246", "v247", "v248", "v249", "v250", "v251", "v252", "v253", "v254", "v255", "s0", "s1", "s2", "s3", "s6", "s7", "s8", "s9", "s10", "s11", "s12", "s13", "s14", "s15", "s16", "s17", "s18", "s19", "s21", "s22", "s23", "s24", "s25", "s26", "s27", "s28", "s29", "s30", "s31", "s34", "s35", "s36", "s37", "s38", "s39", "s40", "s41", "s42", "s43", "s44", "s45", "s46", "s47", "s48", "s49", "s50", "s51", "s52", "s53", "s54", "s55", "s56", "s57", "s58", "s59", "s60", "s61", "s62", "s63", "s64", "s65", "s66", "s67", "s68", "s69", "s70", "s71", "s72", "s73", "s74", "s75", "s76", "s77", "s78", "s79", "s80", "s81", "s82", "s83", "s84", "s85", "s86", "s87", "s88", "s89", "s90", "s92", "s93", "s94", "s95", "s96", "s97", "s98", "s99", "vcc", "scc", "memory");
}

extern "C" void kernel_launch(void* const* d_in, const int* in_sizes, int n_in, void* d_out, int out_size, void* d_ws, size_t ws_size, hipStream_t stream) {
  char* ws = (char*)d_ws;
  void* xn = ws; void* hbuf = ws + (4u << 20); void* obuf = ws + (20u << 20); void* flags = ws + (40u << 20); void* part = ws + (64u << 20);
  k_ln<<<512, 256, 0, stream>>>((const u32x4*)d_in[0], (u32x4*)xn, (unsigned*)flags);
  k_main<<<256, 512, 0, stream>>>(xn, d_in[3], d_in[6], d_in[7], d_in[1], d_in[2], hbuf, obuf, d_in[4], d_in[8], d_in[9], d_in[0], d_out, flags, part);
}
```
